```python
import torch

def _softmax(x, axis=-1):
    return torch.softmax(x, dim=axis)
def _gelu(x):
    return torch.nn.functional.gelu(x, approximate="tanh")
def _silu(x):
    return torch.nn.functional.silu(x)
def _sigmoid(x):
    return torch.sigmoid(x)
def _layernorm(x, axis=-1, eps=1e-5):
    m = x.mean(dim=axis, keepdim=True)
    v = x.var(dim=axis, keepdim=True, unbiased=False)
    return (x - m) / torch.sqrt(v + eps)
def _scatter_add(tgt, idx, src, axis=0):
    return tgt.clone().index_add_(axis, idx, src)
def _segment_sum(src, seg, n):
    out = torch.zeros((n,) + src.shape[1:], dtype=src.dtype)
    return out.index_add_(0, seg, src)
def _pad(x, pw):
    flat = tuple(v for pair in reversed(pw) for v in pair)
    return torch.nn.functional.pad(x, flat)

kernel_name = 'sequential_preLN__softmax_full_mha__mixer_s_mlp__prompt'

def setup_inputs():

    _w = (lambda a: (lambda t: t.to(torch.bfloat16) if t.is_floating_point() else t)(torch.as_tensor(a)))
    def _A_setup():
        B, S, D = 1, 2048, 640
        return {
            "x": _w((lambda *s: torch.randn(*s, dtype=torch.bfloat16) * 0.05)(B, S, D)),
            "wqkv": _w((lambda *s: torch.randn(*s, dtype=torch.bfloat16) * 0.05)(D, 3 * D)),
            "wo": _w((lambda *s: torch.randn(*s, dtype=torch.bfloat16) * 0.05)(D, D)),
        }
    _A_in = _A_setup()

    def _B_setup():
        B, S, D, F = 1, 2048, 640, 1536
        return {
            "x":  _w((lambda *s: torch.randn(*s, dtype=torch.bfloat16) * 0.05)(B, S, D)),
            "w1": _w((lambda *s: torch.randn(*s, dtype=torch.bfloat16) * 3.2)(D, D) * (D ** -0.5)),
            "w2": _w((lambda *s: torch.randn(*s, dtype=torch.bfloat16) * 3.2)(D, D) * (D ** -0.5)),
            "w3": _w((lambda *s: torch.randn(*s, dtype=torch.bfloat16) * 3.2)(D, F) * (D ** -0.5)),
            "w4": _w((lambda *s: torch.randn(*s, dtype=torch.bfloat16) * 3.2)(F, D) * (F ** -0.5)),
        }
    _B_in = _B_setup()

    return {
        "A_x": _A_in["x"],
        "A_wqkv": _A_in["wqkv"],
        "A_wo": _A_in["wo"],
        "B_x": _B_in["x"],
        "B_w1": _B_in["w1"],
        "B_w2": _B_in["w2"],
        "B_w3": _B_in["w3"],
        "B_w4": _B_in["w4"],
    }

def reference(A_x, A_wqkv, A_wo, B_x, B_w1, B_w2, B_w3, B_w4):
    def _A_ref(x, wqkv, wo):
        B, S, D = x.shape
        H = 32; Dh = D // H
        qkv = (lambda x, *d: x.permute(*d))((x @ wqkv).reshape(B, S, 3, H, Dh), 2, 0, 3, 1, 4)
        q, k, v = qkv[0], qkv[1], qkv[2]
        scale = Dh ** -0.5
        scores = torch.einsum("bhqd,bhkd->bhqk", q, k) * scale
        p = _softmax(scores, axis=-1)
        o = torch.einsum("bhqk,bhkd->bhqd", p, v)
        return (lambda x, *d: x.permute(*d))(o, 0, 2, 1, 3).reshape(B, S, D) @ wo

    def _B_ref(x, w1, w2, w3, w4):
        B, S, D = x.shape
        h = _gelu(_layernorm(x, axis=-1) @ w1)
        c = D // 4
        g0 = _pad(h[:, :, 0*c:1*c], ((0, 0), (1, 0), (0, 0)))[:, :S, :]
        g1 = _pad(h[:, :, 1*c:2*c], ((0, 0), (0, 1), (0, 0)))[:, 1:, :]
        g2 = _pad(h[:, :, 2*c:3*c], ((0, 0), (2, 0), (0, 0)))[:, :S, :]
        g3 = _pad(h[:, :, 3*c:   ], ((0, 0), (0, 2), (0, 0)))[:, 2:, :]
        hs = torch.concatenate([g0, g1, g2, g3], axis=-1) @ w2
        x1 = x + hs
        h2 = _gelu(_layernorm(x1, axis=-1) @ w3) @ w4
        return x1 + h2

    x = A_x
    h = x + _A_ref(_layernorm(x, axis=-1), A_wqkv, A_wo)
    h2 = h + _B_ref(_layernorm(h, axis=-1), B_w1, B_w2, B_w3, B_w4)
    return h2
```

```cpp
#include <hip/hip_runtime.h>
#include <hip/hip_bf16.h>
typedef unsigned short u16;
constexpr int S = 2048, D = 640, H = 32, DH = 20, F = 1536;
__device__ __forceinline__ float b2f(u16 v) { return __uint_as_float((unsigned)v << 16); }
__device__ __forceinline__ u16 f2b(float f) { __bf16 b = (__bf16)f; return __builtin_bit_cast(u16, b); }
__global__ void k_ln(const u16* x, u16* y) {
  int r = blockIdx.x, l = threadIdx.x; float v[10]; float s = 0;
  for (int i = 0; i < 10; ++i) { v[i] = b2f(x[r * D + l + 64 * i]); s += v[i]; }
  for (int o = 32; o; o >>= 1) s += __shfl_xor(s, o);
  float m = s / D, q = 0;
  for (int i = 0; i < 10; ++i) { q += (v[i] - m) * (v[i] - m); }
  for (int o = 32; o; o >>= 1) q += __shfl_xor(q, o);
  float rs = rsqrtf(q / D + 1e-5f);
  for (int i = 0; i < 10; ++i) y[r * D + l + 64 * i] = f2b((v[i] - m) * rs);
}
__device__ __forceinline__ float gelu(float x) { float z = 0.7978845608f * (x + 0.044715f * x * x * x); return 0.5f * x * (1.f + tanhf(z)); }
template <int MODE, bool SHIFT>
__global__ void k_gemm(const u16* A, const u16* W, u16* C, int K, int N, const u16* r1, const u16* r2) {
  __shared__ float a[F];
  int r = blockIdx.x;
  for (int k = threadIdx.x; k < K; k += 256) {
    int rr = r; if (SHIFT) { int g = k / (D / 4); rr = r + (g == 0 ? -1 : g == 1 ? 1 : g == 2 ? -2 : 2); }
    a[k] = (rr >= 0 && rr < S) ? b2f(A[rr * K + k]) : 0.f;
  }
  __syncthreads();
  for (int n = threadIdx.x; n < N; n += 256) {
    float acc = 0;
    for (int k = 0; k < K; ++k) acc += a[k] * b2f(W[k * N + n]);
    if (MODE == 1) acc = gelu(b2f(f2b(acc)));
    if (MODE == 2) acc = b2f(f2b(acc)) + b2f(r1[r * N + n]);
    if (MODE == 3) acc = b2f(f2b(b2f(f2b(acc)) + b2f(r1[r * N + n]))) + b2f(r2[r * N + n]);
    C[r * N + n] = f2b(acc);
  }
}
__global__ void k_attn(const u16* qkv, u16* o) {
  int q = blockIdx.x * 64 + threadIdx.x, h = blockIdx.y;
  float qq[DH], acc[DH]; float m = -1e30f, l = 0;
  for (int d = 0; d < DH; ++d) { qq[d] = b2f(qkv[q * 3 * D + h * DH + d]) * 0.22360679775f; acc[d] = 0; }
  for (int k = 0; k < S; ++k) {
    float s = 0; for (int d = 0; d < DH; ++d) s += qq[d] * b2f(qkv[k * 3 * D + D + h * DH + d]);
    float mn = fmaxf(m, s), c = __expf(m - mn), p = __expf(s - mn); l = l * c + p; m = mn;
    for (int d = 0; d < DH; ++d) acc[d] = acc[d] * c + p * b2f(qkv[k * 3 * D + 2 * D + h * DH + d]);
  }
  for (int d = 0; d < DH; ++d) o[q * D + h * DH + d] = f2b(acc[d] / l);
}
extern "C" void kernel_launch(void* const* d_in, const int* in_sizes, int n_in, void* d_out, int out_size, void* d_ws, size_t ws_size, hipStream_t stream) {
  const u16 *x = (const u16*)d_in[0], *wqkv = (const u16*)d_in[1], *wo = (const u16*)d_in[2], *w1 = (const u16*)d_in[4], *w2 = (const u16*)d_in[5], *w3 = (const u16*)d_in[6], *w4 = (const u16*)d_in[7];
  u16* ws = (u16*)d_ws; size_t T = (size_t)S * D;
  u16 *xn = ws, *qkv = ws + T, *o = ws + 4 * T, *h = ws + 5 * T, *hn = ws + 6 * T, *hn2 = ws + 7 * T, *g = ws + 8 * T, *x1 = ws + 9 * T, *x1n = ws + 10 * T, *u = ws + 11 * T;
  k_ln<<<S, 64, 0, stream>>>(x, xn);
  k_gemm<0, false><<<S, 256, 0, stream>>>(xn, wqkv, qkv, D, 3 * D, nullptr, nullptr);
  k_attn<<<dim3(S / 64, H), 64, 0, stream>>>(qkv, o);
  k_gemm<2, false><<<S, 256, 0, stream>>>(o, wo, h, D, D, x, nullptr);
  k_ln<<<S, 64, 0, stream>>>(h, hn);
  k_ln<<<S, 64, 0, stream>>>(hn, hn2);
  k_gemm<1, false><<<S, 256, 0, stream>>>(hn2, w1, g, D, D, nullptr, nullptr);
  k_gemm<2, true><<<S, 256, 0, stream>>>(g, w2, x1, D, D, hn, nullptr);
  k_ln<<<S, 64, 0, stream>>>(x1, x1n);
  k_gemm<1, false><<<S, 256, 0, stream>>>(x1n, w3, u, D, F, nullptr, nullptr);
  k_gemm<3, false><<<S, 256, 0, stream>>>(u, w4, (u16*)d_out, F, D, x1, h);
}
```

```cpp
#include <hip/hip_runtime.h>

typedef unsigned short u16;
typedef unsigned long long u64;
typedef unsigned v4u __attribute__((ext_vector_type(4)));
typedef unsigned v2u __attribute__((ext_vector_type(2)));
typedef float f32x16 __attribute__((ext_vector_type(16)));
typedef float f32x4 __attribute__((ext_vector_type(4)));
typedef float f32x2 __attribute__((ext_vector_type(2)));
typedef __bf16 bf16x8 __attribute__((ext_vector_type(8)));
typedef __bf16 bf16x2 __attribute__((ext_vector_type(2)));
typedef short s16x4 __attribute__((ext_vector_type(4)));
#define LAS __attribute__((address_space(3)))
#define GAS __attribute__((address_space(1)))
#define CAS __attribute__((address_space(4)))
#define DEV __device__ __forceinline__
typedef __amdgpu_buffer_rsrc_t rsrc_t;

#define MK_STOP 99
#define MK_STAMP 0

#define ATT_ASM \
  "s_nop 4\n" \
  "buffer_load_dwordx4 %[q160], %[l16], %[rs], %[sq] offen offset:0 sc1\n" \
  "buffer_load_dwordx4 %[q161], %[l16], %[rs], %[sq] offen offset:1024 sc1\n" \
  "buffer_load_dwordx4 %[q162], %[l16], %[rs], %[sq] offen offset:2048 sc1\n" \
  "buffer_load_dwordx4 %[q163], %[l16], %[rs], %[sq] offen offset:3072 sc1\n" \
  "buffer_load_dwordx4 %[k16a], %[l16], %[rs], %[so] offen offset:0 sc1\n" \
  "buffer_load_dwordx2 %[k8a], %[r8], %[rs], %[so] offen offset:1024 sc1\n" \
  "buffer_load_dwordx2 %[q80], %[r8], %[rs], %[sq8] offen offset:0 sc1\n" \
  "buffer_load_dwordx2 %[q81], %[r8], %[rs], %[sq8] offen offset:256 sc1\n" \
  "buffer_load_dwordx2 %[q82], %[r8], %[rs], %[sq8] offen offset:512 sc1\n" \
  "buffer_load_dwordx2 %[q83], %[r8], %[rs], %[sq8] offen offset:768 sc1\n" \
  "buffer_load_dwordx4 %[v0a], %[vvo], %[rs], %[so] offen offset:1280 sc1\n" \
  "buffer_load_dwordx4 %[v1a], %[vvo], %[rs], %[so] offen offset:2048 sc1\n" \
  "buffer_load_dwordx4 %[k16b], %[l16], %[rs], %[so] offen offset:2816 sc1\n" \
  "buffer_load_dwordx2 %[k8b], %[r8], %[rs], %[so] offen offset:3840 sc1\n" \
  "s_add_u32 %[so], %[so], 2816\n" \
  "s_waitcnt vmcnt(4)\n" \
  "v_mfma_f32_32x32x16_bf16 v[192:207], %[k16a], %[q160], 0\n" \
  "v_mfma_f32_32x32x8_bf16 v[192:207], %[k8a], %[q80], v[192:207]\n" \
  "v_mfma_f32_32x32x16_bf16 v[208:223], %[k16a], %[q161], 0\n" \
  "v_mfma_f32_32x32x8_bf16 v[208:223], %[k8a], %[q81], v[208:223]\n" \
  "s_nop 7\n" \
  "s_nop 3\n" \
  "v_exp_f32 v192, v192\n" \
  "v_exp_f32 v193, v193\n" \
  "s_branch LATT_skip%=\n" \
  "LATT_loop%=:\n" \
  "v_exp_f32 v192, v192\n" \
  "v_exp_f32 v193, v193\n" \
  "v_smfmac_f32_32x32x32_bf16 %[o3], %[v1b], v[248:255], %[idx]\n" \
  "LATT_skip%=:\n" \
  "buffer_load_dwordx4 %[v0b], %[vvo], %[rs], %[so] offen offset:1280 sc1\n" \
  "buffer_load_dwordx4 %[v1b], %[vvo], %[rs], %[so] offen offset:2048 sc1\n" \
  "v_exp_f32 v194, v194\n" \
  "v_exp_f32 v195, v195\n" \
  "v_exp_f32 v196, v196\n" \
  "v_exp_f32 v197, v197\n" \
  "v_mfma_f32_32x32x16_bf16 v[224:239], %[k16a], %[q162], 0\n" \
  "v_exp_f32 v198, v198\n" \
  "v_exp_f32 v199, v199\n" \
  "v_exp_f32 v200, v200\n" \
  "v_exp_f32 v201, v201\n" \
  "s_waitcnt vmcnt(4)\n" \
  "v_smfmac_f32_32x32x32_bf16 %[o0], %[v0a], v[192:199], %[idx]\n" \
  "v_exp_f32 v202, v202\n" \
  "v_exp_f32 v203, v203\n" \
  "v_exp_f32 v204, v204\n" \
  "v_exp_f32 v205, v205\n" \
  "v_mfma_f32_32x32x8_bf16 v[224:239], %[k8a], %[q82], v[224:239]\n" \
  "v_exp_f32 v206, v206\n" \
  "v_exp_f32 v207, v207\n" \
  "v_exp_f32 v208, v208\n" \
  "v_exp_f32 v209, v209\n" \
  "v_smfmac_f32_32x32x32_bf16 %[o0], %[v1a], v[200:207], %[idx]\n" \
  "v_exp_f32 v210, v210\n" \
  "v_exp_f32 v211, v211\n" \
  "v_exp_f32 v212, v212\n" \
  "v_exp_f32 v213, v213\n" \
  "v_mfma_f32_32x32x16_bf16 v[240:255], %[k16a], %[q163], 0\n" \
  "v_exp_f32 v214, v214\n" \
  "v_exp_f32 v215, v215\n" \
  "v_exp_f32 v216, v216\n" \
  "v_exp_f32 v217, v217\n" \
  "v_smfmac_f32_32x32x32_bf16 %[o1], %[v0a], v[208:215], %[idx]\n" \
  "v_exp_f32 v218, v218\n" \
  "v_exp_f32 v219, v219\n" \
  "v_exp_f32 v220, v220\n" \
  "v_exp_f32 v221, v221\n" \
  "v_mfma_f32_32x32x8_bf16 v[240:255], %[k8a], %[q83], v[240:255]\n" \
  "v_exp_f32 v222, v222\n" \
  "v_exp_f32 v223, v223\n" \
  "v_exp_f32 v224, v224\n" \
  "v_exp_f32 v225, v225\n" \
  "v_smfmac_f32_32x32x32_bf16 %[o1], %[v1a], v[216:223], %[idx]\n" \
  "v_exp_f32 v226, v226\n" \
  "v_exp_f32 v227, v227\n" \
  "v_exp_f32 v228, v228\n" \
  "v_exp_f32 v229, v229\n" \
  "s_waitcnt vmcnt(2)\n" \
  "v_mfma_f32_32x32x16_bf16 v[192:207], %[k16b], %[q160], 0\n" \
  "buffer_load_dwordx4 %[k16a], %[l16], %[rs], %[so] offen offset:2816 sc1\n" \
  "buffer_load_dwordx2 %[k8a], %[r8], %[rs], %[so] offen offset:3840 sc1\n" \
  "s_add_u32 %[so], %[so], 2816\n" \
  "v_exp_f32 v230, v230\n" \
  "v_exp_f32 v231, v231\n" \
  "v_exp_f32 v232, v232\n" \
  "v_exp_f32 v233, v233\n" \
  "v_smfmac_f32_32x32x32_bf16 %[o2], %[v0a], v[224:231], %[idx]\n" \
  "v_exp_f32 v234, v234\n" \
  "v_exp_f32 v235, v235\n" \
  "v_exp_f32 v236, v236\n" \
  "v_exp_f32 v237, v237\n" \
  "v_mfma_f32_32x32x8_bf16 v[192:207], %[k8b], %[q80], v[192:207]\n" \
  "v_exp_f32 v238, v238\n" \
  "v_exp_f32 v239, v239\n" \
  "v_exp_f32 v240, v240\n" \
  "v_exp_f32 v241, v241\n" \
  "v_smfmac_f32_32x32x32_bf16 %[o2], %[v1a], v[232:239], %[idx]\n" \
  "v_exp_f32 v242, v242\n" \
  "v_exp_f32 v243, v243\n" \
  "v_exp_f32 v244, v244\n" \
  "v_exp_f32 v245, v245\n" \
  "v_mfma_f32_32x32x16_bf16 v[208:223], %[k16b], %[q161], 0\n" \
  "v_exp_f32 v246, v246\n" \
  "v_exp_f32 v247, v247\n" \
  "v_exp_f32 v248, v248\n" \
  "v_exp_f32 v249, v249\n" \
  "v_smfmac_f32_32x32x32_bf16 %[o3], %[v0a], v[240:247], %[idx]\n" \
  "v_exp_f32 v250, v250\n" \
  "v_exp_f32 v251, v251\n" \
  "v_exp_f32 v252, v252\n" \
  "v_exp_f32 v253, v253\n" \
  "v_mfma_f32_32x32x8_bf16 v[208:223], %[k8b], %[q81], v[208:223]\n" \
  "v_exp_f32 v254, v254\n" \
  "v_exp_f32 v255, v255\n" \
  "v_exp_f32 v192, v192\n" \
  "v_exp_f32 v193, v193\n" \
  "v_smfmac_f32_32x32x32_bf16 %[o3], %[v1a], v[248:255], %[idx]\n" \
  "buffer_load_dwordx4 %[v0a], %[vvo], %[rs], %[so] offen offset:1280 sc1\n" \
  "buffer_load_dwordx4 %[v1a], %[vvo], %[rs], %[so] offen offset:2048 sc1\n" \
  "v_exp_f32 v194, v194\n" \
  "v_exp_f32 v195, v195\n" \
  "v_exp_f32 v196, v196\n" \
  "v_exp_f32 v197, v197\n" \
  "v_mfma_f32_32x32x16_bf16 v[224:239], %[k16b], %[q162], 0\n" \
  "v_exp_f32 v198, v198\n" \
  "v_exp_f32 v199, v199\n" \
  "v_exp_f32 v200, v200\n" \
  "v_exp_f32 v201, v201\n" \
  "s_waitcnt vmcnt(4)\n" \
  "v_smfmac_f32_32x32x32_bf16 %[o0], %[v0b], v[192:199], %[idx]\n" \
  "v_exp_f32 v202, v202\n" \
  "v_exp_f32 v203, v203\n" \
  "v_exp_f32 v204, v204\n" \
  "v_exp_f32 v205, v205\n" \
  "v_mfma_f32_32x32x8_bf16 v[224:239], %[k8b], %[q82], v[224:239]\n" \
  "v_exp_f32 v206, v206\n" \
  "v_exp_f32 v207, v207\n" \
  "v_exp_f32 v208, v208\n" \
  "v_exp_f32 v209, v209\n" \
  "v_smfmac_f32_32x32x32_bf16 %[o0], %[v1b], v[200:207], %[idx]\n" \
  "v_exp_f32 v210, v210\n" \
  "v_exp_f32 v211, v211\n" \
  "v_exp_f32 v212, v212\n" \
  "v_exp_f32 v213, v213\n" \
  "v_mfma_f32_32x32x16_bf16 v[240:255], %[k16b], %[q163], 0\n" \
  "v_exp_f32 v214, v214\n" \
  "v_exp_f32 v215, v215\n" \
  "v_exp_f32 v216, v216\n" \
  "v_exp_f32 v217, v217\n" \
  "v_smfmac_f32_32x32x32_bf16 %[o1], %[v0b], v[208:215], %[idx]\n" \
  "v_exp_f32 v218, v218\n" \
  "v_exp_f32 v219, v219\n" \
  "v_exp_f32 v220, v220\n" \
  "v_exp_f32 v221, v221\n" \
  "v_mfma_f32_32x32x8_bf16 v[240:255], %[k8b], %[q83], v[240:255]\n" \
  "v_exp_f32 v222, v222\n" \
  "v_exp_f32 v223, v223\n" \
  "v_exp_f32 v224, v224\n" \
  "v_exp_f32 v225, v225\n" \
  "v_smfmac_f32_32x32x32_bf16 %[o1], %[v1b], v[216:223], %[idx]\n" \
  "v_exp_f32 v226, v226\n" \
  "v_exp_f32 v227, v227\n" \
  "v_exp_f32 v228, v228\n" \
  "v_exp_f32 v229, v229\n" \
  "s_waitcnt vmcnt(2)\n" \
  "v_mfma_f32_32x32x16_bf16 v[192:207], %[k16a], %[q160], 0\n" \
  "buffer_load_dwordx4 %[k16b], %[l16], %[rs], %[so] offen offset:2816 sc1\n" \
  "buffer_load_dwordx2 %[k8b], %[r8], %[rs], %[so] offen offset:3840 sc1\n" \
  "s_add_u32 %[so], %[so], 2816\n" \
  "v_exp_f32 v230, v230\n" \
  "v_exp_f32 v231, v231\n" \
  "v_exp_f32 v232, v232\n" \
  "v_exp_f32 v233, v233\n" \
  "v_smfmac_f32_32x32x32_bf16 %[o2], %[v0b], v[224:231], %[idx]\n" \
  "v_exp_f32 v234, v234\n" \
  "v_exp_f32 v235, v235\n" \
  "v_exp_f32 v236, v236\n" \
  "v_exp_f32 v237, v237\n" \
  "v_mfma_f32_32x32x8_bf16 v[192:207], %[k8a], %[q80], v[192:207]\n" \
  "v_exp_f32 v238, v238\n" \
  "v_exp_f32 v239, v239\n" \
  "v_exp_f32 v240, v240\n" \
  "v_exp_f32 v241, v241\n" \
  "v_smfmac_f32_32x32x32_bf16 %[o2], %[v1b], v[232:239], %[idx]\n" \
  "v_exp_f32 v242, v242\n" \
  "v_exp_f32 v243, v243\n" \
  "v_exp_f32 v244, v244\n" \
  "v_exp_f32 v245, v245\n" \
  "v_mfma_f32_32x32x16_bf16 v[208:223], %[k16a], %[q161], 0\n" \
  "v_exp_f32 v246, v246\n" \
  "v_exp_f32 v247, v247\n" \
  "v_exp_f32 v248, v248\n" \
  "v_exp_f32 v249, v249\n" \
  "v_smfmac_f32_32x32x32_bf16 %[o3], %[v0b], v[240:247], %[idx]\n" \
  "v_exp_f32 v250, v250\n" \
  "v_exp_f32 v251, v251\n" \
  "v_exp_f32 v252, v252\n" \
  "v_exp_f32 v253, v253\n" \
  "v_mfma_f32_32x32x8_bf16 v[208:223], %[k8a], %[q81], v[208:223]\n" \
  "s_add_i32 %[cnt], %[cnt], -1\n" \
  "s_cmp_lg_u32 %[cnt], 0\n" \
  "v_exp_f32 v254, v254\n" \
  "v_exp_f32 v255, v255\n" \
  "s_cbranch_scc1 LATT_loop%=\n" \
  "s_nop 1\n" \
  "v_smfmac_f32_32x32x32_bf16 %[o3], %[v1b], v[248:255], %[idx]\n" \
  "s_waitcnt vmcnt(0)\n" \
  "s_nop 7\n" \
  "s_nop 3\n" \
  ""
#define ATT_CLOB "v192", "v193", "v194", "v195", "v196", "v197", "v198", "v199", "v200", "v201", "v202", "v203", "v204", "v205", "v206", "v207", "v208", "v209", "v210", "v211", "v212", "v213", "v214", "v215", "v216", "v217", "v218", "v219", "v220", "v221", "v222", "v223", "v224", "v225", "v226", "v227", "v228", "v229", "v230", "v231", "v232", "v233", "v234", "v235", "v236", "v237", "v238", "v239", "v240", "v241", "v242", "v243", "v244", "v245", "v246", "v247", "v248", "v249", "v250", "v251", "v252", "v253", "v254", "v255"
#define P3_ASM \
  "s_nop 4\n" \
  "buffer_load_dwordx4 v[64:67], %[va0], %[rs], %[sa] offen offset:0 sc1\n" \
  "buffer_load_dwordx4 v[68:71], %[va0], %[rs], %[sa] offen offset:1024 sc1\n" \
  "buffer_load_dwordx4 v[72:75], %[va0], %[rs], %[sa] offen offset:2048 sc1\n" \
  "buffer_load_dwordx4 v[76:79], %[va0], %[rs], %[sa] offen offset:3072 sc1\n" \
  "s_add_u32 %[sa], %[sa], 4096\n" \
  "buffer_load_dwordx4 v[80:83], %[va0], %[rs], %[sa] offen offset:0 sc1\n" \
  "buffer_load_dwordx4 v[84:87], %[va0], %[rs], %[sa] offen offset:1024 sc1\n" \
  "buffer_load_dwordx4 v[88:91], %[va0], %[rs], %[sa] offen offset:2048 sc1\n" \
  "buffer_load_dwordx4 v[92:95], %[va0], %[rs], %[sa] offen offset:3072 sc1\n" \
  "s_add_u32 %[sa], %[sa], 4096\n" \
  "buffer_load_dwordx4 v[96:99], %[va0], %[rs], %[sa] offen offset:0 sc1\n" \
  "buffer_load_dwordx4 v[100:103], %[va0], %[rs], %[sa] offen offset:1024 sc1\n" \
  "ds_read_b128 v[104:107], %[vw] offset:0\n" \
  "ds_read_b128 v[108:111], %[vw] offset:1024\n" \
  "ds_read_b128 v[112:115], %[vw] offset:2048\n" \
  "ds_read_b128 v[116:119], %[vw] offset:3072\n" \
  "ds_read_b128 v[120:123], %[vw] offset:4096\n" \
  "ds_read_b128 v[124:127], %[vw] offset:10240\n" \
  "ds_read_b128 v[128:131], %[vw] offset:11264\n" \
  "ds_read_b128 v[132:135], %[vw] offset:12288\n" \
  "s_waitcnt vmcnt(9)\n" \
  "ds_read_b128 v[136:139], %[vw] offset:13312\n" \
  "s_waitcnt lgkmcnt(8)\n" \
  "v_mfma_f32_16x16x32_bf16 %[c0], v[104:107], v[64:67], 0\n" \
  "ds_read_b128 v[140:143], %[vw] offset:14336\n" \
  "s_waitcnt lgkmcnt(8)\n" \
  "v_mfma_f32_16x16x32_bf16 %[c1], v[108:111], v[64:67], 0\n" \
  "ds_read_b128 v[104:107], %[vw] offset:20480\n" \
  "s_waitcnt lgkmcnt(8)\n" \
  "v_mfma_f32_16x16x32_bf16 %[c2], v[112:115], v[64:67], 0\n" \
  "ds_read_b128 v[108:111], %[vw] offset:21504\n" \
  "s_waitcnt lgkmcnt(8)\n" \
  "v_mfma_f32_16x16x32_bf16 %[c3], v[116:119], v[64:67], 0\n" \
  "ds_read_b128 v[112:115], %[vw] offset:22528\n" \
  "s_waitcnt lgkmcnt(8)\n" \
  "v_mfma_f32_16x16x32_bf16 %[c4], v[120:123], v[64:67], 0\n" \
  "s_waitcnt vmcnt(8)\n" \
  "ds_read_b128 v[116:119], %[vw] offset:23552\n" \
  "s_waitcnt lgkmcnt(8)\n" \
  "v_mfma_f32_16x16x32_bf16 %[c0], v[124:127], v[68:71], %[c0]\n" \
  "ds_read_b128 v[120:123], %[vw] offset:24576\n" \
  "s_waitcnt lgkmcnt(8)\n" \
  "v_mfma_f32_16x16x32_bf16 %[c1], v[128:131], v[68:71], %[c1]\n" \
  "ds_read_b128 v[124:127], %[vw] offset:30720\n" \
  "s_waitcnt lgkmcnt(8)\n" \
  "v_mfma_f32_16x16x32_bf16 %[c2], v[132:135], v[68:71], %[c2]\n" \
  "ds_read_b128 v[128:131], %[vw] offset:31744\n" \
  "s_waitcnt lgkmcnt(8)\n" \
  "v_mfma_f32_16x16x32_bf16 %[c3], v[136:139], v[68:71], %[c3]\n" \
  "ds_read_b128 v[132:135], %[vw] offset:32768\n" \
  "s_waitcnt lgkmcnt(8)\n" \
  "v_mfma_f32_16x16x32_bf16 %[c4], v[140:143], v[68:71], %[c4]\n" \
  "s_waitcnt vmcnt(7)\n" \
  "ds_read_b128 v[136:139], %[vw] offset:33792\n" \
  "s_waitcnt lgkmcnt(8)\n" \
  "v_mfma_f32_16x16x32_bf16 %[c0], v[104:107], v[72:75], %[c0]\n" \
  "ds_read_b128 v[140:143], %[vw] offset:34816\n" \
  "s_waitcnt lgkmcnt(8)\n" \
  "v_mfma_f32_16x16x32_bf16 %[c1], v[108:111], v[72:75], %[c1]\n" \
  "ds_read_b128 v[104:107], %[vw] offset:40960\n" \
  "s_waitcnt lgkmcnt(8)\n" \
  "v_mfma_f32_16x16x32_bf16 %[c2], v[112:115], v[72:75], %[c2]\n" \
  "ds_read_b128 v[108:111], %[vw] offset:41984\n" \
  "s_waitcnt lgkmcnt(8)\n" \
  "v_mfma_f32_16x16x32_bf16 %[c3], v[116:119], v[72:75], %[c3]\n" \
  "ds_read_b128 v[112:115], %[vw] offset:43008\n" \
  "s_waitcnt lgkmcnt(8)\n" \
  "v_mfma_f32_16x16x32_bf16 %[c4], v[120:123], v[72:75], %[c4]\n" \
  "s_waitcnt vmcnt(6)\n" \
  "ds_read_b128 v[116:119], %[vw] offset:44032\n" \
  "s_waitcnt lgkmcnt(8)\n" \
  "v_mfma_f32_16x16x32_bf16 %[c0], v[124:127], v[76:79], %[c0]\n" \
  "ds_read_b128 v[120:123], %[vw] offset:45056\n" \
  "s_waitcnt lgkmcnt(8)\n" \
  "v_mfma_f32_16x16x32_bf16 %[c1], v[128:131], v[76:79], %[c1]\n" \
  "ds_read_b128 v[124:127], %[vw] offset:51200\n" \
  "s_waitcnt lgkmcnt(8)\n" \
  "v_mfma_f32_16x16x32_bf16 %[c2], v[132:135], v[76:79], %[c2]\n" \
  "ds_read_b128 v[128:131], %[vw] offset:52224\n" \
  "s_waitcnt lgkmcnt(8)\n" \
  "v_mfma_f32_16x16x32_bf16 %[c3], v[136:139], v[76:79], %[c3]\n" \
  "ds_read_b128 v[132:135], %[vw] offset:53248\n" \
  "s_waitcnt lgkmcnt(8)\n" \
  "v_mfma_f32_16x16x32_bf16 %[c4], v[140:143], v[76:79], %[c4]\n" \
  "s_barrier\n" \
  "s_mov_b32 m0, %[dm0]\n" \
  "s_nop 0\n" \
  "buffer_load_dwordx4 %[l16], %[rs], %[ds0] offen offset:0 sc1 lds\n" \
  "buffer_load_dwordx4 %[l16], %[rs], %[ds0] offen offset:1024 sc1 lds\n" \
  "s_waitcnt vmcnt(7)\n" \
  "ds_read_b128 v[136:139], %[vw] offset:54272\n" \
  "s_waitcnt lgkmcnt(8)\n" \
  "v_mfma_f32_16x16x32_bf16 %[c0], v[104:107], v[80:83], %[c0]\n" \
  "ds_read_b128 v[140:143], %[vw] offset:55296\n" \
  "s_waitcnt lgkmcnt(8)\n" \
  "v_mfma_f32_16x16x32_bf16 %[c1], v[108:111], v[80:83], %[c1]\n" \
  "ds_read_b128 v[104:107], %[vw] offset:61440\n" \
  "s_waitcnt lgkmcnt(8)\n" \
  "v_mfma_f32_16x16x32_bf16 %[c2], v[112:115], v[80:83], %[c2]\n" \
  "ds_read_b128 v[108:111], %[vw] offset:62464\n" \
  "s_waitcnt lgkmcnt(8)\n" \
  "v_mfma_f32_16x16x32_bf16 %[c3], v[116:119], v[80:83], %[c3]\n" \
  "ds_read_b128 v[112:115], %[vw] offset:63488\n" \
  "s_waitcnt lgkmcnt(8)\n" \
  "v_mfma_f32_16x16x32_bf16 %[c4], v[120:123], v[80:83], %[c4]\n" \
  "s_waitcnt vmcnt(6)\n" \
  "ds_read_b128 v[116:119], %[vw] offset:64512\n" \
  "s_waitcnt lgkmcnt(8)\n" \
  "v_mfma_f32_16x16x32_bf16 %[c0], v[124:127], v[84:87], %[c0]\n" \
  "v_add_u32 %[vw], 65536, %[vw]\n" \
  "ds_read_b128 v[120:123], %[vw] offset:0\n" \
  "s_waitcnt lgkmcnt(8)\n" \
  "v_mfma_f32_16x16x32_bf16 %[c1], v[128:131], v[84:87], %[c1]\n" \
  "ds_read_b128 v[124:127], %[vw] offset:6144\n" \
  "s_waitcnt lgkmcnt(8)\n" \
  "v_mfma_f32_16x16x32_bf16 %[c2], v[132:135], v[84:87], %[c2]\n" \
  "ds_read_b128 v[128:131], %[vw] offset:7168\n" \
  "s_waitcnt lgkmcnt(8)\n" \
  "v_mfma_f32_16x16x32_bf16 %[c3], v[136:139], v[84:87], %[c3]\n" \
  "ds_read_b128 v[132:135], %[vw] offset:8192\n" \
  "s_waitcnt lgkmcnt(8)\n" \
  "v_mfma_f32_16x16x32_bf16 %[c4], v[140:143], v[84:87], %[c4]\n" \
  "s_waitcnt vmcnt(5)\n" \
  "ds_read_b128 v[136:139], %[vw] offset:9216\n" \
  "s_waitcnt lgkmcnt(8)\n" \
  "v_mfma_f32_16x16x32_bf16 %[c0], v[104:107], v[88:91], %[c0]\n" \
  "ds_read_b128 v[140:143], %[vw] offset:10240\n" \
  "s_waitcnt lgkmcnt(8)\n" \
  "v_mfma_f32_16x16x32_bf16 %[c1], v[108:111], v[88:91], %[c1]\n" \
  "ds_read_b128 v[104:107], %[vw] offset:16384\n" \
  "s_waitcnt lgkmcnt(8)\n" \
  "v_mfma_f32_16x16x32_bf16 %[c2], v[112:115], v[88:91], %[c2]\n" \
  "ds_read_b128 v[108:111], %[vw] offset:17408\n" \
  "s_waitcnt lgkmcnt(8)\n" \
  "v_mfma_f32_16x16x32_bf16 %[c3], v[116:119], v[88:91], %[c3]\n" \
  "ds_read_b128 v[112:115], %[vw] offset:18432\n" \
  "s_waitcnt lgkmcnt(8)\n" \
  "v_mfma_f32_16x16x32_bf16 %[c4], v[120:123], v[88:91], %[c4]\n" \
  "s_waitcnt vmcnt(4)\n" \
  "ds_read_b128 v[116:119], %[vw] offset:19456\n" \
  "s_waitcnt lgkmcnt(8)\n" \
  "v_mfma_f32_16x16x32_bf16 %[c0], v[124:127], v[92:95], %[c0]\n" \
  "ds_read_b128 v[120:123], %[vw] offset:20480\n" \
  "s_waitcnt lgkmcnt(8)\n" \
  "v_mfma_f32_16x16x32_bf16 %[c1], v[128:131], v[92:95], %[c1]\n" \
  "ds_read_b128 v[124:127], %[vw] offset:26624\n" \
  "s_waitcnt lgkmcnt(8)\n" \
  "v_mfma_f32_16x16x32_bf16 %[c2], v[132:135], v[92:95], %[c2]\n" \
  "ds_read_b128 v[128:131], %[vw] offset:27648\n" \
  "s_waitcnt lgkmcnt(8)\n" \
  "v_mfma_f32_16x16x32_bf16 %[c3], v[136:139], v[92:95], %[c3]\n" \
  "ds_read_b128 v[132:135], %[vw] offset:28672\n" \
  "s_waitcnt lgkmcnt(8)\n" \
  "v_mfma_f32_16x16x32_bf16 %[c4], v[140:143], v[92:95], %[c4]\n" \
  "s_waitcnt vmcnt(3)\n" \
  "ds_read_b128 v[136:139], %[vw] offset:29696\n" \
  "s_waitcnt lgkmcnt(8)\n" \
  "v_mfma_f32_16x16x32_bf16 %[c0], v[104:107], v[96:99], %[c0]\n" \
  "ds_read_b128 v[140:143], %[vw] offset:30720\n" \
  "s_waitcnt lgkmcnt(8)\n" \
  "v_mfma_f32_16x16x32_bf16 %[c1], v[108:111], v[96:99], %[c1]\n" \
  "s_waitcnt lgkmcnt(7)\n" \
  "v_mfma_f32_16x16x32_bf16 %[c2], v[112:115], v[96:99], %[c2]\n" \
  "s_waitcnt lgkmcnt(6)\n" \
  "v_mfma_f32_16x16x32_bf16 %[c3], v[116:119], v[96:99], %[c3]\n" \
  "s_waitcnt lgkmcnt(5)\n" \
  "v_mfma_f32_16x16x32_bf16 %[c4], v[120:123], v[96:99], %[c4]\n" \
  "s_waitcnt vmcnt(2)\n" \
  "s_waitcnt lgkmcnt(4)\n" \
  "v_mfma_f32_16x16x32_bf16 %[c0], v[124:127], v[100:103], %[c0]\n" \
  "s_waitcnt lgkmcnt(3)\n" \
  "v_mfma_f32_16x16x32_bf16 %[c1], v[128:131], v[100:103], %[c1]\n" \
  "s_waitcnt lgkmcnt(2)\n" \
  "v_mfma_f32_16x16x32_bf16 %[c2], v[132:135], v[100:103], %[c2]\n" \
  "s_waitcnt lgkmcnt(1)\n" \
  "v_mfma_f32_16x16x32_bf16 %[c3], v[136:139], v[100:103], %[c3]\n" \
  "s_waitcnt lgkmcnt(0)\n" \
  "v_mfma_f32_16x16x32_bf16 %[c4], v[140:143], v[100:103], %[c4]\n" \
  "s_nop 7\n" \
  ""
#define P4_ASM \
  "s_nop 4\n" \
  "buffer_load_dwordx4 v[64:67], %[va0], %[rs], %[sa] offen offset:0 sc1\n" \
  "buffer_load_dwordx4 v[68:71], %[va0], %[rs], %[sa] offen offset:1024 sc1\n" \
  "buffer_load_dwordx4 v[72:75], %[va0], %[rs], %[sa] offen offset:2048 sc1\n" \
  "buffer_load_dwordx4 v[76:79], %[va0], %[rs], %[sa] offen offset:3072 sc1\n" \
  "s_add_u32 %[sa], %[sa], 4096\n" \
  "buffer_load_dwordx4 v[80:83], %[va0], %[rs], %[sa] offen offset:0 sc1\n" \
  "buffer_load_dwordx4 v[84:87], %[va0], %[rs], %[sa] offen offset:1024 sc1\n" \
  "buffer_load_dwordx4 v[88:91], %[va0], %[rs], %[sa] offen offset:2048 sc1\n" \
  "buffer_load_dwordx4 v[92:95], %[va0], %[rs], %[sa] offen offset:3072 sc1\n" \
  "s_add_u32 %[sa], %[sa], 4096\n" \
  "buffer_load_dwordx4 v[96:99], %[va0], %[rs], %[sa] offen offset:0 sc1\n" \
  "buffer_load_dwordx4 v[100:103], %[va0], %[rs], %[sa] offen offset:1024 sc1\n" \
  "buffer_load_dwordx2 %[st0], %[vst], %[rs], 0 offen offset:0 sc1\n" \
  "buffer_load_dwordx2 %[st1], %[vst], %[rs], 0 offen offset:128 sc1\n" \
  "buffer_load_dwordx2 %[st2], %[vst], %[rs], 0 offen offset:256 sc1\n" \
  "buffer_load_dwordx2 %[st3], %[vst], %[rs], 0 offen offset:384 sc1\n" \
  "v_add_u32 %[vw], 98304, %[vw]\n" \
  "ds_read_b128 v[104:107], %[vw] offset:4096\n" \
  "ds_read_b128 v[108:111], %[vw] offset:5120\n" \
  "ds_read_b128 v[112:115], %[vw] offset:6144\n" \
  "ds_read_b128 v[116:119], %[vw] offset:7168\n" \
  "ds_read_b128 v[120:123], %[vw] offset:8192\n" \
  "ds_read_b128 v[124:127], %[vw] offset:14336\n" \
  "ds_read_b128 v[128:131], %[vw] offset:15360\n" \
  "ds_read_b128 v[132:135], %[vw] offset:16384\n" \
  "s_waitcnt vmcnt(13)\n" \
  "ds_read_b128 v[136:139], %[vw] offset:17408\n" \
  "s_waitcnt lgkmcnt(8)\n" \
  "v_mfma_f32_16x16x32_bf16 %[c0], v[104:107], v[64:67], 0\n" \
  "ds_read_b128 v[140:143], %[vw] offset:18432\n" \
  "s_waitcnt lgkmcnt(8)\n" \
  "v_mfma_f32_16x16x32_bf16 %[c1], v[108:111], v[64:67], 0\n" \
  "ds_read_b128 v[104:107], %[vw] offset:24576\n" \
  "s_waitcnt lgkmcnt(8)\n" \
  "v_mfma_f32_16x16x32_bf16 %[c2], v[112:115], v[64:67], 0\n" \
  "ds_read_b128 v[108:111], %[vw] offset:25600\n" \
  "s_waitcnt lgkmcnt(8)\n" \
  "v_mfma_f32_16x16x32_bf16 %[c3], v[116:119], v[64:67], 0\n" \
  "ds_read_b128 v[112:115], %[vw] offset:26624\n" \
  "s_waitcnt lgkmcnt(8)\n" \
  "v_mfma_f32_16x16x32_bf16 %[c4], v[120:123], v[64:67], 0\n" \
  "s_waitcnt vmcnt(12)\n" \
  "ds_read_b128 v[116:119], %[vw] offset:27648\n" \
  "s_waitcnt lgkmcnt(8)\n" \
  "v_mfma_f32_16x16x32_bf16 %[c0], v[124:127], v[68:71], %[c0]\n" \
  "ds_read_b128 v[120:123], %[vw] offset:28672\n" \
  "s_waitcnt lgkmcnt(8)\n" \
  "v_mfma_f32_16x16x32_bf16 %[c1], v[128:131], v[68:71], %[c1]\n" \
  "ds_read_b128 v[124:127], %[vw] offset:34816\n" \
  "s_waitcnt lgkmcnt(8)\n" \
  "v_mfma_f32_16x16x32_bf16 %[c2], v[132:135], v[68:71], %[c2]\n" \
  "ds_read_b128 v[128:131], %[vw] offset:35840\n" \
  "s_waitcnt lgkmcnt(8)\n" \
  "v_mfma_f32_16x16x32_bf16 %[c3], v[136:139], v[68:71], %[c3]\n" \
  "ds_read_b128 v[132:135], %[vw] offset:36864\n" \
  "s_waitcnt lgkmcnt(8)\n" \
  "v_mfma_f32_16x16x32_bf16 %[c4], v[140:143], v[68:71], %[c4]\n" \
  "s_waitcnt vmcnt(11)\n" \
  "ds_read_b128 v[136:139], %[vw] offset:37888\n" \
  "s_waitcnt lgkmcnt(8)\n" \
  "v_mfma_f32_16x16x32_bf16 %[c0], v[104:107], v[72:75], %[c0]\n" \
  "ds_read_b128 v[140:143], %[vw] offset:38912\n" \
  "s_waitcnt lgkmcnt(8)\n" \
  "v_mfma_f32_16x16x32_bf16 %[c1], v[108:111], v[72:75], %[c1]\n" \
  "ds_read_b128 v[104:107], %[vw] offset:45056\n" \
  "s_waitcnt lgkmcnt(8)\n" \
  "v_mfma_f32_16x16x32_bf16 %[c2], v[112:115], v[72:75], %[c2]\n" \
  "ds_read_b128 v[108:111], %[vw] offset:46080\n" \
  "s_waitcnt lgkmcnt(8)\n" \
  "v_mfma_f32_16x16x32_bf16 %[c3], v[116:119], v[72:75], %[c3]\n" \
  "ds_read_b128 v[112:115], %[vw] offset:47104\n" \
  "s_waitcnt lgkmcnt(8)\n" \
  "v_mfma_f32_16x16x32_bf16 %[c4], v[120:123], v[72:75], %[c4]\n" \
  "s_waitcnt vmcnt(10)\n" \
  "ds_read_b128 v[116:119], %[vw] offset:48128\n" \
  "s_waitcnt lgkmcnt(8)\n" \
  "v_mfma_f32_16x16x32_bf16 %[c0], v[124:127], v[76:79], %[c0]\n" \
  "ds_read_b128 v[120:123], %[vw] offset:49152\n" \
  "s_waitcnt lgkmcnt(8)\n" \
  "v_mfma_f32_16x16x32_bf16 %[c1], v[128:131], v[76:79], %[c1]\n" \
  "v_add_u32 %[vw], 4294868992, %[vw]\n" \
  "ds_read_b128 v[124:127], %[vw] offset:0\n" \
  "s_waitcnt lgkmcnt(8)\n" \
  "v_mfma_f32_16x16x32_bf16 %[c2], v[132:135], v[76:79], %[c2]\n" \
  "ds_read_b128 v[128:131], %[vw] offset:1024\n" \
  "s_waitcnt lgkmcnt(8)\n" \
  "v_mfma_f32_16x16x32_bf16 %[c3], v[136:139], v[76:79], %[c3]\n" \
  "ds_read_b128 v[132:135], %[vw] offset:2048\n" \
  "s_waitcnt lgkmcnt(8)\n" \
  "v_mfma_f32_16x16x32_bf16 %[c4], v[140:143], v[76:79], %[c4]\n" \
  "s_barrier\n" \
  "s_mov_b32 m0, %[dm0]\n" \
  "s_nop 0\n" \
  "buffer_load_dwordx4 %[l16], %[rs], %[ds0] offen offset:0 sc1 lds\n" \
  "buffer_load_dwordx4 %[l16], %[rs], %[ds0] offen offset:1024 sc1 lds\n" \
  "s_waitcnt vmcnt(11)\n" \
  "ds_read_b128 v[136:139], %[vw] offset:3072\n" \
  "s_waitcnt lgkmcnt(8)\n" \
  "v_mfma_f32_16x16x32_bf16 %[c0], v[104:107], v[80:83], %[c0]\n" \
  "ds_read_b128 v[140:143], %[vw] offset:4096\n" \
  "s_waitcnt lgkmcnt(8)\n" \
  "v_mfma_f32_16x16x32_bf16 %[c1], v[108:111], v[80:83], %[c1]\n" \
  "ds_read_b128 v[104:107], %[vw] offset:10240\n" \
  "s_waitcnt lgkmcnt(8)\n" \
  "v_mfma_f32_16x16x32_bf16 %[c2], v[112:115], v[80:83], %[c2]\n" \
  "ds_read_b128 v[108:111], %[vw] offset:11264\n" \
  "s_waitcnt lgkmcnt(8)\n" \
  "v_mfma_f32_16x16x32_bf16 %[c3], v[116:119], v[80:83], %[c3]\n" \
  "ds_read_b128 v[112:115], %[vw] offset:12288\n" \
  "s_waitcnt lgkmcnt(8)\n" \
  "v_mfma_f32_16x16x32_bf16 %[c4], v[120:123], v[80:83], %[c4]\n" \
  "s_waitcnt vmcnt(10)\n" \
  "ds_read_b128 v[116:119], %[vw] offset:13312\n" \
  "s_waitcnt lgkmcnt(8)\n" \
  "v_mfma_f32_16x16x32_bf16 %[c0], v[124:127], v[84:87], %[c0]\n" \
  "ds_read_b128 v[120:123], %[vw] offset:14336\n" \
  "s_waitcnt lgkmcnt(8)\n" \
  "v_mfma_f32_16x16x32_bf16 %[c1], v[128:131], v[84:87], %[c1]\n" \
  "ds_read_b128 v[124:127], %[vw] offset:20480\n" \
  "s_waitcnt lgkmcnt(8)\n" \
  "v_mfma_f32_16x16x32_bf16 %[c2], v[132:135], v[84:87], %[c2]\n" \
  "ds_read_b128 v[128:131], %[vw] offset:21504\n" \
  "s_waitcnt lgkmcnt(8)\n" \
  "v_mfma_f32_16x16x32_bf16 %[c3], v[136:139], v[84:87], %[c3]\n" \
  "ds_read_b128 v[132:135], %[vw] offset:22528\n" \
  "s_waitcnt lgkmcnt(8)\n" \
  "v_mfma_f32_16x16x32_bf16 %[c4], v[140:143], v[84:87], %[c4]\n" \
  "s_waitcnt vmcnt(9)\n" \
  "ds_read_b128 v[136:139], %[vw] offset:23552\n" \
  "s_waitcnt lgkmcnt(8)\n" \
  "v_mfma_f32_16x16x32_bf16 %[c0], v[104:107], v[88:91], %[c0]\n" \
  "ds_read_b128 v[140:143], %[vw] offset:24576\n" \
  "s_waitcnt lgkmcnt(8)\n" \
  "v_mfma_f32_16x16x32_bf16 %[c1], v[108:111], v[88:91], %[c1]\n" \
  "ds_read_b128 v[104:107], %[vw] offset:30720\n" \
  "s_waitcnt lgkmcnt(8)\n" \
  "v_mfma_f32_16x16x32_bf16 %[c2], v[112:115], v[88:91], %[c2]\n" \
  "ds_read_b128 v[108:111], %[vw] offset:31744\n" \
  "s_waitcnt lgkmcnt(8)\n" \
  "v_mfma_f32_16x16x32_bf16 %[c3], v[116:119], v[88:91], %[c3]\n" \
  "ds_read_b128 v[112:115], %[vw] offset:32768\n" \
  "s_waitcnt lgkmcnt(8)\n" \
  "v_mfma_f32_16x16x32_bf16 %[c4], v[120:123], v[88:91], %[c4]\n" \
  "s_waitcnt vmcnt(8)\n" \
  "ds_read_b128 v[116:119], %[vw] offset:33792\n" \
  "s_waitcnt lgkmcnt(8)\n" \
  "v_mfma_f32_16x16x32_bf16 %[c0], v[124:127], v[92:95], %[c0]\n" \
  "ds_read_b128 v[120:123], %[vw] offset:34816\n" \
  "s_waitcnt lgkmcnt(8)\n" \
  "v_mfma_f32_16x16x32_bf16 %[c1], v[128:131], v[92:95], %[c1]\n" \
  "ds_read_b128 v[124:127], %[vw] offset:40960\n" \
  "s_waitcnt lgkmcnt(8)\n" \
  "v_mfma_f32_16x16x32_bf16 %[c2], v[132:135], v[92:95], %[c2]\n" \
  "ds_read_b128 v[128:131], %[vw] offset:41984\n" \
  "s_waitcnt lgkmcnt(8)\n" \
  "v_mfma_f32_16x16x32_bf16 %[c3], v[136:139], v[92:95], %[c3]\n" \
  "ds_read_b128 v[132:135], %[vw] offset:43008\n" \
  "s_waitcnt lgkmcnt(8)\n" \
  "v_mfma_f32_16x16x32_bf16 %[c4], v[140:143], v[92:95], %[c4]\n" \
  "s_waitcnt vmcnt(7)\n" \
  "ds_read_b128 v[136:139], %[vw] offset:44032\n" \
  "s_waitcnt lgkmcnt(8)\n" \
  "v_mfma_f32_16x16x32_bf16 %[c0], v[104:107], v[96:99], %[c0]\n" \
  "ds_read_b128 v[140:143], %[vw] offset:45056\n" \
  "s_waitcnt lgkmcnt(8)\n" \
  "v_mfma_f32_16x16x32_bf16 %[c1], v[108:111], v[96:99], %[c1]\n" \
  "s_waitcnt lgkmcnt(7)\n" \
  "v_mfma_f32_16x16x32_bf16 %[c2], v[112:115], v[96:99], %[c2]\n" \
  "s_waitcnt lgkmcnt(6)\n" \
  "v_mfma_f32_16x16x32_bf16 %[c3], v[116:119], v[96:99], %[c3]\n" \
  "s_waitcnt lgkmcnt(5)\n" \
  "v_mfma_f32_16x16x32_bf16 %[c4], v[120:123], v[96:99], %[c4]\n" \
  "s_waitcnt vmcnt(6)\n" \
  "s_waitcnt lgkmcnt(4)\n" \
  "v_mfma_f32_16x16x32_bf16 %[c0], v[124:127], v[100:103], %[c0]\n" \
  "s_waitcnt lgkmcnt(3)\n" \
  "v_mfma_f32_16x16x32_bf16 %[c1], v[128:131], v[100:103], %[c1]\n" \
  "s_waitcnt lgkmcnt(2)\n" \
  "v_mfma_f32_16x16x32_bf16 %[c2], v[132:135], v[100:103], %[c2]\n" \
  "s_waitcnt lgkmcnt(1)\n" \
  "v_mfma_f32_16x16x32_bf16 %[c3], v[136:139], v[100:103], %[c3]\n" \
  "s_waitcnt lgkmcnt(0)\n" \
  "v_mfma_f32_16x16x32_bf16 %[c4], v[140:143], v[100:103], %[c4]\n" \
  "s_waitcnt vmcnt(2)\n" \
  "s_nop 7\n" \
  ""
#define P5_ASM \
  "s_nop 4\n" \
  "buffer_load_dwordx4 v[64:67], %[va0], %[rs], %[sa] offen offset:0 sc1\n" \
  "buffer_load_dwordx4 v[68:71], %[va0], %[rs], %[sa] offen offset:1024 sc1\n" \
  "buffer_load_dwordx4 v[72:75], %[va0], %[rs], %[sa] offen offset:2048 sc1\n" \
  "buffer_load_dwordx4 v[76:79], %[va0], %[rs], %[sa] offen offset:3072 sc1\n" \
  "s_add_u32 %[sa], %[sa], 4096\n" \
  "buffer_load_dwordx4 v[80:83], %[va0], %[rs], %[sa] offen offset:0 sc1\n" \
  "buffer_load_dwordx4 v[84:87], %[va1], %[rs], %[sa] offen offset:1024 sc1\n" \
  "buffer_load_dwordx4 v[88:91], %[va1], %[rs], %[sa] offen offset:2048 sc1\n" \
  "buffer_load_dwordx4 v[92:95], %[va1], %[rs], %[sa] offen offset:3072 sc1\n" \
  "s_add_u32 %[sa], %[sa], 4096\n" \
  "buffer_load_dwordx4 v[96:99], %[va1], %[rs], %[sa] offen offset:0 sc1\n" \
  "buffer_load_dwordx4 v[100:103], %[va1], %[rs], %[sa] offen offset:1024 sc1\n" \
  "ds_read_b128 v[104:107], %[vw] offset:51200\n" \
  "ds_read_b128 v[108:111], %[vw] offset:52224\n" \
  "ds_read_b128 v[112:115], %[vw] offset:53248\n" \
  "ds_read_b128 v[116:119], %[vw] offset:54272\n" \
  "ds_read_b128 v[120:123], %[vw] offset:55296\n" \
  "ds_read_b128 v[124:127], %[vw] offset:61440\n" \
  "ds_read_b128 v[128:131], %[vw] offset:62464\n" \
  "ds_read_b128 v[132:135], %[vw] offset:63488\n" \
  "s_waitcnt vmcnt(9)\n" \
  "ds_read_b128 v[136:139], %[vw] offset:64512\n" \
  "s_waitcnt lgkmcnt(8)\n" \
  "v_mfma_f32_16x16x32_bf16 %[c0], v[104:107], v[64:67], 0\n" \
  "v_add_u32 %[vw], 65536, %[vw]\n" \
  "ds_read_b128 v[140:143], %[vw] offset:0\n" \
  "s_waitcnt lgkmcnt(8)\n" \
  "v_mfma_f32_16x16x32_bf16 %[c1], v[108:111], v[64:67], 0\n" \
  "ds_read_b128 v[104:107], %[vw] offset:6144\n" \
  "s_waitcnt lgkmcnt(8)\n" \
  "v_mfma_f32_16x16x32_bf16 %[c2], v[112:115], v[64:67], 0\n" \
  "ds_read_b128 v[108:111], %[vw] offset:7168\n" \
  "s_waitcnt lgkmcnt(8)\n" \
  "v_mfma_f32_16x16x32_bf16 %[c3], v[116:119], v[64:67], 0\n" \
  "ds_read_b128 v[112:115], %[vw] offset:8192\n" \
  "s_waitcnt lgkmcnt(8)\n" \
  "v_mfma_f32_16x16x32_bf16 %[c4], v[120:123], v[64:67], 0\n" \
  "s_waitcnt vmcnt(8)\n" \
  "ds_read_b128 v[116:119], %[vw] offset:9216\n" \
  "s_waitcnt lgkmcnt(8)\n" \
  "v_mfma_f32_16x16x32_bf16 %[c0], v[124:127], v[68:71], %[c0]\n" \
  "ds_read_b128 v[120:123], %[vw] offset:10240\n" \
  "s_waitcnt lgkmcnt(8)\n" \
  "v_mfma_f32_16x16x32_bf16 %[c1], v[128:131], v[68:71], %[c1]\n" \
  "ds_read_b128 v[124:127], %[vw] offset:16384\n" \
  "s_waitcnt lgkmcnt(8)\n" \
  "v_mfma_f32_16x16x32_bf16 %[c2], v[132:135], v[68:71], %[c2]\n" \
  "ds_read_b128 v[128:131], %[vw] offset:17408\n" \
  "s_waitcnt lgkmcnt(8)\n" \
  "v_mfma_f32_16x16x32_bf16 %[c3], v[136:139], v[68:71], %[c3]\n" \
  "ds_read_b128 v[132:135], %[vw] offset:18432\n" \
  "s_waitcnt lgkmcnt(8)\n" \
  "v_mfma_f32_16x16x32_bf16 %[c4], v[140:143], v[68:71], %[c4]\n" \
  "s_waitcnt vmcnt(7)\n" \
  "ds_read_b128 v[136:139], %[vw] offset:19456\n" \
  "s_waitcnt lgkmcnt(8)\n" \
  "v_mfma_f32_16x16x32_bf16 %[c0], v[104:107], v[72:75], %[c0]\n" \
  "ds_read_b128 v[140:143], %[vw] offset:20480\n" \
  "s_waitcnt lgkmcnt(8)\n" \
  "v_mfma_f32_16x16x32_bf16 %[c1], v[108:111], v[72:75], %[c1]\n" \
  "ds_read_b128 v[104:107], %[vw] offset:26624\n" \
  "s_waitcnt lgkmcnt(8)\n" \
  "v_mfma_f32_16x16x32_bf16 %[c2], v[112:115], v[72:75], %[c2]\n" \
  "ds_read_b128 v[108:111], %[vw] offset:27648\n" \
  "s_waitcnt lgkmcnt(8)\n" \
  "v_mfma_f32_16x16x32_bf16 %[c3], v[116:119], v[72:75], %[c3]\n" \
  "ds_read_b128 v[112:115], %[vw] offset:28672\n" \
  "s_waitcnt lgkmcnt(8)\n" \
  "v_mfma_f32_16x16x32_bf16 %[c4], v[120:123], v[72:75], %[c4]\n" \
  "s_waitcnt vmcnt(6)\n" \
  "ds_read_b128 v[116:119], %[vw] offset:29696\n" \
  "s_waitcnt lgkmcnt(8)\n" \
  "v_mfma_f32_16x16x32_bf16 %[c0], v[124:127], v[76:79], %[c0]\n" \
  "ds_read_b128 v[120:123], %[vw] offset:30720\n" \
  "s_waitcnt lgkmcnt(8)\n" \
  "v_mfma_f32_16x16x32_bf16 %[c1], v[128:131], v[76:79], %[c1]\n" \
  "ds_read_b128 v[124:127], %[vw] offset:36864\n" \
  "s_waitcnt lgkmcnt(8)\n" \
  "v_mfma_f32_16x16x32_bf16 %[c2], v[132:135], v[76:79], %[c2]\n" \
  "ds_read_b128 v[128:131], %[vw] offset:37888\n" \
  "s_waitcnt lgkmcnt(8)\n" \
  "v_mfma_f32_16x16x32_bf16 %[c3], v[136:139], v[76:79], %[c3]\n" \
  "ds_read_b128 v[132:135], %[vw] offset:38912\n" \
  "s_waitcnt lgkmcnt(8)\n" \
  "v_mfma_f32_16x16x32_bf16 %[c4], v[140:143], v[76:79], %[c4]\n" \
  "s_barrier\n" \
  "s_mov_b32 m0, %[dm0]\n" \
  "s_nop 0\n" \
  "buffer_load_dwordx4 %[l16], %[rs], %[ds0] offen offset:0 sc1 lds\n" \
  "buffer_load_dwordx4 %[l16], %[rs], %[ds0] offen offset:1024 sc1 lds\n" \
  "s_waitcnt vmcnt(7)\n" \
  "ds_read_b128 v[136:139], %[vw] offset:39936\n" \
  "s_waitcnt lgkmcnt(8)\n" \
  "v_mfma_f32_16x16x32_bf16 %[c0], v[104:107], v[80:83], %[c0]\n" \
  "ds_read_b128 v[140:143], %[vw] offset:40960\n" \
  "s_waitcnt lgkmcnt(8)\n" \
  "v_mfma_f32_16x16x32_bf16 %[c1], v[108:111], v[80:83], %[c1]\n" \
  "ds_read_b128 v[104:107], %[vw] offset:47104\n" \
  "s_waitcnt lgkmcnt(8)\n" \
  "v_mfma_f32_16x16x32_bf16 %[c2], v[112:115], v[80:83], %[c2]\n" \
  "ds_read_b128 v[108:111], %[vw] offset:48128\n" \
  "s_waitcnt lgkmcnt(8)\n" \
  "v_mfma_f32_16x16x32_bf16 %[c3], v[116:119], v[80:83], %[c3]\n" \
  "ds_read_b128 v[112:115], %[vw] offset:49152\n" \
  "s_waitcnt lgkmcnt(8)\n" \
  "v_mfma_f32_16x16x32_bf16 %[c4], v[120:123], v[80:83], %[c4]\n" \
  "s_waitcnt vmcnt(6)\n" \
  "ds_read_b128 v[116:119], %[vw] offset:50176\n" \
  "s_waitcnt lgkmcnt(8)\n" \
  "v_mfma_f32_16x16x32_bf16 %[c0], v[124:127], v[84:87], %[c0]\n" \
  "ds_read_b128 v[120:123], %[vw] offset:51200\n" \
  "s_waitcnt lgkmcnt(8)\n" \
  "v_mfma_f32_16x16x32_bf16 %[c1], v[128:131], v[84:87], %[c1]\n" \
  "ds_read_b128 v[124:127], %[vw] offset:57344\n" \
  "s_waitcnt lgkmcnt(8)\n" \
  "v_mfma_f32_16x16x32_bf16 %[c2], v[132:135], v[84:87], %[c2]\n" \
  "ds_read_b128 v[128:131], %[vw] offset:58368\n" \
  "s_waitcnt lgkmcnt(8)\n" \
  "v_mfma_f32_16x16x32_bf16 %[c3], v[136:139], v[84:87], %[c3]\n" \
  "ds_read_b128 v[132:135], %[vw] offset:59392\n" \
  "s_waitcnt lgkmcnt(8)\n" \
  "v_mfma_f32_16x16x32_bf16 %[c4], v[140:143], v[84:87], %[c4]\n" \
  "s_waitcnt vmcnt(5)\n" \
  "ds_read_b128 v[136:139], %[vw] offset:60416\n" \
  "s_waitcnt lgkmcnt(8)\n" \
  "v_mfma_f32_16x16x32_bf16 %[c0], v[104:107], v[88:91], %[c0]\n" \
  "ds_read_b128 v[140:143], %[vw] offset:61440\n" \
  "s_waitcnt lgkmcnt(8)\n" \
  "v_mfma_f32_16x16x32_bf16 %[c1], v[108:111], v[88:91], %[c1]\n" \
  "v_add_u32 %[vw], 65536, %[vw]\n" \
  "ds_read_b128 v[104:107], %[vw] offset:2048\n" \
  "s_waitcnt lgkmcnt(8)\n" \
  "v_mfma_f32_16x16x32_bf16 %[c2], v[112:115], v[88:91], %[c2]\n" \
  "ds_read_b128 v[108:111], %[vw] offset:3072\n" \
  "s_waitcnt lgkmcnt(8)\n" \
  "v_mfma_f32_16x16x32_bf16 %[c3], v[116:119], v[88:91], %[c3]\n" \
  "ds_read_b128 v[112:115], %[vw] offset:4096\n" \
  "s_waitcnt lgkmcnt(8)\n" \
  "v_mfma_f32_16x16x32_bf16 %[c4], v[120:123], v[88:91], %[c4]\n" \
  "s_waitcnt vmcnt(4)\n" \
  "ds_read_b128 v[116:119], %[vw] offset:5120\n" \
  "s_waitcnt lgkmcnt(8)\n" \
  "v_mfma_f32_16x16x32_bf16 %[c0], v[124:127], v[92:95], %[c0]\n" \
  "ds_read_b128 v[120:123], %[vw] offset:6144\n" \
  "s_waitcnt lgkmcnt(8)\n" \
  "v_mfma_f32_16x16x32_bf16 %[c1], v[128:131], v[92:95], %[c1]\n" \
  "ds_read_b128 v[124:127], %[vw] offset:12288\n" \
  "s_waitcnt lgkmcnt(8)\n" \
  "v_mfma_f32_16x16x32_bf16 %[c2], v[132:135], v[92:95], %[c2]\n" \
  "ds_read_b128 v[128:131], %[vw] offset:13312\n" \
  "s_waitcnt lgkmcnt(8)\n" \
  "v_mfma_f32_16x16x32_bf16 %[c3], v[136:139], v[92:95], %[c3]\n" \
  "ds_read_b128 v[132:135], %[vw] offset:14336\n" \
  "s_waitcnt lgkmcnt(8)\n" \
  "v_mfma_f32_16x16x32_bf16 %[c4], v[140:143], v[92:95], %[c4]\n" \
  "s_waitcnt vmcnt(3)\n" \
  "ds_read_b128 v[136:139], %[vw] offset:15360\n" \
  "s_waitcnt lgkmcnt(8)\n" \
  "v_mfma_f32_16x16x32_bf16 %[c0], v[104:107], v[96:99], %[c0]\n" \
  "ds_read_b128 v[140:143], %[vw] offset:16384\n" \
  "s_waitcnt lgkmcnt(8)\n" \
  "v_mfma_f32_16x16x32_bf16 %[c1], v[108:111], v[96:99], %[c1]\n" \
  "s_waitcnt lgkmcnt(7)\n" \
  "v_mfma_f32_16x16x32_bf16 %[c2], v[112:115], v[96:99], %[c2]\n" \
  "s_waitcnt lgkmcnt(6)\n" \
  "v_mfma_f32_16x16x32_bf16 %[c3], v[116:119], v[96:99], %[c3]\n" \
  "s_waitcnt lgkmcnt(5)\n" \
  "v_mfma_f32_16x16x32_bf16 %[c4], v[120:123], v[96:99], %[c4]\n" \
  "s_waitcnt vmcnt(2)\n" \
  "s_waitcnt lgkmcnt(4)\n" \
  "v_mfma_f32_16x16x32_bf16 %[c0], v[124:127], v[100:103], %[c0]\n" \
  "s_waitcnt lgkmcnt(3)\n" \
  "v_mfma_f32_16x16x32_bf16 %[c1], v[128:131], v[100:103], %[c1]\n" \
  "s_waitcnt lgkmcnt(2)\n" \
  "v_mfma_f32_16x16x32_bf16 %[c2], v[132:135], v[100:103], %[c2]\n" \
  "s_waitcnt lgkmcnt(1)\n" \
  "v_mfma_f32_16x16x32_bf16 %[c3], v[136:139], v[100:103], %[c3]\n" \
  "s_waitcnt lgkmcnt(0)\n" \
  "v_mfma_f32_16x16x32_bf16 %[c4], v[140:143], v[100:103], %[c4]\n" \
  "s_nop 7\n" \
  ""
#define PS_CLOB "v64", "v65", "v66", "v67", "v68", "v69", "v70", "v71", "v72", "v73", "v74", "v75", "v76", "v77", "v78", "v79", "v80", "v81", "v82", "v83", "v84", "v85", "v86", "v87", "v88", "v89", "v90", "v91", "v92", "v93", "v94", "v95", "v96", "v97", "v98", "v99", "v100", "v101", "v102", "v103", "v104", "v105", "v106", "v107", "v108", "v109", "v110", "v111", "v112", "v113", "v114", "v115", "v116", "v117", "v118", "v119", "v120", "v121", "v122", "v123", "v124", "v125", "v126", "v127", "v128", "v129", "v130", "v131", "v132", "v133", "v134", "v135", "v136", "v137", "v138", "v139", "v140", "v141", "v142", "v143"
#define P6_ASM \
  "s_nop 4\n" \
  "buffer_load_dwordx4 v[64:67], %[va0], %[rs], %[sa] offen offset:0 sc1\n" \
  "buffer_load_dwordx4 v[68:71], %[va0], %[rs], %[sa] offen offset:1024 sc1\n" \
  "buffer_load_dwordx4 v[72:75], %[va0], %[rs], %[sa] offen offset:2048 sc1\n" \
  "buffer_load_dwordx4 v[76:79], %[va0], %[rs], %[sa] offen offset:3072 sc1\n" \
  "s_add_u32 %[sa], %[sa], 4096\n" \
  "buffer_load_dwordx4 v[80:83], %[va0], %[rs], %[sa] offen offset:0 sc1\n" \
  "buffer_load_dwordx4 v[84:87], %[va0], %[rs], %[sa] offen offset:1024 sc1\n" \
  "buffer_load_dwordx4 v[88:91], %[va0], %[rs], %[sa] offen offset:2048 sc1\n" \
  "buffer_load_dwordx4 v[92:95], %[va0], %[rs], %[sa] offen offset:3072 sc1\n" \
  "s_add_u32 %[sa], %[sa], 4096\n" \
  "buffer_load_dwordx4 v[96:99], %[va0], %[rs], %[sa] offen offset:0 sc1\n" \
  "buffer_load_dwordx4 v[100:103], %[va0], %[rs], %[sa] offen offset:1024 sc1\n" \
  "buffer_load_dwordx4 v[104:107], %[va0], %[rs], %[sa] offen offset:2048 sc1\n" \
  "buffer_load_dwordx4 v[108:111], %[va0], %[rs], %[sa] offen offset:3072 sc1\n" \
  "s_add_u32 %[sa], %[sa], 4096\n" \
  "buffer_load_dwordx4 v[112:115], %[va0], %[rs], %[sa] offen offset:0 sc1\n" \
  "buffer_load_dwordx4 v[116:119], %[va0], %[rs], %[sa] offen offset:1024 sc1\n" \
  "buffer_load_dwordx4 v[120:123], %[va0], %[rs], %[sa] offen offset:2048 sc1\n" \
  "buffer_load_dwordx4 v[124:127], %[va0], %[rs], %[sa] offen offset:3072 sc1\n" \
  "s_add_u32 %[sa], %[sa], 4096\n" \
  "buffer_load_dwordx4 v[128:131], %[va0], %[rs], %[sa] offen offset:0 sc1\n" \
  "buffer_load_dwordx4 v[132:135], %[va0], %[rs], %[sa] offen offset:1024 sc1\n" \
  "buffer_load_dwordx4 v[136:139], %[va0], %[rs], %[sa] offen offset:2048 sc1\n" \
  "buffer_load_dwordx4 v[140:143], %[va0], %[rs], %[sa] offen offset:3072 sc1\n" \
  "buffer_load_dwordx2 %[st0], %[vst], %[rs], 0 offen offset:0 sc1\n" \
  "buffer_load_dwordx2 %[st1], %[vst], %[rs], 0 offen offset:128 sc1\n" \
  "buffer_load_dwordx2 %[st2], %[vst], %[rs], 0 offen offset:256 sc1\n" \
  "buffer_load_dwordx2 %[st3], %[vst], %[rs], 0 offen offset:384 sc1\n" \
  "ds_read_b128 v[144:147], %[vw] offset:0\n" \
  "ds_read_b128 v[148:151], %[vw] offset:1024\n" \
  "ds_read_b128 v[152:155], %[vw] offset:2048\n" \
  "ds_read_b128 v[156:159], %[vw] offset:3072\n" \
  "ds_read_b128 v[160:163], %[vw] offset:4096\n" \
  "ds_read_b128 v[164:167], %[vw] offset:5120\n" \
  "ds_read_b128 v[168:171], %[vw] offset:6144\n" \
  "ds_read_b128 v[172:175], %[vw] offset:7168\n" \
  "s_waitcnt vmcnt(23)\n" \
  "ds_read_b128 v[176:179], %[vw] offset:8192\n" \
  "s_waitcnt lgkmcnt(8)\n" \
  "v_mfma_f32_16x16x32_bf16 %[c0], v[144:147], v[64:67], 0\n" \
  "ds_read_b128 v[180:183], %[vw] offset:9216\n" \
  "s_waitcnt lgkmcnt(8)\n" \
  "v_mfma_f32_16x16x32_bf16 %[c1], v[148:151], v[64:67], 0\n" \
  "ds_read_b128 v[144:147], %[vw] offset:10240\n" \
  "s_waitcnt lgkmcnt(8)\n" \
  "v_mfma_f32_16x16x32_bf16 %[c2], v[152:155], v[64:67], 0\n" \
  "ds_read_b128 v[148:151], %[vw] offset:11264\n" \
  "s_waitcnt lgkmcnt(8)\n" \
  "v_mfma_f32_16x16x32_bf16 %[c3], v[156:159], v[64:67], 0\n" \
  "ds_read_b128 v[152:155], %[vw] offset:12288\n" \
  "s_waitcnt lgkmcnt(8)\n" \
  "v_mfma_f32_16x16x32_bf16 %[c4], v[160:163], v[64:67], 0\n" \
  "ds_read_b128 v[156:159], %[vw] offset:13312\n" \
  "s_waitcnt lgkmcnt(8)\n" \
  "v_mfma_f32_16x16x32_bf16 %[c5], v[164:167], v[64:67], 0\n" \
  "s_waitcnt vmcnt(22)\n" \
  "ds_read_b128 v[160:163], %[vw] offset:14336\n" \
  "s_waitcnt lgkmcnt(8)\n" \
  "v_mfma_f32_16x16x32_bf16 %[c0], v[168:171], v[68:71], %[c0]\n" \
  "ds_read_b128 v[164:167], %[vw] offset:15360\n" \
  "s_waitcnt lgkmcnt(8)\n" \
  "v_mfma_f32_16x16x32_bf16 %[c1], v[172:175], v[68:71], %[c1]\n" \
  "ds_read_b128 v[168:171], %[vw] offset:16384\n" \
  "s_waitcnt lgkmcnt(8)\n" \
  "v_mfma_f32_16x16x32_bf16 %[c2], v[176:179], v[68:71], %[c2]\n" \
  "ds_read_b128 v[172:175], %[vw] offset:17408\n" \
  "s_waitcnt lgkmcnt(8)\n" \
  "v_mfma_f32_16x16x32_bf16 %[c3], v[180:183], v[68:71], %[c3]\n" \
  "ds_read_b128 v[176:179], %[vw] offset:18432\n" \
  "s_waitcnt lgkmcnt(8)\n" \
  "v_mfma_f32_16x16x32_bf16 %[c4], v[144:147], v[68:71], %[c4]\n" \
  "ds_read_b128 v[180:183], %[vw] offset:19456\n" \
  "s_waitcnt lgkmcnt(8)\n" \
  "v_mfma_f32_16x16x32_bf16 %[c5], v[148:151], v[68:71], %[c5]\n" \
  "s_waitcnt vmcnt(21)\n" \
  "ds_read_b128 v[144:147], %[vw] offset:20480\n" \
  "s_waitcnt lgkmcnt(8)\n" \
  "v_mfma_f32_16x16x32_bf16 %[c0], v[152:155], v[72:75], %[c0]\n" \
  "ds_read_b128 v[148:151], %[vw] offset:21504\n" \
  "s_waitcnt lgkmcnt(8)\n" \
  "v_mfma_f32_16x16x32_bf16 %[c1], v[156:159], v[72:75], %[c1]\n" \
  "ds_read_b128 v[152:155], %[vw] offset:22528\n" \
  "s_waitcnt lgkmcnt(8)\n" \
  "v_mfma_f32_16x16x32_bf16 %[c2], v[160:163], v[72:75], %[c2]\n" \
  "ds_read_b128 v[156:159], %[vw] offset:23552\n" \
  "s_waitcnt lgkmcnt(8)\n" \
  "v_mfma_f32_16x16x32_bf16 %[c3], v[164:167], v[72:75], %[c3]\n" \
  "ds_read_b128 v[160:163], %[vw] offset:24576\n" \
  "s_waitcnt lgkmcnt(8)\n" \
  "v_mfma_f32_16x16x32_bf16 %[c4], v[168:171], v[72:75], %[c4]\n" \
  "ds_read_b128 v[164:167], %[vw] offset:25600\n" \
  "s_waitcnt lgkmcnt(8)\n" \
  "v_mfma_f32_16x16x32_bf16 %[c5], v[172:175], v[72:75], %[c5]\n" \
  "s_waitcnt vmcnt(20)\n" \
  "ds_read_b128 v[168:171], %[vw] offset:26624\n" \
  "s_waitcnt lgkmcnt(8)\n" \
  "v_mfma_f32_16x16x32_bf16 %[c0], v[176:179], v[76:79], %[c0]\n" \
  "ds_read_b128 v[172:175], %[vw] offset:27648\n" \
  "s_waitcnt lgkmcnt(8)\n" \
  "v_mfma_f32_16x16x32_bf16 %[c1], v[180:183], v[76:79], %[c1]\n" \
  "ds_read_b128 v[176:179], %[vw] offset:28672\n" \
  "s_waitcnt lgkmcnt(8)\n" \
  "v_mfma_f32_16x16x32_bf16 %[c2], v[144:147], v[76:79], %[c2]\n" \
  "ds_read_b128 v[180:183], %[vw] offset:29696\n" \
  "s_waitcnt lgkmcnt(8)\n" \
  "v_mfma_f32_16x16x32_bf16 %[c3], v[148:151], v[76:79], %[c3]\n" \
  "ds_read_b128 v[144:147], %[vw] offset:30720\n" \
  "s_waitcnt lgkmcnt(8)\n" \
  "v_mfma_f32_16x16x32_bf16 %[c4], v[152:155], v[76:79], %[c4]\n" \
  "ds_read_b128 v[148:151], %[vw] offset:31744\n" \
  "s_waitcnt lgkmcnt(8)\n" \
  "v_mfma_f32_16x16x32_bf16 %[c5], v[156:159], v[76:79], %[c5]\n" \
  "s_waitcnt vmcnt(19)\n" \
  "ds_read_b128 v[152:155], %[vw] offset:32768\n" \
  "s_waitcnt lgkmcnt(8)\n" \
  "v_mfma_f32_16x16x32_bf16 %[c0], v[160:163], v[80:83], %[c0]\n" \
  "ds_read_b128 v[156:159], %[vw] offset:33792\n" \
  "s_waitcnt lgkmcnt(8)\n" \
  "v_mfma_f32_16x16x32_bf16 %[c1], v[164:167], v[80:83], %[c1]\n" \
  "ds_read_b128 v[160:163], %[vw] offset:34816\n" \
  "s_waitcnt lgkmcnt(8)\n" \
  "v_mfma_f32_16x16x32_bf16 %[c2], v[168:171], v[80:83], %[c2]\n" \
  "ds_read_b128 v[164:167], %[vw] offset:35840\n" \
  "s_waitcnt lgkmcnt(8)\n" \
  "v_mfma_f32_16x16x32_bf16 %[c3], v[172:175], v[80:83], %[c3]\n" \
  "ds_read_b128 v[168:171], %[vw] offset:36864\n" \
  "s_waitcnt lgkmcnt(8)\n" \
  "v_mfma_f32_16x16x32_bf16 %[c4], v[176:179], v[80:83], %[c4]\n" \
  "ds_read_b128 v[172:175], %[vw] offset:37888\n" \
  "s_waitcnt lgkmcnt(8)\n" \
  "v_mfma_f32_16x16x32_bf16 %[c5], v[180:183], v[80:83], %[c5]\n" \
  "s_waitcnt vmcnt(18)\n" \
  "ds_read_b128 v[176:179], %[vw] offset:38912\n" \
  "s_waitcnt lgkmcnt(8)\n" \
  "v_mfma_f32_16x16x32_bf16 %[c0], v[144:147], v[84:87], %[c0]\n" \
  "ds_read_b128 v[180:183], %[vw] offset:39936\n" \
  "s_waitcnt lgkmcnt(8)\n" \
  "v_mfma_f32_16x16x32_bf16 %[c1], v[148:151], v[84:87], %[c1]\n" \
  "ds_read_b128 v[144:147], %[vw] offset:40960\n" \
  "s_waitcnt lgkmcnt(8)\n" \
  "v_mfma_f32_16x16x32_bf16 %[c2], v[152:155], v[84:87], %[c2]\n" \
  "ds_read_b128 v[148:151], %[vw] offset:41984\n" \
  "s_waitcnt lgkmcnt(8)\n" \
  "v_mfma_f32_16x16x32_bf16 %[c3], v[156:159], v[84:87], %[c3]\n" \
  "ds_read_b128 v[152:155], %[vw] offset:43008\n" \
  "s_waitcnt lgkmcnt(8)\n" \
  "v_mfma_f32_16x16x32_bf16 %[c4], v[160:163], v[84:87], %[c4]\n" \
  "ds_read_b128 v[156:159], %[vw] offset:44032\n" \
  "s_waitcnt lgkmcnt(8)\n" \
  "v_mfma_f32_16x16x32_bf16 %[c5], v[164:167], v[84:87], %[c5]\n" \
  "s_waitcnt vmcnt(17)\n" \
  "ds_read_b128 v[160:163], %[vw] offset:45056\n" \
  "s_waitcnt lgkmcnt(8)\n" \
  "v_mfma_f32_16x16x32_bf16 %[c0], v[168:171], v[88:91], %[c0]\n" \
  "ds_read_b128 v[164:167], %[vw] offset:46080\n" \
  "s_waitcnt lgkmcnt(8)\n" \
  "v_mfma_f32_16x16x32_bf16 %[c1], v[172:175], v[88:91], %[c1]\n" \
  "ds_read_b128 v[168:171], %[vw] offset:47104\n" \
  "s_waitcnt lgkmcnt(8)\n" \
  "v_mfma_f32_16x16x32_bf16 %[c2], v[176:179], v[88:91], %[c2]\n" \
  "ds_read_b128 v[172:175], %[vw] offset:48128\n" \
  "s_waitcnt lgkmcnt(8)\n" \
  "v_mfma_f32_16x16x32_bf16 %[c3], v[180:183], v[88:91], %[c3]\n" \
  "ds_read_b128 v[176:179], %[vw] offset:49152\n" \
  "s_waitcnt lgkmcnt(8)\n" \
  "v_mfma_f32_16x16x32_bf16 %[c4], v[144:147], v[88:91], %[c4]\n" \
  "ds_read_b128 v[180:183], %[vw] offset:50176\n" \
  "s_waitcnt lgkmcnt(8)\n" \
  "v_mfma_f32_16x16x32_bf16 %[c5], v[148:151], v[88:91], %[c5]\n" \
  "s_waitcnt vmcnt(16)\n" \
  "ds_read_b128 v[144:147], %[vw] offset:51200\n" \
  "s_waitcnt lgkmcnt(8)\n" \
  "v_mfma_f32_16x16x32_bf16 %[c0], v[152:155], v[92:95], %[c0]\n" \
  "ds_read_b128 v[148:151], %[vw] offset:52224\n" \
  "s_waitcnt lgkmcnt(8)\n" \
  "v_mfma_f32_16x16x32_bf16 %[c1], v[156:159], v[92:95], %[c1]\n" \
  "ds_read_b128 v[152:155], %[vw] offset:53248\n" \
  "s_waitcnt lgkmcnt(8)\n" \
  "v_mfma_f32_16x16x32_bf16 %[c2], v[160:163], v[92:95], %[c2]\n" \
  "ds_read_b128 v[156:159], %[vw] offset:54272\n" \
  "s_waitcnt lgkmcnt(8)\n" \
  "v_mfma_f32_16x16x32_bf16 %[c3], v[164:167], v[92:95], %[c3]\n" \
  "ds_read_b128 v[160:163], %[vw] offset:55296\n" \
  "s_waitcnt lgkmcnt(8)\n" \
  "v_mfma_f32_16x16x32_bf16 %[c4], v[168:171], v[92:95], %[c4]\n" \
  "ds_read_b128 v[164:167], %[vw] offset:56320\n" \
  "s_waitcnt lgkmcnt(8)\n" \
  "v_mfma_f32_16x16x32_bf16 %[c5], v[172:175], v[92:95], %[c5]\n" \
  "s_barrier\n" \
  "s_mov_b32 m0, %[dm0]\n" \
  "s_nop 0\n" \
  "buffer_load_dwordx4 %[l16], %[rs], %[ds0] offen offset:0 sc1 lds\n" \
  "buffer_load_dwordx4 %[l16], %[rs], %[ds0] offen offset:1024 sc1 lds\n" \
  "buffer_load_dwordx4 %[l16], %[rs], %[ds0] offen offset:2048 sc1 lds\n" \
  "buffer_load_dwordx4 %[l16], %[rs], %[ds0] offen offset:3072 sc1 lds\n" \
  "s_add_u32 %[dm0], %[dm0], 4096\n" \
  "s_add_u32 %[ds0], %[ds0], 4096\n" \
  "s_mov_b32 m0, %[dm0]\n" \
  "s_nop 0\n" \
  "buffer_load_dwordx4 %[l16], %[rs], %[ds0] offen offset:0 sc1 lds\n" \
  "buffer_load_dwordx4 %[l16], %[rs], %[ds0] offen offset:1024 sc1 lds\n" \
  "s_waitcnt vmcnt(21)\n" \
  "ds_read_b128 v[168:171], %[vw] offset:57344\n" \
  "s_waitcnt lgkmcnt(8)\n" \
  "v_mfma_f32_16x16x32_bf16 %[c0], v[176:179], v[96:99], %[c0]\n" \
  "ds_read_b128 v[172:175], %[vw] offset:58368\n" \
  "s_waitcnt lgkmcnt(8)\n" \
  "v_mfma_f32_16x16x32_bf16 %[c1], v[180:183], v[96:99], %[c1]\n" \
  "ds_read_b128 v[176:179], %[vw] offset:59392\n" \
  "s_waitcnt lgkmcnt(8)\n" \
  "v_mfma_f32_16x16x32_bf16 %[c2], v[144:147], v[96:99], %[c2]\n" \
  "ds_read_b128 v[180:183], %[vw] offset:60416\n" \
  "s_waitcnt lgkmcnt(8)\n" \
  "v_mfma_f32_16x16x32_bf16 %[c3], v[148:151], v[96:99], %[c3]\n" \
  "ds_read_b128 v[144:147], %[vw] offset:61440\n" \
  "s_waitcnt lgkmcnt(8)\n" \
  "v_mfma_f32_16x16x32_bf16 %[c4], v[152:155], v[96:99], %[c4]\n" \
  "ds_read_b128 v[148:151], %[vw] offset:62464\n" \
  "s_waitcnt lgkmcnt(8)\n" \
  "v_mfma_f32_16x16x32_bf16 %[c5], v[156:159], v[96:99], %[c5]\n" \
  "s_waitcnt vmcnt(20)\n" \
  "ds_read_b128 v[152:155], %[vw] offset:63488\n" \
  "s_waitcnt lgkmcnt(8)\n" \
  "v_mfma_f32_16x16x32_bf16 %[c0], v[160:163], v[100:103], %[c0]\n" \
  "ds_read_b128 v[156:159], %[vw] offset:64512\n" \
  "s_waitcnt lgkmcnt(8)\n" \
  "v_mfma_f32_16x16x32_bf16 %[c1], v[164:167], v[100:103], %[c1]\n" \
  "v_add_u32 %[vw], 65536, %[vw]\n" \
  "ds_read_b128 v[160:163], %[vw] offset:0\n" \
  "s_waitcnt lgkmcnt(8)\n" \
  "v_mfma_f32_16x16x32_bf16 %[c2], v[168:171], v[100:103], %[c2]\n" \
  "ds_read_b128 v[164:167], %[vw] offset:1024\n" \
  "s_waitcnt lgkmcnt(8)\n" \
  "v_mfma_f32_16x16x32_bf16 %[c3], v[172:175], v[100:103], %[c3]\n" \
  "ds_read_b128 v[168:171], %[vw] offset:2048\n" \
  "s_waitcnt lgkmcnt(8)\n" \
  "v_mfma_f32_16x16x32_bf16 %[c4], v[176:179], v[100:103], %[c4]\n" \
  "ds_read_b128 v[172:175], %[vw] offset:3072\n" \
  "s_waitcnt lgkmcnt(8)\n" \
  "v_mfma_f32_16x16x32_bf16 %[c5], v[180:183], v[100:103], %[c5]\n" \
  "s_waitcnt vmcnt(19)\n" \
  "ds_read_b128 v[176:179], %[vw] offset:4096\n" \
  "s_waitcnt lgkmcnt(8)\n" \
  "v_mfma_f32_16x16x32_bf16 %[c0], v[144:147], v[104:107], %[c0]\n" \
  "ds_read_b128 v[180:183], %[vw] offset:5120\n" \
  "s_waitcnt lgkmcnt(8)\n" \
  "v_mfma_f32_16x16x32_bf16 %[c1], v[148:151], v[104:107], %[c1]\n" \
  "ds_read_b128 v[144:147], %[vw] offset:6144\n" \
  "s_waitcnt lgkmcnt(8)\n" \
  "v_mfma_f32_16x16x32_bf16 %[c2], v[152:155], v[104:107], %[c2]\n" \
  "ds_read_b128 v[148:151], %[vw] offset:7168\n" \
  "s_waitcnt lgkmcnt(8)\n" \
  "v_mfma_f32_16x16x32_bf16 %[c3], v[156:159], v[104:107], %[c3]\n" \
  "ds_read_b128 v[152:155], %[vw] offset:8192\n" \
  "s_waitcnt lgkmcnt(8)\n" \
  "v_mfma_f32_16x16x32_bf16 %[c4], v[160:163], v[104:107], %[c4]\n" \
  "ds_read_b128 v[156:159], %[vw] offset:9216\n" \
  "s_waitcnt lgkmcnt(8)\n" \
  "v_mfma_f32_16x16x32_bf16 %[c5], v[164:167], v[104:107], %[c5]\n" \
  "s_waitcnt vmcnt(18)\n" \
  "ds_read_b128 v[160:163], %[vw] offset:10240\n" \
  "s_waitcnt lgkmcnt(8)\n" \
  "v_mfma_f32_16x16x32_bf16 %[c0], v[168:171], v[108:111], %[c0]\n" \
  "ds_read_b128 v[164:167], %[vw] offset:11264\n" \
  "s_waitcnt lgkmcnt(8)\n" \
  "v_mfma_f32_16x16x32_bf16 %[c1], v[172:175], v[108:111], %[c1]\n" \
  "ds_read_b128 v[168:171], %[vw] offset:12288\n" \
  "s_waitcnt lgkmcnt(8)\n" \
  "v_mfma_f32_16x16x32_bf16 %[c2], v[176:179], v[108:111], %[c2]\n" \
  "ds_read_b128 v[172:175], %[vw] offset:13312\n" \
  "s_waitcnt lgkmcnt(8)\n" \
  "v_mfma_f32_16x16x32_bf16 %[c3], v[180:183], v[108:111], %[c3]\n" \
  "ds_read_b128 v[176:179], %[vw] offset:14336\n" \
  "s_waitcnt lgkmcnt(8)\n" \
  "v_mfma_f32_16x16x32_bf16 %[c4], v[144:147], v[108:111], %[c4]\n" \
  "ds_read_b128 v[180:183], %[vw] offset:15360\n" \
  "s_waitcnt lgkmcnt(8)\n" \
  "v_mfma_f32_16x16x32_bf16 %[c5], v[148:151], v[108:111], %[c5]\n" \
  "s_waitcnt vmcnt(17)\n" \
  "ds_read_b128 v[144:147], %[vw] offset:16384\n" \
  "s_waitcnt lgkmcnt(8)\n" \
  "v_mfma_f32_16x16x32_bf16 %[c0], v[152:155], v[112:115], %[c0]\n" \
  "ds_read_b128 v[148:151], %[vw] offset:17408\n" \
  "s_waitcnt lgkmcnt(8)\n" \
  "v_mfma_f32_16x16x32_bf16 %[c1], v[156:159], v[112:115], %[c1]\n" \
  "ds_read_b128 v[152:155], %[vw] offset:18432\n" \
  "s_waitcnt lgkmcnt(8)\n" \
  "v_mfma_f32_16x16x32_bf16 %[c2], v[160:163], v[112:115], %[c2]\n" \
  "ds_read_b128 v[156:159], %[vw] offset:19456\n" \
  "s_waitcnt lgkmcnt(8)\n" \
  "v_mfma_f32_16x16x32_bf16 %[c3], v[164:167], v[112:115], %[c3]\n" \
  "ds_read_b128 v[160:163], %[vw] offset:20480\n" \
  "s_waitcnt lgkmcnt(8)\n" \
  "v_mfma_f32_16x16x32_bf16 %[c4], v[168:171], v[112:115], %[c4]\n" \
  "ds_read_b128 v[164:167], %[vw] offset:21504\n" \
  "s_waitcnt lgkmcnt(8)\n" \
  "v_mfma_f32_16x16x32_bf16 %[c5], v[172:175], v[112:115], %[c5]\n" \
  "s_waitcnt vmcnt(16)\n" \
  "ds_read_b128 v[168:171], %[vw] offset:22528\n" \
  "s_waitcnt lgkmcnt(8)\n" \
  "v_mfma_f32_16x16x32_bf16 %[c0], v[176:179], v[116:119], %[c0]\n" \
  "ds_read_b128 v[172:175], %[vw] offset:23552\n" \
  "s_waitcnt lgkmcnt(8)\n" \
  "v_mfma_f32_16x16x32_bf16 %[c1], v[180:183], v[116:119], %[c1]\n" \
  "ds_read_b128 v[176:179], %[vw] offset:24576\n" \
  "s_waitcnt lgkmcnt(8)\n" \
  "v_mfma_f32_16x16x32_bf16 %[c2], v[144:147], v[116:119], %[c2]\n" \
  "ds_read_b128 v[180:183], %[vw] offset:25600\n" \
  "s_waitcnt lgkmcnt(8)\n" \
  "v_mfma_f32_16x16x32_bf16 %[c3], v[148:151], v[116:119], %[c3]\n" \
  "ds_read_b128 v[144:147], %[vw] offset:26624\n" \
  "s_waitcnt lgkmcnt(8)\n" \
  "v_mfma_f32_16x16x32_bf16 %[c4], v[152:155], v[116:119], %[c4]\n" \
  "ds_read_b128 v[148:151], %[vw] offset:27648\n" \
  "s_waitcnt lgkmcnt(8)\n" \
  "v_mfma_f32_16x16x32_bf16 %[c5], v[156:159], v[116:119], %[c5]\n" \
  "s_waitcnt vmcnt(15)\n" \
  "ds_read_b128 v[152:155], %[vw] offset:28672\n" \
  "s_waitcnt lgkmcnt(8)\n" \
  "v_mfma_f32_16x16x32_bf16 %[c0], v[160:163], v[120:123], %[c0]\n" \
  "ds_read_b128 v[156:159], %[vw] offset:29696\n" \
  "s_waitcnt lgkmcnt(8)\n" \
  "v_mfma_f32_16x16x32_bf16 %[c1], v[164:167], v[120:123], %[c1]\n" \
  "ds_read_b128 v[160:163], %[vw] offset:30720\n" \
  "s_waitcnt lgkmcnt(8)\n" \
  "v_mfma_f32_16x16x32_bf16 %[c2], v[168:171], v[120:123], %[c2]\n" \
  "ds_read_b128 v[164:167], %[vw] offset:31744\n" \
  "s_waitcnt lgkmcnt(8)\n" \
  "v_mfma_f32_16x16x32_bf16 %[c3], v[172:175], v[120:123], %[c3]\n" \
  "ds_read_b128 v[168:171], %[vw] offset:32768\n" \
  "s_waitcnt lgkmcnt(8)\n" \
  "v_mfma_f32_16x16x32_bf16 %[c4], v[176:179], v[120:123], %[c4]\n" \
  "ds_read_b128 v[172:175], %[vw] offset:33792\n" \
  "s_waitcnt lgkmcnt(8)\n" \
  "v_mfma_f32_16x16x32_bf16 %[c5], v[180:183], v[120:123], %[c5]\n" \
  "s_waitcnt vmcnt(14)\n" \
  "ds_read_b128 v[176:179], %[vw] offset:34816\n" \
  "s_waitcnt lgkmcnt(8)\n" \
  "v_mfma_f32_16x16x32_bf16 %[c0], v[144:147], v[124:127], %[c0]\n" \
  "ds_read_b128 v[180:183], %[vw] offset:35840\n" \
  "s_waitcnt lgkmcnt(8)\n" \
  "v_mfma_f32_16x16x32_bf16 %[c1], v[148:151], v[124:127], %[c1]\n" \
  "ds_read_b128 v[144:147], %[vw] offset:36864\n" \
  "s_waitcnt lgkmcnt(8)\n" \
  "v_mfma_f32_16x16x32_bf16 %[c2], v[152:155], v[124:127], %[c2]\n" \
  "ds_read_b128 v[148:151], %[vw] offset:37888\n" \
  "s_waitcnt lgkmcnt(8)\n" \
  "v_mfma_f32_16x16x32_bf16 %[c3], v[156:159], v[124:127], %[c3]\n" \
  "ds_read_b128 v[152:155], %[vw] offset:38912\n" \
  "s_waitcnt lgkmcnt(8)\n" \
  "v_mfma_f32_16x16x32_bf16 %[c4], v[160:163], v[124:127], %[c4]\n" \
  "ds_read_b128 v[156:159], %[vw] offset:39936\n" \
  "s_waitcnt lgkmcnt(8)\n" \
  "v_mfma_f32_16x16x32_bf16 %[c5], v[164:167], v[124:127], %[c5]\n" \
  "s_waitcnt vmcnt(13)\n" \
  "ds_read_b128 v[160:163], %[vw] offset:40960\n" \
  "s_waitcnt lgkmcnt(8)\n" \
  "v_mfma_f32_16x16x32_bf16 %[c0], v[168:171], v[128:131], %[c0]\n" \
  "ds_read_b128 v[164:167], %[vw] offset:41984\n" \
  "s_waitcnt lgkmcnt(8)\n" \
  "v_mfma_f32_16x16x32_bf16 %[c1], v[172:175], v[128:131], %[c1]\n" \
  "ds_read_b128 v[168:171], %[vw] offset:43008\n" \
  "s_waitcnt lgkmcnt(8)\n" \
  "v_mfma_f32_16x16x32_bf16 %[c2], v[176:179], v[128:131], %[c2]\n" \
  "ds_read_b128 v[172:175], %[vw] offset:44032\n" \
  "s_waitcnt lgkmcnt(8)\n" \
  "v_mfma_f32_16x16x32_bf16 %[c3], v[180:183], v[128:131], %[c3]\n" \
  "ds_read_b128 v[176:179], %[vw] offset:45056\n" \
  "s_waitcnt lgkmcnt(8)\n" \
  "v_mfma_f32_16x16x32_bf16 %[c4], v[144:147], v[128:131], %[c4]\n" \
  "ds_read_b128 v[180:183], %[vw] offset:46080\n" \
  "s_waitcnt lgkmcnt(8)\n" \
  "v_mfma_f32_16x16x32_bf16 %[c5], v[148:151], v[128:131], %[c5]\n" \
  "s_waitcnt vmcnt(12)\n" \
  "ds_read_b128 v[144:147], %[vw] offset:47104\n" \
  "s_waitcnt lgkmcnt(8)\n" \
  "v_mfma_f32_16x16x32_bf16 %[c0], v[152:155], v[132:135], %[c0]\n" \
  "ds_read_b128 v[148:151], %[vw] offset:48128\n" \
  "s_waitcnt lgkmcnt(8)\n" \
  "v_mfma_f32_16x16x32_bf16 %[c1], v[156:159], v[132:135], %[c1]\n" \
  "ds_read_b128 v[152:155], %[vw] offset:49152\n" \
  "s_waitcnt lgkmcnt(8)\n" \
  "v_mfma_f32_16x16x32_bf16 %[c2], v[160:163], v[132:135], %[c2]\n" \
  "ds_read_b128 v[156:159], %[vw] offset:50176\n" \
  "s_waitcnt lgkmcnt(8)\n" \
  "v_mfma_f32_16x16x32_bf16 %[c3], v[164:167], v[132:135], %[c3]\n" \
  "ds_read_b128 v[160:163], %[vw] offset:51200\n" \
  "s_waitcnt lgkmcnt(8)\n" \
  "v_mfma_f32_16x16x32_bf16 %[c4], v[168:171], v[132:135], %[c4]\n" \
  "ds_read_b128 v[164:167], %[vw] offset:52224\n" \
  "s_waitcnt lgkmcnt(8)\n" \
  "v_mfma_f32_16x16x32_bf16 %[c5], v[172:175], v[132:135], %[c5]\n" \
  "s_waitcnt vmcnt(11)\n" \
  "ds_read_b128 v[168:171], %[vw] offset:53248\n" \
  "s_waitcnt lgkmcnt(8)\n" \
  "v_mfma_f32_16x16x32_bf16 %[c0], v[176:179], v[136:139], %[c0]\n" \
  "ds_read_b128 v[172:175], %[vw] offset:54272\n" \
  "s_waitcnt lgkmcnt(8)\n" \
  "v_mfma_f32_16x16x32_bf16 %[c1], v[180:183], v[136:139], %[c1]\n" \
  "ds_read_b128 v[176:179], %[vw] offset:55296\n" \
  "s_waitcnt lgkmcnt(8)\n" \
  "v_mfma_f32_16x16x32_bf16 %[c2], v[144:147], v[136:139], %[c2]\n" \
  "ds_read_b128 v[180:183], %[vw] offset:56320\n" \
  "s_waitcnt lgkmcnt(8)\n" \
  "v_mfma_f32_16x16x32_bf16 %[c3], v[148:151], v[136:139], %[c3]\n" \
  "s_waitcnt lgkmcnt(7)\n" \
  "v_mfma_f32_16x16x32_bf16 %[c4], v[152:155], v[136:139], %[c4]\n" \
  "s_waitcnt lgkmcnt(6)\n" \
  "v_mfma_f32_16x16x32_bf16 %[c5], v[156:159], v[136:139], %[c5]\n" \
  "s_waitcnt vmcnt(10)\n" \
  "s_waitcnt lgkmcnt(5)\n" \
  "v_mfma_f32_16x16x32_bf16 %[c0], v[160:163], v[140:143], %[c0]\n" \
  "s_waitcnt lgkmcnt(4)\n" \
  "v_mfma_f32_16x16x32_bf16 %[c1], v[164:167], v[140:143], %[c1]\n" \
  "s_waitcnt lgkmcnt(3)\n" \
  "v_mfma_f32_16x16x32_bf16 %[c2], v[168:171], v[140:143], %[c2]\n" \
  "s_waitcnt lgkmcnt(2)\n" \
  "v_mfma_f32_16x16x32_bf16 %[c3], v[172:175], v[140:143], %[c3]\n" \
  "s_waitcnt lgkmcnt(1)\n" \
  "v_mfma_f32_16x16x32_bf16 %[c4], v[176:179], v[140:143], %[c4]\n" \
  "s_waitcnt lgkmcnt(0)\n" \
  "v_mfma_f32_16x16x32_bf16 %[c5], v[180:183], v[140:143], %[c5]\n" \
  "s_waitcnt vmcnt(6)\n" \
  "s_nop 7\n" \
  ""
#define P6_CLOB "v64", "v65", "v66", "v67", "v68", "v69", "v70", "v71", "v72", "v73", "v74", "v75", "v76", "v77", "v78", "v79", "v80", "v81", "v82", "v83", "v84", "v85", "v86", "v87", "v88", "v89", "v90", "v91", "v92", "v93", "v94", "v95", "v96", "v97", "v98", "v99", "v100", "v101", "v102", "v103", "v104", "v105", "v106", "v107", "v108", "v109", "v110", "v111", "v112", "v113", "v114", "v115", "v116", "v117", "v118", "v119", "v120", "v121", "v122", "v123", "v124", "v125", "v126", "v127", "v128", "v129", "v130", "v131", "v132", "v133", "v134", "v135", "v136", "v137", "v138", "v139", "v140", "v141", "v142", "v143", "v144", "v145", "v146", "v147", "v148", "v149", "v150", "v151", "v152", "v153", "v154", "v155", "v156", "v157", "v158", "v159", "v160", "v161", "v162", "v163", "v164", "v165", "v166", "v167", "v168", "v169", "v170", "v171", "v172", "v173", "v174", "v175", "v176", "v177", "v178", "v179", "v180", "v181", "v182", "v183"
#define P7_ASM \
  "s_nop 4\n" \
  "buffer_load_dwordx4 v[64:67], %[va0], %[rs], %[sa] offen offset:0 sc1\n" \
  "buffer_load_dwordx4 v[68:71], %[va0], %[rs], %[sa] offen offset:1024 sc1\n" \
  "buffer_load_dwordx4 v[72:75], %[va0], %[rs], %[sa] offen offset:2048 sc1\n" \
  "buffer_load_dwordx4 v[76:79], %[va0], %[rs], %[sa] offen offset:3072 sc1\n" \
  "s_add_u32 %[sa], %[sa], 4096\n" \
  "buffer_load_dwordx4 v[80:83], %[va0], %[rs], %[sa] offen offset:0 sc1\n" \
  "buffer_load_dwordx4 v[84:87], %[va0], %[rs], %[sa] offen offset:1024 sc1\n" \
  "buffer_load_dwordx4 v[88:91], %[va0], %[rs], %[sa] offen offset:2048 sc1\n" \
  "buffer_load_dwordx4 v[92:95], %[va0], %[rs], %[sa] offen offset:3072 sc1\n" \
  "s_add_u32 %[sa], %[sa], 4096\n" \
  "buffer_load_dwordx4 v[96:99], %[va0], %[rs], %[sa] offen offset:0 sc1\n" \
  "buffer_load_dwordx4 v[100:103], %[va0], %[rs], %[sa] offen offset:1024 sc1\n" \
  "buffer_load_dwordx4 v[104:107], %[va0], %[rs], %[sa] offen offset:2048 sc1\n" \
  "buffer_load_dwordx4 v[108:111], %[va0], %[rs], %[sa] offen offset:3072 sc1\n" \
  "s_add_u32 %[sa], %[sa], 4096\n" \
  "buffer_load_dwordx4 v[112:115], %[va0], %[rs], %[sa] offen offset:0 sc1\n" \
  "buffer_load_dwordx4 v[116:119], %[va0], %[rs], %[sa] offen offset:1024 sc1\n" \
  "buffer_load_dwordx4 v[120:123], %[va0], %[rs], %[sa] offen offset:2048 sc1\n" \
  "buffer_load_dwordx4 v[124:127], %[va0], %[rs], %[sa] offen offset:3072 sc1\n" \
  "s_add_u32 %[sa], %[sa], 4096\n" \
  "buffer_load_dwordx4 v[128:131], %[va0], %[rs], %[sa] offen offset:0 sc1\n" \
  "buffer_load_dwordx4 v[132:135], %[va0], %[rs], %[sa] offen offset:1024 sc1\n" \
  "buffer_load_dwordx4 v[136:139], %[va0], %[rs], %[sa] offen offset:2048 sc1\n" \
  "buffer_load_dwordx4 v[140:143], %[va0], %[rs], %[sa] offen offset:3072 sc1\n" \
  "s_add_u32 %[sa], %[sa], 4096\n" \
  "buffer_load_dwordx4 v[144:147], %[va0], %[rs], %[sa] offen offset:0 sc1\n" \
  "buffer_load_dwordx4 v[148:151], %[va0], %[rs], %[sa] offen offset:1024 sc1\n" \
  "buffer_load_dwordx4 v[152:155], %[va0], %[rs], %[sa] offen offset:2048 sc1\n" \
  "buffer_load_dwordx4 v[156:159], %[va0], %[rs], %[sa] offen offset:3072 sc1\n" \
  "v_add_u32 %[vw], 98304, %[vw]\n" \
  "ds_read_b128 v[160:163], %[vw] offset:24576\n" \
  "ds_read_b128 v[164:167], %[vw] offset:25600\n" \
  "ds_read_b128 v[168:171], %[vw] offset:26624\n" \
  "ds_read_b128 v[172:175], %[vw] offset:27648\n" \
  "ds_read_b128 v[176:179], %[vw] offset:28672\n" \
  "ds_read_b128 v[180:183], %[vw] offset:34816\n" \
  "ds_read_b128 v[184:187], %[vw] offset:35840\n" \
  "ds_read_b128 v[188:191], %[vw] offset:36864\n" \
  "s_waitcnt vmcnt(23)\n" \
  "ds_read_b128 v[192:195], %[vw] offset:37888\n" \
  "s_waitcnt lgkmcnt(8)\n" \
  "v_mfma_f32_16x16x32_bf16 %[c0], v[160:163], v[64:67], 0\n" \
  "ds_read_b128 v[196:199], %[vw] offset:38912\n" \
  "s_waitcnt lgkmcnt(8)\n" \
  "v_mfma_f32_16x16x32_bf16 %[c1], v[164:167], v[64:67], 0\n" \
  "ds_read_b128 v[160:163], %[vw] offset:45056\n" \
  "s_waitcnt lgkmcnt(8)\n" \
  "v_mfma_f32_16x16x32_bf16 %[c2], v[168:171], v[64:67], 0\n" \
  "ds_read_b128 v[164:167], %[vw] offset:46080\n" \
  "s_waitcnt lgkmcnt(8)\n" \
  "v_mfma_f32_16x16x32_bf16 %[c3], v[172:175], v[64:67], 0\n" \
  "ds_read_b128 v[168:171], %[vw] offset:47104\n" \
  "s_waitcnt lgkmcnt(8)\n" \
  "v_mfma_f32_16x16x32_bf16 %[c4], v[176:179], v[64:67], 0\n" \
  "s_waitcnt vmcnt(22)\n" \
  "ds_read_b128 v[172:175], %[vw] offset:48128\n" \
  "s_waitcnt lgkmcnt(8)\n" \
  "v_mfma_f32_16x16x32_bf16 %[c0], v[180:183], v[68:71], %[c0]\n" \
  "ds_read_b128 v[176:179], %[vw] offset:49152\n" \
  "s_waitcnt lgkmcnt(8)\n" \
  "v_mfma_f32_16x16x32_bf16 %[c1], v[184:187], v[68:71], %[c1]\n" \
  "v_add_u32 %[vw], 4294868992, %[vw]\n" \
  "ds_read_b128 v[180:183], %[vw] offset:0\n" \
  "s_waitcnt lgkmcnt(8)\n" \
  "v_mfma_f32_16x16x32_bf16 %[c2], v[188:191], v[68:71], %[c2]\n" \
  "ds_read_b128 v[184:187], %[vw] offset:1024\n" \
  "s_waitcnt lgkmcnt(8)\n" \
  "v_mfma_f32_16x16x32_bf16 %[c3], v[192:195], v[68:71], %[c3]\n" \
  "ds_read_b128 v[188:191], %[vw] offset:2048\n" \
  "s_waitcnt lgkmcnt(8)\n" \
  "v_mfma_f32_16x16x32_bf16 %[c4], v[196:199], v[68:71], %[c4]\n" \
  "s_waitcnt vmcnt(21)\n" \
  "ds_read_b128 v[192:195], %[vw] offset:3072\n" \
  "s_waitcnt lgkmcnt(8)\n" \
  "v_mfma_f32_16x16x32_bf16 %[c0], v[160:163], v[72:75], %[c0]\n" \
  "ds_read_b128 v[196:199], %[vw] offset:4096\n" \
  "s_waitcnt lgkmcnt(8)\n" \
  "v_mfma_f32_16x16x32_bf16 %[c1], v[164:167], v[72:75], %[c1]\n" \
  "ds_read_b128 v[160:163], %[vw] offset:10240\n" \
  "s_waitcnt lgkmcnt(8)\n" \
  "v_mfma_f32_16x16x32_bf16 %[c2], v[168:171], v[72:75], %[c2]\n" \
  "ds_read_b128 v[164:167], %[vw] offset:11264\n" \
  "s_waitcnt lgkmcnt(8)\n" \
  "v_mfma_f32_16x16x32_bf16 %[c3], v[172:175], v[72:75], %[c3]\n" \
  "ds_read_b128 v[168:171], %[vw] offset:12288\n" \
  "s_waitcnt lgkmcnt(8)\n" \
  "v_mfma_f32_16x16x32_bf16 %[c4], v[176:179], v[72:75], %[c4]\n" \
  "s_waitcnt vmcnt(20)\n" \
  "ds_read_b128 v[172:175], %[vw] offset:13312\n" \
  "s_waitcnt lgkmcnt(8)\n" \
  "v_mfma_f32_16x16x32_bf16 %[c0], v[180:183], v[76:79], %[c0]\n" \
  "ds_read_b128 v[176:179], %[vw] offset:14336\n" \
  "s_waitcnt lgkmcnt(8)\n" \
  "v_mfma_f32_16x16x32_bf16 %[c1], v[184:187], v[76:79], %[c1]\n" \
  "ds_read_b128 v[180:183], %[vw] offset:20480\n" \
  "s_waitcnt lgkmcnt(8)\n" \
  "v_mfma_f32_16x16x32_bf16 %[c2], v[188:191], v[76:79], %[c2]\n" \
  "ds_read_b128 v[184:187], %[vw] offset:21504\n" \
  "s_waitcnt lgkmcnt(8)\n" \
  "v_mfma_f32_16x16x32_bf16 %[c3], v[192:195], v[76:79], %[c3]\n" \
  "ds_read_b128 v[188:191], %[vw] offset:22528\n" \
  "s_waitcnt lgkmcnt(8)\n" \
  "v_mfma_f32_16x16x32_bf16 %[c4], v[196:199], v[76:79], %[c4]\n" \
  "s_barrier\n" \
  "s_mov_b32 m0, %[dm0]\n" \
  "s_nop 0\n" \
  "buffer_load_dwordx4 %[l16], %[rs], %[ds0] offen offset:0 sc1 lds\n" \
  "buffer_load_dwordx4 %[l16], %[rs], %[ds0] offen offset:1024 sc1 lds\n" \
  "buffer_load_dwordx4 %[l16], %[rs], %[ds0] offen offset:2048 sc1 lds\n" \
  "buffer_load_dwordx4 %[l16], %[rs], %[ds0] offen offset:3072 sc1 lds\n" \
  "s_add_u32 %[dm0], %[dm0], 4096\n" \
  "s_add_u32 %[ds0], %[ds0], 4096\n" \
  "s_mov_b32 m0, %[dm0]\n" \
  "s_nop 0\n" \
  "buffer_load_dwordx4 %[l16], %[rs], %[ds0] offen offset:0 sc1 lds\n" \
  "s_waitcnt vmcnt(24)\n" \
  "ds_read_b128 v[192:195], %[vw] offset:23552\n" \
  "s_waitcnt lgkmcnt(8)\n" \
  "v_mfma_f32_16x16x32_bf16 %[c0], v[160:163], v[80:83], %[c0]\n" \
  "ds_read_b128 v[196:199], %[vw] offset:24576\n" \
  "s_waitcnt lgkmcnt(8)\n" \
  "v_mfma_f32_16x16x32_bf16 %[c1], v[164:167], v[80:83], %[c1]\n" \
  "ds_read_b128 v[160:163], %[vw] offset:30720\n" \
  "s_waitcnt lgkmcnt(8)\n" \
  "v_mfma_f32_16x16x32_bf16 %[c2], v[168:171], v[80:83], %[c2]\n" \
  "ds_read_b128 v[164:167], %[vw] offset:31744\n" \
  "s_waitcnt lgkmcnt(8)\n" \
  "v_mfma_f32_16x16x32_bf16 %[c3], v[172:175], v[80:83], %[c3]\n" \
  "ds_read_b128 v[168:171], %[vw] offset:32768\n" \
  "s_waitcnt lgkmcnt(8)\n" \
  "v_mfma_f32_16x16x32_bf16 %[c4], v[176:179], v[80:83], %[c4]\n" \
  "s_waitcnt vmcnt(23)\n" \
  "ds_read_b128 v[172:175], %[vw] offset:33792\n" \
  "s_waitcnt lgkmcnt(8)\n" \
  "v_mfma_f32_16x16x32_bf16 %[c0], v[180:183], v[84:87], %[c0]\n" \
  "ds_read_b128 v[176:179], %[vw] offset:34816\n" \
  "s_waitcnt lgkmcnt(8)\n" \
  "v_mfma_f32_16x16x32_bf16 %[c1], v[184:187], v[84:87], %[c1]\n" \
  "ds_read_b128 v[180:183], %[vw] offset:40960\n" \
  "s_waitcnt lgkmcnt(8)\n" \
  "v_mfma_f32_16x16x32_bf16 %[c2], v[188:191], v[84:87], %[c2]\n" \
  "ds_read_b128 v[184:187], %[vw] offset:41984\n" \
  "s_waitcnt lgkmcnt(8)\n" \
  "v_mfma_f32_16x16x32_bf16 %[c3], v[192:195], v[84:87], %[c3]\n" \
  "ds_read_b128 v[188:191], %[vw] offset:43008\n" \
  "s_waitcnt lgkmcnt(8)\n" \
  "v_mfma_f32_16x16x32_bf16 %[c4], v[196:199], v[84:87], %[c4]\n" \
  "s_waitcnt vmcnt(22)\n" \
  "ds_read_b128 v[192:195], %[vw] offset:44032\n" \
  "s_waitcnt lgkmcnt(8)\n" \
  "v_mfma_f32_16x16x32_bf16 %[c0], v[160:163], v[88:91], %[c0]\n" \
  "ds_read_b128 v[196:199], %[vw] offset:45056\n" \
  "s_waitcnt lgkmcnt(8)\n" \
  "v_mfma_f32_16x16x32_bf16 %[c1], v[164:167], v[88:91], %[c1]\n" \
  "ds_read_b128 v[160:163], %[vw] offset:51200\n" \
  "s_waitcnt lgkmcnt(8)\n" \
  "v_mfma_f32_16x16x32_bf16 %[c2], v[168:171], v[88:91], %[c2]\n" \
  "ds_read_b128 v[164:167], %[vw] offset:52224\n" \
  "s_waitcnt lgkmcnt(8)\n" \
  "v_mfma_f32_16x16x32_bf16 %[c3], v[172:175], v[88:91], %[c3]\n" \
  "ds_read_b128 v[168:171], %[vw] offset:53248\n" \
  "s_waitcnt lgkmcnt(8)\n" \
  "v_mfma_f32_16x16x32_bf16 %[c4], v[176:179], v[88:91], %[c4]\n" \
  "s_waitcnt vmcnt(21)\n" \
  "ds_read_b128 v[172:175], %[vw] offset:54272\n" \
  "s_waitcnt lgkmcnt(8)\n" \
  "v_mfma_f32_16x16x32_bf16 %[c0], v[180:183], v[92:95], %[c0]\n" \
  "ds_read_b128 v[176:179], %[vw] offset:55296\n" \
  "s_waitcnt lgkmcnt(8)\n" \
  "v_mfma_f32_16x16x32_bf16 %[c1], v[184:187], v[92:95], %[c1]\n" \
  "ds_read_b128 v[180:183], %[vw] offset:61440\n" \
  "s_waitcnt lgkmcnt(8)\n" \
  "v_mfma_f32_16x16x32_bf16 %[c2], v[188:191], v[92:95], %[c2]\n" \
  "ds_read_b128 v[184:187], %[vw] offset:62464\n" \
  "s_waitcnt lgkmcnt(8)\n" \
  "v_mfma_f32_16x16x32_bf16 %[c3], v[192:195], v[92:95], %[c3]\n" \
  "ds_read_b128 v[188:191], %[vw] offset:63488\n" \
  "s_waitcnt lgkmcnt(8)\n" \
  "v_mfma_f32_16x16x32_bf16 %[c4], v[196:199], v[92:95], %[c4]\n" \
  "s_waitcnt vmcnt(20)\n" \
  "ds_read_b128 v[192:195], %[vw] offset:64512\n" \
  "s_waitcnt lgkmcnt(8)\n" \
  "v_mfma_f32_16x16x32_bf16 %[c0], v[160:163], v[96:99], %[c0]\n" \
  "v_add_u32 %[vw], 65536, %[vw]\n" \
  "ds_read_b128 v[196:199], %[vw] offset:0\n" \
  "s_waitcnt lgkmcnt(8)\n" \
  "v_mfma_f32_16x16x32_bf16 %[c1], v[164:167], v[96:99], %[c1]\n" \
  "ds_read_b128 v[160:163], %[vw] offset:6144\n" \
  "s_waitcnt lgkmcnt(8)\n" \
  "v_mfma_f32_16x16x32_bf16 %[c2], v[168:171], v[96:99], %[c2]\n" \
  "ds_read_b128 v[164:167], %[vw] offset:7168\n" \
  "s_waitcnt lgkmcnt(8)\n" \
  "v_mfma_f32_16x16x32_bf16 %[c3], v[172:175], v[96:99], %[c3]\n" \
  "ds_read_b128 v[168:171], %[vw] offset:8192\n" \
  "s_waitcnt lgkmcnt(8)\n" \
  "v_mfma_f32_16x16x32_bf16 %[c4], v[176:179], v[96:99], %[c4]\n" \
  "s_waitcnt vmcnt(19)\n" \
  "ds_read_b128 v[172:175], %[vw] offset:9216\n" \
  "s_waitcnt lgkmcnt(8)\n" \
  "v_mfma_f32_16x16x32_bf16 %[c0], v[180:183], v[100:103], %[c0]\n" \
  "ds_read_b128 v[176:179], %[vw] offset:10240\n" \
  "s_waitcnt lgkmcnt(8)\n" \
  "v_mfma_f32_16x16x32_bf16 %[c1], v[184:187], v[100:103], %[c1]\n" \
  "ds_read_b128 v[180:183], %[vw] offset:16384\n" \
  "s_waitcnt lgkmcnt(8)\n" \
  "v_mfma_f32_16x16x32_bf16 %[c2], v[188:191], v[100:103], %[c2]\n" \
  "ds_read_b128 v[184:187], %[vw] offset:17408\n" \
  "s_waitcnt lgkmcnt(8)\n" \
  "v_mfma_f32_16x16x32_bf16 %[c3], v[192:195], v[100:103], %[c3]\n" \
  "ds_read_b128 v[188:191], %[vw] offset:18432\n" \
  "s_waitcnt lgkmcnt(8)\n" \
  "v_mfma_f32_16x16x32_bf16 %[c4], v[196:199], v[100:103], %[c4]\n" \
  "s_barrier\n" \
  "s_mov_b32 m0, %[dm1]\n" \
  "s_nop 0\n" \
  "buffer_load_dwordx4 %[l16], %[rs], %[ds1] offen offset:0 sc1 lds\n" \
  "buffer_load_dwordx4 %[l16], %[rs], %[ds1] offen offset:1024 sc1 lds\n" \
  "buffer_load_dwordx4 %[l16], %[rs], %[ds1] offen offset:2048 sc1 lds\n" \
  "buffer_load_dwordx4 %[l16], %[rs], %[ds1] offen offset:3072 sc1 lds\n" \
  "s_add_u32 %[dm1], %[dm1], 4096\n" \
  "s_add_u32 %[ds1], %[ds1], 4096\n" \
  "s_mov_b32 m0, %[dm1]\n" \
  "s_nop 0\n" \
  "buffer_load_dwordx4 %[l16], %[rs], %[ds1] offen offset:0 sc1 lds\n" \
  "buffer_load_dwordx4 %[l16], %[rs], %[ds1] offen offset:1024 sc1 lds\n" \
  "buffer_load_dwordx4 %[l16], %[rs], %[ds1] offen offset:2048 sc1 lds\n" \
  "s_waitcnt vmcnt(25)\n" \
  "ds_read_b128 v[192:195], %[vw] offset:19456\n" \
  "s_waitcnt lgkmcnt(8)\n" \
  "v_mfma_f32_16x16x32_bf16 %[c0], v[160:163], v[104:107], %[c0]\n" \
  "ds_read_b128 v[196:199], %[vw] offset:20480\n" \
  "s_waitcnt lgkmcnt(8)\n" \
  "v_mfma_f32_16x16x32_bf16 %[c1], v[164:167], v[104:107], %[c1]\n" \
  "ds_read_b128 v[160:163], %[vw] offset:26624\n" \
  "s_waitcnt lgkmcnt(8)\n" \
  "v_mfma_f32_16x16x32_bf16 %[c2], v[168:171], v[104:107], %[c2]\n" \
  "ds_read_b128 v[164:167], %[vw] offset:27648\n" \
  "s_waitcnt lgkmcnt(8)\n" \
  "v_mfma_f32_16x16x32_bf16 %[c3], v[172:175], v[104:107], %[c3]\n" \
  "ds_read_b128 v[168:171], %[vw] offset:28672\n" \
  "s_waitcnt lgkmcnt(8)\n" \
  "v_mfma_f32_16x16x32_bf16 %[c4], v[176:179], v[104:107], %[c4]\n" \
  "s_waitcnt vmcnt(24)\n" \
  "ds_read_b128 v[172:175], %[vw] offset:29696\n" \
  "s_waitcnt lgkmcnt(8)\n" \
  "v_mfma_f32_16x16x32_bf16 %[c0], v[180:183], v[108:111], %[c0]\n" \
  "ds_read_b128 v[176:179], %[vw] offset:30720\n" \
  "s_waitcnt lgkmcnt(8)\n" \
  "v_mfma_f32_16x16x32_bf16 %[c1], v[184:187], v[108:111], %[c1]\n" \
  "ds_read_b128 v[180:183], %[vw] offset:36864\n" \
  "s_waitcnt lgkmcnt(8)\n" \
  "v_mfma_f32_16x16x32_bf16 %[c2], v[188:191], v[108:111], %[c2]\n" \
  "ds_read_b128 v[184:187], %[vw] offset:37888\n" \
  "s_waitcnt lgkmcnt(8)\n" \
  "v_mfma_f32_16x16x32_bf16 %[c3], v[192:195], v[108:111], %[c3]\n" \
  "ds_read_b128 v[188:191], %[vw] offset:38912\n" \
  "s_waitcnt lgkmcnt(8)\n" \
  "v_mfma_f32_16x16x32_bf16 %[c4], v[196:199], v[108:111], %[c4]\n" \
  "s_waitcnt vmcnt(23)\n" \
  "ds_read_b128 v[192:195], %[vw] offset:39936\n" \
  "s_waitcnt lgkmcnt(8)\n" \
  "v_mfma_f32_16x16x32_bf16 %[c0], v[160:163], v[112:115], %[c0]\n" \
  "ds_read_b128 v[196:199], %[vw] offset:40960\n" \
  "s_waitcnt lgkmcnt(8)\n" \
  "v_mfma_f32_16x16x32_bf16 %[c1], v[164:167], v[112:115], %[c1]\n" \
  "ds_read_b128 v[160:163], %[vw] offset:47104\n" \
  "s_waitcnt lgkmcnt(8)\n" \
  "v_mfma_f32_16x16x32_bf16 %[c2], v[168:171], v[112:115], %[c2]\n" \
  "ds_read_b128 v[164:167], %[vw] offset:48128\n" \
  "s_waitcnt lgkmcnt(8)\n" \
  "v_mfma_f32_16x16x32_bf16 %[c3], v[172:175], v[112:115], %[c3]\n" \
  "ds_read_b128 v[168:171], %[vw] offset:49152\n" \
  "s_waitcnt lgkmcnt(8)\n" \
  "v_mfma_f32_16x16x32_bf16 %[c4], v[176:179], v[112:115], %[c4]\n" \
  "s_waitcnt vmcnt(22)\n" \
  "ds_read_b128 v[172:175], %[vw] offset:50176\n" \
  "s_waitcnt lgkmcnt(8)\n" \
  "v_mfma_f32_16x16x32_bf16 %[c0], v[180:183], v[116:119], %[c0]\n" \
  "ds_read_b128 v[176:179], %[vw] offset:51200\n" \
  "s_waitcnt lgkmcnt(8)\n" \
  "v_mfma_f32_16x16x32_bf16 %[c1], v[184:187], v[116:119], %[c1]\n" \
  "s_waitcnt vmcnt(7)\n" \
  "s_barrier\n" \
  "ds_read_b128 v[180:183], %[vw] offset:57344\n" \
  "s_waitcnt lgkmcnt(8)\n" \
  "v_mfma_f32_16x16x32_bf16 %[c2], v[188:191], v[116:119], %[c2]\n" \
  "ds_read_b128 v[184:187], %[vw] offset:58368\n" \
  "s_waitcnt lgkmcnt(8)\n" \
  "v_mfma_f32_16x16x32_bf16 %[c3], v[192:195], v[116:119], %[c3]\n" \
  "ds_read_b128 v[188:191], %[vw] offset:59392\n" \
  "s_waitcnt lgkmcnt(8)\n" \
  "v_mfma_f32_16x16x32_bf16 %[c4], v[196:199], v[116:119], %[c4]\n" \
  "s_waitcnt vmcnt(21)\n" \
  "ds_read_b128 v[192:195], %[vw] offset:60416\n" \
  "s_waitcnt lgkmcnt(8)\n" \
  "v_mfma_f32_16x16x32_bf16 %[c0], v[160:163], v[120:123], %[c0]\n" \
  "ds_read_b128 v[196:199], %[vw] offset:61440\n" \
  "s_waitcnt lgkmcnt(8)\n" \
  "v_mfma_f32_16x16x32_bf16 %[c1], v[164:167], v[120:123], %[c1]\n" \
  "v_add_u32 %[vw], 65536, %[vw]\n" \
  "ds_read_b128 v[160:163], %[vw] offset:2048\n" \
  "s_waitcnt lgkmcnt(8)\n" \
  "v_mfma_f32_16x16x32_bf16 %[c2], v[168:171], v[120:123], %[c2]\n" \
  "ds_read_b128 v[164:167], %[vw] offset:3072\n" \
  "s_waitcnt lgkmcnt(8)\n" \
  "v_mfma_f32_16x16x32_bf16 %[c3], v[172:175], v[120:123], %[c3]\n" \
  "ds_read_b128 v[168:171], %[vw] offset:4096\n" \
  "s_waitcnt lgkmcnt(8)\n" \
  "v_mfma_f32_16x16x32_bf16 %[c4], v[176:179], v[120:123], %[c4]\n" \
  "s_waitcnt vmcnt(20)\n" \
  "ds_read_b128 v[172:175], %[vw] offset:5120\n" \
  "s_waitcnt lgkmcnt(8)\n" \
  "v_mfma_f32_16x16x32_bf16 %[c0], v[180:183], v[124:127], %[c0]\n" \
  "ds_read_b128 v[176:179], %[vw] offset:6144\n" \
  "s_waitcnt lgkmcnt(8)\n" \
  "v_mfma_f32_16x16x32_bf16 %[c1], v[184:187], v[124:127], %[c1]\n" \
  "ds_read_b128 v[180:183], %[vw] offset:12288\n" \
  "s_waitcnt lgkmcnt(8)\n" \
  "v_mfma_f32_16x16x32_bf16 %[c2], v[188:191], v[124:127], %[c2]\n" \
  "ds_read_b128 v[184:187], %[vw] offset:13312\n" \
  "s_waitcnt lgkmcnt(8)\n" \
  "v_mfma_f32_16x16x32_bf16 %[c3], v[192:195], v[124:127], %[c3]\n" \
  "ds_read_b128 v[188:191], %[vw] offset:14336\n" \
  "s_waitcnt lgkmcnt(8)\n" \
  "v_mfma_f32_16x16x32_bf16 %[c4], v[196:199], v[124:127], %[c4]\n" \
  "s_waitcnt vmcnt(19)\n" \
  "ds_read_b128 v[192:195], %[vw] offset:15360\n" \
  "s_waitcnt lgkmcnt(8)\n" \
  "v_mfma_f32_16x16x32_bf16 %[c0], v[160:163], v[128:131], %[c0]\n" \
  "ds_read_b128 v[196:199], %[vw] offset:16384\n" \
  "s_waitcnt lgkmcnt(8)\n" \
  "v_mfma_f32_16x16x32_bf16 %[c1], v[164:167], v[128:131], %[c1]\n" \
  "v_add_u32 %[vw], 4294836224, %[vw]\n" \
  "ds_read_b128 v[160:163], %[vw] offset:0\n" \
  "s_waitcnt lgkmcnt(8)\n" \
  "v_mfma_f32_16x16x32_bf16 %[c2], v[168:171], v[128:131], %[c2]\n" \
  "ds_read_b128 v[164:167], %[vw] offset:1024\n" \
  "s_waitcnt lgkmcnt(8)\n" \
  "v_mfma_f32_16x16x32_bf16 %[c3], v[172:175], v[128:131], %[c3]\n" \
  "ds_read_b128 v[168:171], %[vw] offset:2048\n" \
  "s_waitcnt lgkmcnt(8)\n" \
  "v_mfma_f32_16x16x32_bf16 %[c4], v[176:179], v[128:131], %[c4]\n" \
  "s_waitcnt vmcnt(18)\n" \
  "ds_read_b128 v[172:175], %[vw] offset:3072\n" \
  "s_waitcnt lgkmcnt(8)\n" \
  "v_mfma_f32_16x16x32_bf16 %[c0], v[180:183], v[132:135], %[c0]\n" \
  "ds_read_b128 v[176:179], %[vw] offset:4096\n" \
  "s_waitcnt lgkmcnt(8)\n" \
  "v_mfma_f32_16x16x32_bf16 %[c1], v[184:187], v[132:135], %[c1]\n" \
  "s_waitcnt vmcnt(0)\n" \
  "s_barrier\n" \
  "ds_read_b128 v[180:183], %[vw] offset:10240\n" \
  "s_waitcnt lgkmcnt(8)\n" \
  "v_mfma_f32_16x16x32_bf16 %[c2], v[188:191], v[132:135], %[c2]\n" \
  "ds_read_b128 v[184:187], %[vw] offset:11264\n" \
  "s_waitcnt lgkmcnt(8)\n" \
  "v_mfma_f32_16x16x32_bf16 %[c3], v[192:195], v[132:135], %[c3]\n" \
  "ds_read_b128 v[188:191], %[vw] offset:12288\n" \
  "s_waitcnt lgkmcnt(8)\n" \
  "v_mfma_f32_16x16x32_bf16 %[c4], v[196:199], v[132:135], %[c4]\n" \
  "s_waitcnt vmcnt(17)\n" \
  "ds_read_b128 v[192:195], %[vw] offset:13312\n" \
  "s_waitcnt lgkmcnt(8)\n" \
  "v_mfma_f32_16x16x32_bf16 %[c0], v[160:163], v[136:139], %[c0]\n" \
  "ds_read_b128 v[196:199], %[vw] offset:14336\n" \
  "s_waitcnt lgkmcnt(8)\n" \
  "v_mfma_f32_16x16x32_bf16 %[c1], v[164:167], v[136:139], %[c1]\n" \
  "ds_read_b128 v[160:163], %[vw] offset:20480\n" \
  "s_waitcnt lgkmcnt(8)\n" \
  "v_mfma_f32_16x16x32_bf16 %[c2], v[168:171], v[136:139], %[c2]\n" \
  "ds_read_b128 v[164:167], %[vw] offset:21504\n" \
  "s_waitcnt lgkmcnt(8)\n" \
  "v_mfma_f32_16x16x32_bf16 %[c3], v[172:175], v[136:139], %[c3]\n" \
  "ds_read_b128 v[168:171], %[vw] offset:22528\n" \
  "s_waitcnt lgkmcnt(8)\n" \
  "v_mfma_f32_16x16x32_bf16 %[c4], v[176:179], v[136:139], %[c4]\n" \
  "s_waitcnt vmcnt(16)\n" \
  "ds_read_b128 v[172:175], %[vw] offset:23552\n" \
  "s_waitcnt lgkmcnt(8)\n" \
  "v_mfma_f32_16x16x32_bf16 %[c0], v[180:183], v[140:143], %[c0]\n" \
  "ds_read_b128 v[176:179], %[vw] offset:24576\n" \
  "s_waitcnt lgkmcnt(8)\n" \
  "v_mfma_f32_16x16x32_bf16 %[c1], v[184:187], v[140:143], %[c1]\n" \
  "ds_read_b128 v[180:183], %[vw] offset:30720\n" \
  "s_waitcnt lgkmcnt(8)\n" \
  "v_mfma_f32_16x16x32_bf16 %[c2], v[188:191], v[140:143], %[c2]\n" \
  "ds_read_b128 v[184:187], %[vw] offset:31744\n" \
  "s_waitcnt lgkmcnt(8)\n" \
  "v_mfma_f32_16x16x32_bf16 %[c3], v[192:195], v[140:143], %[c3]\n" \
  "ds_read_b128 v[188:191], %[vw] offset:32768\n" \
  "s_waitcnt lgkmcnt(8)\n" \
  "v_mfma_f32_16x16x32_bf16 %[c4], v[196:199], v[140:143], %[c4]\n" \
  "s_waitcnt vmcnt(15)\n" \
  "ds_read_b128 v[192:195], %[vw] offset:33792\n" \
  "s_waitcnt lgkmcnt(8)\n" \
  "v_mfma_f32_16x16x32_bf16 %[c0], v[160:163], v[144:147], %[c0]\n" \
  "ds_read_b128 v[196:199], %[vw] offset:34816\n" \
  "s_waitcnt lgkmcnt(8)\n" \
  "v_mfma_f32_16x16x32_bf16 %[c1], v[164:167], v[144:147], %[c1]\n" \
  "ds_read_b128 v[160:163], %[vw] offset:40960\n" \
  "s_waitcnt lgkmcnt(8)\n" \
  "v_mfma_f32_16x16x32_bf16 %[c2], v[168:171], v[144:147], %[c2]\n" \
  "ds_read_b128 v[164:167], %[vw] offset:41984\n" \
  "s_waitcnt lgkmcnt(8)\n" \
  "v_mfma_f32_16x16x32_bf16 %[c3], v[172:175], v[144:147], %[c3]\n" \
  "ds_read_b128 v[168:171], %[vw] offset:43008\n" \
  "s_waitcnt lgkmcnt(8)\n" \
  "v_mfma_f32_16x16x32_bf16 %[c4], v[176:179], v[144:147], %[c4]\n" \
  "s_waitcnt vmcnt(14)\n" \
  "ds_read_b128 v[172:175], %[vw] offset:44032\n" \
  "s_waitcnt lgkmcnt(8)\n" \
  "v_mfma_f32_16x16x32_bf16 %[c0], v[180:183], v[148:151], %[c0]\n" \
  "ds_read_b128 v[176:179], %[vw] offset:45056\n" \
  "s_waitcnt lgkmcnt(8)\n" \
  "v_mfma_f32_16x16x32_bf16 %[c1], v[184:187], v[148:151], %[c1]\n" \
  "ds_read_b128 v[180:183], %[vw] offset:51200\n" \
  "s_waitcnt lgkmcnt(8)\n" \
  "v_mfma_f32_16x16x32_bf16 %[c2], v[188:191], v[148:151], %[c2]\n" \
  "ds_read_b128 v[184:187], %[vw] offset:52224\n" \
  "s_waitcnt lgkmcnt(8)\n" \
  "v_mfma_f32_16x16x32_bf16 %[c3], v[192:195], v[148:151], %[c3]\n" \
  "ds_read_b128 v[188:191], %[vw] offset:53248\n" \
  "s_waitcnt lgkmcnt(8)\n" \
  "v_mfma_f32_16x16x32_bf16 %[c4], v[196:199], v[148:151], %[c4]\n" \
  "s_waitcnt vmcnt(13)\n" \
  "ds_read_b128 v[192:195], %[vw] offset:54272\n" \
  "s_waitcnt lgkmcnt(8)\n" \
  "v_mfma_f32_16x16x32_bf16 %[c0], v[160:163], v[152:155], %[c0]\n" \
  "ds_read_b128 v[196:199], %[vw] offset:55296\n" \
  "s_waitcnt lgkmcnt(8)\n" \
  "v_mfma_f32_16x16x32_bf16 %[c1], v[164:167], v[152:155], %[c1]\n" \
  "s_waitcnt lgkmcnt(7)\n" \
  "v_mfma_f32_16x16x32_bf16 %[c2], v[168:171], v[152:155], %[c2]\n" \
  "s_waitcnt lgkmcnt(6)\n" \
  "v_mfma_f32_16x16x32_bf16 %[c3], v[172:175], v[152:155], %[c3]\n" \
  "s_waitcnt lgkmcnt(5)\n" \
  "v_mfma_f32_16x16x32_bf16 %[c4], v[176:179], v[152:155], %[c4]\n" \
  "s_waitcnt vmcnt(12)\n" \
  "s_waitcnt lgkmcnt(4)\n" \
  "v_mfma_f32_16x16x32_bf16 %[c0], v[180:183], v[156:159], %[c0]\n" \
  "s_waitcnt lgkmcnt(3)\n" \
  "v_mfma_f32_16x16x32_bf16 %[c1], v[184:187], v[156:159], %[c1]\n" \
  "s_waitcnt lgkmcnt(2)\n" \
  "v_mfma_f32_16x16x32_bf16 %[c2], v[188:191], v[156:159], %[c2]\n" \
  "s_waitcnt lgkmcnt(1)\n" \
  "v_mfma_f32_16x16x32_bf16 %[c3], v[192:195], v[156:159], %[c3]\n" \
  "s_waitcnt lgkmcnt(0)\n" \
  "v_mfma_f32_16x16x32_bf16 %[c4], v[196:199], v[156:159], %[c4]\n" \
  "s_nop 7\n" \
  ""
#define P7_CLOB "v64", "v65", "v66", "v67", "v68", "v69", "v70", "v71", "v72", "v73", "v74", "v75", "v76", "v77", "v78", "v79", "v80", "v81", "v82", "v83", "v84", "v85", "v86", "v87", "v88", "v89", "v90", "v91", "v92", "v93", "v94", "v95", "v96", "v97", "v98", "v99", "v100", "v101", "v102", "v103", "v104", "v105", "v106", "v107", "v108", "v109", "v110", "v111", "v112", "v113", "v114", "v115", "v116", "v117", "v118", "v119", "v120", "v121", "v122", "v123", "v124", "v125", "v126", "v127", "v128", "v129", "v130", "v131", "v132", "v133", "v134", "v135", "v136", "v137", "v138", "v139", "v140", "v141", "v142", "v143", "v144", "v145", "v146", "v147", "v148", "v149", "v150", "v151", "v152", "v153", "v154", "v155", "v156", "v157", "v158", "v159", "v160", "v161", "v162", "v163", "v164", "v165", "v166", "v167", "v168", "v169", "v170", "v171", "v172", "v173", "v174", "v175", "v176", "v177", "v178", "v179", "v180", "v181", "v182", "v183", "v184", "v185", "v186", "v187", "v188", "v189", "v190", "v191", "v192", "v193", "v194", "v195", "v196", "v197", "v198", "v199"

constexpr int S = 2048, D = 640, F = 1536;
constexpr float EPS = 1e-5f;
constexpr unsigned OOB = 0x80000000u;
constexpr int FLG_OFF = 4096;
constexpr int XST_OFF = FLG_OFF + 7 * 256 * 256;
constexpr int HST_OFF = XST_OFF + S * 16;
constexpr int X1ST_OFF = HST_OFF + 128 * 2048;
constexpr int CS1_OFF = X1ST_OFF + 128 * 2048;
constexpr int CS3_OFF = CS1_OFF + 20 * D * 4;
constexpr int Q16_OFF = CS3_OFF + 20 * F * 4;
constexpr int Q8_OFF = Q16_OFF + 32 * 64 * 1024;
constexpr int KV_OFF = Q8_OFF + 32 * 64 * 256;
constexpr int O_OFF = KV_OFF + (32 * 64 + 4) * 2816;
constexpr int H_OFF = O_OFF + S * D * 2;
constexpr int G_OFF = H_OFF + S * D * 2;
constexpr int X1_OFF = G_OFF + S * D * 2;
constexpr int U_OFF = X1_OFF + S * D * 2;
constexpr int WG_OFF = U_OFF + S * F * 2;
constexpr int WG_STR = 548 * 1024;
constexpr int W3_OFF = WG_OFF + 8 * WG_STR;
constexpr int DBG_OFF = W3_OFF + 16 * 120 * 1024;
constexpr int WS_END = DBG_OFF + 256 * 8 * 128;
enum { FK_P1 = 0, FK_R = 1, FK_P2 = 2, FK_P3 = 3, FK_P4 = 4, FK_P5 = 5, FK_P6 = 6 };
constexpr int LDS_BYTES = 163840;
constexpr int RING = 150;
constexpr int MISC = RING * 1024;
constexpr int L_CS1 = MISC, L_CS3 = MISC + 4 * 80 * 4, L_VOTE = L_CS3 + 4 * 96 * 4, L_MAX = L_VOTE + 64;
constexpr unsigned SPIN_CAP = 1u << 17;

struct Params {
  const u16 *x, *wqkv, *wo, *w1, *w2, *w3, *w4;
  u16* out;
  unsigned char* ws;
};

DEV rsrc_t mk_rsrc(const void* p, int bytes) { return __builtin_amdgcn_make_buffer_rsrc((void*)p, 0, bytes, 0x00020000); }
DEV float bflo(unsigned v) { return __uint_as_float(v << 16); }
DEV float bfhi(unsigned v) { return __uint_as_float(v & 0xffff0000u); }
DEV unsigned pk2(float a, float b) {
  bf16x2 r = {(__bf16)a, (__bf16)b};
  return __builtin_bit_cast(unsigned, r);
}
DEV float xsum16(float v) { return v + __shfl_xor(v, 16); }
DEV float quadsum(float v) { v += __shfl_xor(v, 16); return v + __shfl_xor(v, 32); }
DEV float dot1(unsigned v, float c) { return __builtin_amdgcn_fdot2_f32_bf16(__builtin_bit_cast(bf16x2, v), __builtin_bit_cast(bf16x2, 0x3f803f80u), c, false); }
DEV f32x4 mfma16(v4u a, v4u b, f32x4 c) { return __builtin_amdgcn_mfma_f32_16x16x32_bf16(__builtin_bit_cast(bf16x8, a), __builtin_bit_cast(bf16x8, b), c, 0, 0, 0); }
DEV v2u trrd(LAS char* p) { return __builtin_bit_cast(v2u, __builtin_amdgcn_ds_read_tr16_b64_v4i16((LAS s16x4*)p)); }
DEV void st16(rsrc_t rs, unsigned vo, v4u v) { __builtin_amdgcn_raw_buffer_store_b128(v, rs, vo, 0, 16); }
DEV void st8(rsrc_t rs, unsigned vo, v2u v) { __builtin_amdgcn_raw_buffer_store_b64(v, rs, vo, 0, 16); }
DEV v4u swap_pair(v2u a, v2u b) {
  auto r0 = __builtin_amdgcn_permlane16_swap(a.x, b.x, false, false);
  auto r1 = __builtin_amdgcn_permlane16_swap(a.y, b.y, false, false);
  return v4u{r0[0], r1[0], r0[1], r1[1]};
}
DEV v2u pk4(f32x4 v) { return v2u{pk2(v.x, v.y), pk2(v.z, v.w)}; }
DEV f32x4 unpk4(v2u v) { return f32x4{bflo(v.x), bfhi(v.x), bflo(v.y), bfhi(v.y)}; }
DEV float gelu1(float t) {
  const float c0 = -2.f * 0.7978845608028654f * 1.4426950408889634f, c1 = c0 * 0.044715f;
  float e = __builtin_amdgcn_exp2f(t * __builtin_fmaf(t * t, c1, c0));
  return t * __builtin_amdgcn_rcpf(1.f + e);
}
DEV f32x4 gelu4(f32x4 t) { return f32x4{gelu1(t.x), gelu1(t.y), gelu1(t.z), gelu1(t.w)}; }
template <bool DEVSCOPE> DEV void dma1(rsrc_t rs, unsigned vo, unsigned so, unsigned m0v) {
  if (DEVSCOPE) asm volatile("s_mov_b32 m0, %2\n\ts_nop 3\n\tbuffer_load_dwordx4 %0, %1, %3 offen sc1 lds" ::"v"(vo), "s"(rs), "s"(m0v), "s"(so) : "memory");
  else asm volatile("s_mov_b32 m0, %2\n\ts_nop 3\n\tbuffer_load_dwordx4 %0, %1, %3 offen lds" ::"v"(vo), "s"(rs), "s"(m0v), "s"(so) : "memory");
}
#define WAIT_VM(n) asm volatile("s_waitcnt vmcnt(" #n ")" ::: "memory")
#define BARRIER() asm volatile("s_waitcnt lgkmcnt(0)\n\ts_barrier" ::: "memory")
#define DRAIN_BARRIER() asm volatile("s_waitcnt vmcnt(0) lgkmcnt(0)\n\ts_barrier" ::: "memory")

struct Ctx {
  rsrc_t ws;
  GAS unsigned char* wsp;
  u64 tag;
  int b, w, l, g, G;
};
DEV unsigned stream_src(const Ctx& c, int n) {
  return n < 300 ? WG_OFF + c.g * WG_STR + n * 1024 : n < 420 ? W3_OFF + (c.G * 120 + n - 300) * 1024 : WG_OFF + c.g * WG_STR + (n - 120) * 1024;
}
template <int W0> DEV void dma_batch(const Ctx& c, int n0, int n1) {
  if (c.w >= W0)
    for (int n = n0 + c.w - W0; n < n1; n += 8 - W0) dma1<true>(c.ws, c.l * 16, stream_src(c, n), (n % RING) * 1024);
}
DEV void set_flag(const Ctx& c, int kind) {
  if (threadIdx.x == 0) __hip_atomic_store((GAS u64*)(c.wsp + FLG_OFF + (kind * 256 + c.b) * 256), c.tag, __ATOMIC_RELAXED, __HIP_MEMORY_SCOPE_AGENT);
}
DEV void poll_flags(const Ctx& c, int kind, int blk) {
  GAS u64* f = (GAS u64*)(c.wsp + FLG_OFF + (kind * 256 + blk) * 256);
  for (unsigned spins = 0; spins < SPIN_CAP; ++spins)
    if (__all(__hip_atomic_load(f, __ATOMIC_RELAXED, __HIP_MEMORY_SCOPE_AGENT) == c.tag)) break;
  __builtin_amdgcn_fence(__ATOMIC_ACQUIRE, "wavefront");
}
#define STAMP(i) do {} while (0)

template <bool OA, bool OB>
DEV void p1_loop(LAS char* sm, rsrc_t rx, rsrc_t rw, rsrc_t rws, unsigned xvo, unsigned wvo0, unsigned wvo1, unsigned stvo, int w,
                 unsigned xa0, unsigned xa1, unsigned wa, unsigned wb, f32x4 (&acc)[2][4], float (&cs)[2], v4u& st) {
#pragma unroll 1
  for (int kt = 0; kt < 10; ++kt) {
    if (kt == 0) WAIT_VM(17); else if (kt < 4) WAIT_VM(13); else if (kt < 8) WAIT_VM(8); else if (kt == 8) WAIT_VM(5); else WAIT_VM(1);
    asm volatile("s_barrier" ::: "memory");
    if (kt >= 1 && kt <= 6) {
      const unsigned sl = ((kt - 1) & 3) * 32768, k4 = kt + 3;
      dma1<false>(rx, xvo, k4 * 128, sl + w * 2048);
      dma1<false>(rx, xvo, k4 * 128 + 10240, sl + w * 2048 + 1024);
      dma1<false>(rw, wvo0, k4 * (64 * 3840), sl + 16384 + w * 2048);
      dma1<false>(rw, wvo1, k4 * (64 * 3840), sl + 16384 + w * 2048 + 1024);
    }
    if (kt == 7) asm volatile("buffer_load_dwordx4 %0, %1, %2, 0 offen sc1" : "=v"(st) : "v"(stvo), "s"(rws) : "memory");
    LAS char* base = sm + (kt & 3) * 32768;
#pragma unroll
    for (int s = 0; s < 2; ++s) {
      v4u xf[4];
#pragma unroll
      for (int m = 0; m < 4; ++m) xf[m] = *(LAS v4u*)(base + (s ? xa1 : xa0) + m * 2048);
      v2u a0 = trrd(base + wa + s * 8192), a1 = trrd(base + wa + s * 8192 + 2048);
      v2u b0 = trrd(base + wb + s * 8192), b1 = trrd(base + wb + s * 8192 + 2048);
      v4u fa = {a0.x, a0.y, a1.x, a1.y}, fb = {b0.x, b0.y, b1.x, b1.y};
#pragma unroll
      for (int m = 0; m < 4; ++m) {
        acc[0][m] = OA ? mfma16(fa, xf[m], acc[0][m]) : mfma16(xf[m], fa, acc[0][m]);
        acc[1][m] = OB ? mfma16(fb, xf[m], acc[1][m]) : mfma16(xf[m], fb, acc[1][m]);
      }
      cs[0] = dot1(fa.w, dot1(fa.z, dot1(fa.y, dot1(fa.x, cs[0]))));
      cs[1] = dot1(fb.w, dot1(fb.z, dot1(fb.y, dot1(fb.x, cs[1]))));
    }
    asm volatile("s_waitcnt lgkmcnt(0)" ::: "memory");
  }
}

DEV void pair_reduce(LAS char* scr, int w, int l, f32x4 (&c)[5]) {
  const int kh = w >> 2;
  LAS char* mine = scr + (w & 3) * 5120 + l * 16;
  *(LAS f32x4*)(mine + kh * 2048) = c[2];
  *(LAS f32x4*)(mine + kh * 2048 + 1024) = c[3];
  if (kh) *(LAS f32x4*)(mine + 4096) = c[4];
  BARRIER();
  c[0] += *(LAS f32x4*)(mine + (kh ^ 1) * 2048);
  c[1] += *(LAS f32x4*)(mine + (kh ^ 1) * 2048 + 1024);
  if (!kh) c[4] += *(LAS f32x4*)(mine + 4096);
}
template <bool STATS> DEV void store_tile(const Ctx& c, int buf, int stbuf, int mt, v2u a, v2u b, v2u e) {
  const int kh = c.w >> 2, R = c.l >> 4, t = c.l & 15;
  const int cc = 10 * c.g + 4 * kh + 2 * (R & 1) + (R >> 1);
  st16(c.ws, buf + (mt * 20 + (cc >> 2)) * 1024 + ((cc & 3) * 16 + t) * 16, swap_pair(a, b));
  if (!kh) {
    const int c4 = 10 * c.g + 8 + (R >> 1);
    st8(c.ws, buf + (mt * 20 + (c4 >> 2)) * 1024 + ((c4 & 3) * 16 + t) * 16 + (R & 1) * 8, e);
  }
  if (STATS) {
    f32x4 fa = unpk4(a), fb = unpk4(b), fe = unpk4(kh ? v2u{0, 0} : e);
    f32x4 s4 = fa + fb + fe, q4 = fa * fa + fb * fb + fe * fe;
    float s = quadsum((s4.x + s4.y) + (s4.z + s4.w)), q = quadsum((q4.x + q4.y) + (q4.z + q4.w));
    st8(c.ws, R == 0 ? stbuf + mt * 2048 + (2 * c.g + kh) * 128 + t * 8 : OOB, v2u{__float_as_uint(s), __float_as_uint(q)});
  }
}
DEV void row_stats(v2u s0, v2u s1, v2u s2, v2u s3, float& mean, float& var) {
  float s = (__uint_as_float(s0.x) + __uint_as_float(s1.x)) + (__uint_as_float(s2.x) + __uint_as_float(s3.x));
  float q = (__uint_as_float(s0.y) + __uint_as_float(s1.y)) + (__uint_as_float(s2.y) + __uint_as_float(s3.y));
  mean = quadsum(s) * (1.f / D);
  var = fmaxf(quadsum(q) * (1.f / D) - mean * mean, 0.f);
}
DEV f32x4 cs_read(LAS char* sm, int off, int stride) {
  return (*(LAS f32x4*)(sm + off) + *(LAS f32x4*)(sm + off + stride)) + (*(LAS f32x4*)(sm + off + 2 * stride) + *(LAS f32x4*)(sm + off + 3 * stride));
}

#define GEMM_SMALL(NAME, C, VA0, VA1, SA, VW, DM, DS)                                                                            \
  asm volatile(NAME : [c0] "=&v"(C[0]), [c1] "=&v"(C[1]), [c2] "=&v"(C[2]), [c3] "=&v"(C[3]), [c4] "=&v"(C[4]), [vw] "+v"(VW),   \
               [sa] "+s"(SA), [dm0] "+s"(DM), [ds0] "+s"(DS)                                                                     \
               : [va0] "v"(VA0), [va1] "v"(VA1), [rs] "s"(c.ws), [l16] "v"(l16) : PS_CLOB, "scc", "memory")

__global__ void __launch_bounds__(512, 2) mk_fwd(Params p) {
  extern __shared__ __attribute__((aligned(16))) char smem_[];
  LAS char* sm = (LAS char*)smem_;
  const int b = blockIdx.x, w = __builtin_amdgcn_readfirstlane(threadIdx.x >> 6), l = threadIdx.x & 63;
  const int xc = b & 7, j = b >> 3;
  STAMP(0);
  Ctx c;
  c.ws = mk_rsrc(p.ws, WS_END);
  c.wsp = (GAS unsigned char*)p.ws;
  c.b = b; c.w = w; c.l = l; c.g = j & 7; c.G = j & 15;
  const unsigned l16 = l * 16;
  const int R = l >> 4, t16 = l & 15;
  const rsrc_t rx = mk_rsrc(p.x, S * D * 2);

  {
    const int T = 8 * (xc & 1) + (j & 7), hp = 4 * (xc >> 1) + (j >> 3);
    const rsrc_t rw = mk_rsrc(p.wqkv, D * 3 * D * 2);
    v4u xr0, xr1;
    {
      const unsigned vo = l < 40 ? (8 * b + w) * 1280 + l * 32 : OOB;
      asm volatile("buffer_load_dwordx4 %0, %2, %3, 0 offen\n\tbuffer_load_dwordx4 %1, %2, %3, 0 offen offset:16" : "=&v"(xr0), "=&v"(xr1) : "v"(vo), "s"(rx) : "memory");
    }
    const unsigned xvo = (128 * T + 16 * w + (l >> 3)) * 1280 + (((l & 7) ^ ((l >> 3) & 7)) << 4);
    unsigned wvo[2];
#pragma unroll
    for (int i = 0; i < 2; ++i) {
      const int k = 32 * (w >> 2) + 16 * ((w >> 1) & 1) + 8 * i + 4 * (w & 1) + R;
      const int ch = t16 ^ ((4 * i + R) << 1);
      const int ten = ch / 5;
      wvo[i] = ch == 15 ? OOB : k * 3840 + ten * 1280 + 80 * hp + (ch - 5 * ten) * 16;
    }
#pragma unroll
    for (int kt = 0; kt < 4; ++kt) {
      dma1<false>(rx, xvo, kt * 128, kt * 32768 + w * 2048);
      dma1<false>(rx, xvo, kt * 128 + 10240, kt * 32768 + w * 2048 + 1024);
      dma1<false>(rw, wvo[0], kt * (64 * 3840), kt * 32768 + 16384 + w * 2048);
      dma1<false>(rw, wvo[1], kt * (64 * 3840), kt * 32768 + 16384 + w * 2048 + 1024);
    }
    const int fam = b >> 5, u0 = 5 * (b & 31);
#pragma unroll
    for (int i = 0; i < 4; ++i) {
      const int ins = w * 4 + i, u = u0 + ins / 6, pc = ins % 6;
      const bool is3 = u >= 108;
      const u16* src = u < 20 ? p.wo : u < 40 ? p.w1 : u < 60 ? p.w2 : u < 108 ? p.w4 : p.w3;
      const int ks = u < 60 ? u % 20 : u < 108 ? u - 60 : (u - 108) % 20;
      const int cidx = pc * 64 + l;
      const int row = is3 ? cidx / 12 : cidx / 10, cc = cidx - row * (is3 ? 12 : 10);
      const unsigned vo = (ins < 30 && u < 148 && row < 32) ? (is3 ? (32 * ks + row) * 3072 + (2 * fam + (u - 108) / 20) * 192 + cc * 16 : (32 * ks + row) * 1280 + fam * 160 + cc * 16) : OOB;
      dma1<false>(mk_rsrc(src, u < 60 ? D * D * 2 : D * F * 2), vo, 0, 131072 + ins * 1024);
    }
    const unsigned E = *(const CAS unsigned*)p.ws;
    const unsigned tlo = E * 0x9E3779B1u + 0x7F4A7C15u, thi = tlo * 0x85EBCA6Bu + 0xC2B2AE35u;
    c.tag = ((u64)thi << 32) | tlo;
    {
      asm volatile("s_waitcnt vmcnt(20)" : "+v"(xr0), "+v"(xr1)::"memory");
      float v[16];
#pragma unroll
      for (int i = 0; i < 4; ++i) { v[2 * i] = bflo(xr0[i]); v[2 * i + 1] = bfhi(xr0[i]); v[8 + 2 * i] = bflo(xr1[i]); v[9 + 2 * i] = bfhi(xr1[i]); }
      float s = 0;
#pragma unroll
      for (int i = 0; i < 16; ++i) s += v[i];
#pragma unroll
      for (int o = 1; o < 64; o <<= 1) s += __shfl_xor(s, o);
      const float mean = s * (1.f / D);
      float q = 0;
#pragma unroll
      for (int i = 0; i < 16; ++i) q += (v[i] - mean) * (v[i] - mean);
      q = l < 40 ? q : 0.f;
#pragma unroll
      for (int o = 1; o < 64; o <<= 1) q += __shfl_xor(q, o);
      const float rstd = __builtin_amdgcn_rsqf(q * (1.f / D) + EPS);
      const v4u gr = {__float_as_uint(mean), tlo, __float_as_uint(rstd), tlo};
      const unsigned vo = l == 0 ? XST_OFF + (8 * b + w) * 16 : OOB;
      asm volatile("buffer_store_dwordx4 %0, %1, %2, 0 offen sc1\n\ts_nop 1" ::"v"(gr), "v"(vo), "s"(c.ws) : "memory");
    }
    STAMP(1);
    const int wc = w & 3, th = w >> 2;
    const int row0 = 64 * th + t16;
    const unsigned xa0 = row0 * 128 + ((R ^ (row0 & 7)) << 4), xa1 = xa0 ^ 64;
    unsigned wad[2];
    {
      const int q4 = t16 >> 2, pp = l & 3, r7 = 4 * (R & 1) + q4;
#pragma unroll
      for (int i = 0; i < 2; ++i) {
        const int tile = 2 * wc + i;
        const int B = tile < 4 ? 40 * tile + 8 * pp : tile == 4 ? 32 + 40 * pp : 32 * tile + 8 * pp;
        wad[i] = 16384 + (16 * (R >> 1) + r7) * 256 + (((B >> 4) ^ (r7 << 1)) << 4) + (B & 8);
      }
    }
    f32x4 acc[2][4];
#pragma unroll
    for (int i = 0; i < 2; ++i)
#pragma unroll
      for (int m = 0; m < 4; ++m) acc[i][m] = f32x4{0, 0, 0, 0};
    float cs[2] = {0.f, 0.f};
    v4u st;
    const unsigned stvo = XST_OFF + (128 * T + 64 * th + l) * 16;
    if (wc < 2) p1_loop<true, true>(sm, rx, rw, c.ws, xvo, wvo[0], wvo[1], stvo, w, xa0, xa1, wad[0], wad[1], acc, cs, st);
    else if (wc == 2) p1_loop<true, false>(sm, rx, rw, c.ws, xvo, wvo[0], wvo[1], stvo, w, xa0, xa1, wad[0], wad[1], acc, cs, st);
    else p1_loop<false, false>(sm, rx, rw, c.ws, xvo, wvo[0], wvo[1], stvo, w, xa0, xa1, wad[0], wad[1], acc, cs, st);
    asm volatile("s_waitcnt vmcnt(0)" : "+v"(st)::"memory");
    STAMP(2);
    for (unsigned spins = 0; spins < SPIN_CAP && !__all(st.y == tlo && st.w == tlo); ++spins) {
      asm volatile("" ::: "memory");
      st = __builtin_amdgcn_raw_buffer_load_b128(c.ws, stvo, 0, 16);
    }
    LAS char* scr = sm + 65536 + w * 1024;
    {
      const float rs = __uint_as_float(st.z);
      *(LAS f32x2*)(scr + l * 8) = f32x2{rs, -__uint_as_float(st.x) * rs};
      cs[0] = quadsum(cs[0]); cs[1] = quadsum(cs[1]);
      *(LAS float*)(scr + 512 + (R & 1) * 64 + t16 * 4) = (R & 1) ? cs[1] : cs[0];
    }
    const float QS = 0.22360679774997896f * 1.4426950408889634f;
    const int tb = 4 * T + 2 * th;
    if (wc < 2) {
      const f32x4 ca = *(LAS f32x4*)(scr + 512 + R * 16), cb = *(LAS f32x4*)(scr + 576 + R * 16);
      const int h = 2 * hp + (R & 1);
#pragma unroll
      for (int m = 0; m < 4; ++m) {
        f32x2 ab = *(LAS f32x2*)(scr + (16 * m + t16) * 8);
        if (wc == 0) ab *= QS;
        const v4u o = swap_pair(pk4(acc[0][m] * ab.x + ca * ab.y), pk4(acc[1][m] * ab.x + cb * ab.y));
        const int rec = h * 64 + tb + (m >> 1);
        st16(c.ws, (wc == 0 ? Q16_OFF + rec * 1024 : KV_OFF + rec * 2816) + ((R >> 1) * 32 + 16 * (m & 1) + t16) * 16, o);
      }
    } else {
      if (wc == 2) {
        const f32x4 ca = *(LAS f32x4*)(scr + 512 + R * 16);
        const int h = 2 * hp + (R & 1);
#pragma unroll
        for (int m = 0; m < 4; ++m) {
          f32x2 ab = *(LAS f32x2*)(scr + (16 * m + t16) * 8);
          if (R < 2) ab *= QS;
          const int rec = h * 64 + tb + (m >> 1);
          st8(c.ws, (R < 2 ? Q8_OFF + rec * 256 : KV_OFF + rec * 2816 + 1024) + (16 * (m & 1) + t16) * 8, pk4(acc[0][m] * ab.x + ca * ab.y));
        }
      }
#pragma unroll
      for (int i = 0; i < 2; ++i) {
        if (i == 0 && wc == 2) continue;
        const int vc = 16 * (2 * wc + i - 5) + t16;
        const int hd = vc < 40 ? vc / 20 : vc - 40, d = vc < 40 ? vc - 20 * hd : 20;
#pragma unroll
        for (int u = 0; u < 2; ++u) {
          v2u pk[2];
#pragma unroll
          for (int mm = 0; mm < 2; ++mm) {
            const int m = 2 * u + mm;
            const f32x4 p01 = *(LAS f32x4*)(scr + (16 * m + 4 * R) * 8), p23 = *(LAS f32x4*)(scr + (16 * m + 4 * R) * 8 + 16);
            pk[mm] = pk4(acc[i][m] * f32x4{p01.x, p01.z, p23.x, p23.z} + f32x4{p01.y, p01.w, p23.y, p23.w} * cs[i]);
          }
          v4u o = swap_pair(pk[0], pk[1]);
          if (vc >= 40) o = v4u{0x3f803f80u, 0x3f803f80u, 0x3f803f80u, 0x3f803f80u};
          st16(c.ws, vc < 42 ? KV_OFF + ((2 * hp + hd) * 64 + tb + u) * 2816 + 1280 + ((2 * (R & 1) + (R >> 1)) * 24 + d) * 16 : OOB, o);
        }
      }
    }
    DRAIN_BARRIER();
    set_flag(c, FK_P1);
    STAMP(3);
#pragma unroll 1
    for (int f = w; f < 30; f += 8) {
      const int u = u0 + f / 6, nt = f % 6;
      const bool is3 = u >= 108;
      if (u >= 148 || (nt == 5 && !is3)) continue;
      const int rb = is3 ? 192 : 160;
      LAS char* a = sm + 131072 + (f / 6) * 6144 + (8 * R + (t16 >> 2)) * rb + nt * 32 + (l & 3) * 8;
      const v2u f0 = trrd(a), f1 = trrd(a + 4 * rb);
      int dst;
      if (is3) dst = W3_OFF + ((2 * fam + (u - 108) / 20) * 120 + ((u - 108) % 20) * 6 + nt) * 1024;
      else {
        const int half = u < 60 ? 10 : 24, ks = u < 60 ? u % 20 : u - 60, kh = ks >= half, ds = ks - kh * half;
        dst = WG_OFF + fam * WG_STR + ((u < 60 ? (u / 20) * 100 : 300) + ds * 10 + kh * 5 + ((kh && nt < 4) ? nt ^ 2 : nt)) * 1024;
      }
      st16(c.ws, dst + l16, v4u{f0.x, f0.y, f1.x, f1.y});
      if (is3 || (u >= 20 && u < 40)) {
        const float s = quadsum(dot1(f1.y, dot1(f1.x, dot1(f0.y, dot1(f0.x, 0.f)))));
        const int co = is3 ? CS3_OFF + (((u - 108) % 20) * F + (2 * fam + (u - 108) / 20) * 96 + nt * 16 + t16) * 4 : CS1_OFF + ((u - 20) * D + fam * 80 + nt * 16 + t16) * 4;
        __builtin_amdgcn_raw_buffer_store_b32(__float_as_uint(s), c.ws, R == 0 ? co : OOB, 0, 16);
      }
    }
  }
  {
    const int hp = 2 * xc + (j & 1), T = j >> 1;
    if (w == 0) poll_flags(c, FK_P1, (((hp >> 2) << 1) | (t16 >> 3)) + 8 * (((hp & 3) << 3) | (t16 & 7)));
    DRAIN_BARRIER();
    set_flag(c, FK_R);
    STAMP(4);
    if (MK_STOP <= 1) return;
    const int hh = w & 1, kq = w >> 1, h = 2 * hp + hh;
    const int r = l & 31, hi = l >> 5;
    const unsigned r8 = hi ? OOB : r * 8, vvo = r <= 20 ? (hi * 24 + r) * 16 : OOB;
    f32x16 o[4];
#pragma unroll
    for (int i = 0; i < 4; ++i) o[i] = f32x16{0, 0, 0, 0, 0, 0, 0, 0, 0, 0, 0, 0, 0, 0, 0, 0};
    {
      int so = KV_OFF + (h * 64 + kq * 16) * 2816, cnt = 8;
      const int sq = Q16_OFF + (h * 64 + T * 4) * 1024, sq8 = Q8_OFF + (h * 64 + T * 4) * 256;
      v4u q16[4], k16a, k16b, v0a, v0b, v1a, v1b;
      v2u q8[4], k8a, k8b;
      asm volatile(ATT_ASM
                   : [o0] "+v"(o[0]), [o1] "+v"(o[1]), [o2] "+v"(o[2]), [o3] "+v"(o[3]), [k16a] "=&v"(k16a), [k16b] "=&v"(k16b), [k8a] "=&v"(k8a),
                     [k8b] "=&v"(k8b), [v0a] "=&v"(v0a), [v0b] "=&v"(v0b), [v1a] "=&v"(v1a), [v1b] "=&v"(v1b), [so] "+s"(so), [cnt] "+s"(cnt),
                     [q160] "=&v"(q16[0]), [q161] "=&v"(q16[1]), [q162] "=&v"(q16[2]), [q163] "=&v"(q16[3]), [q80] "=&v"(q8[0]), [q81] "=&v"(q8[1]),
                     [q82] "=&v"(q8[2]), [q83] "=&v"(q8[3])
                   : [idx] "v"(0xDDDDDDDD), [l16] "v"(l16), [r8] "v"(r8), [vvo] "v"(vvo), [rs] "s"(c.ws), [sq] "s"(sq), [sq8] "s"(sq8)
                   : ATT_CLOB, "scc", "memory");
    }
    STAMP(5);
    poll_flags(c, FK_R, 32 * (l < 32 ? c.g : c.G >> 1) + (l & 31));
    dma_batch<0>(c, 0, 100);
    LAS char* cb = sm + 102400;
#pragma unroll
    for (int i = 0; i < 4; ++i) {
      LAS char* a = cb + ((hh * 4 + kq) * 4 + i) * 1536;
      *(LAS v4u*)(a + l16) = v4u{pk2(o[i][0], o[i][1]), pk2(o[i][2], o[i][3]), pk2(o[i][4], o[i][5]), pk2(o[i][6], o[i][7])};
      *(LAS v2u*)(a + 1024 + l * 8) = hi ? v2u{__float_as_uint(o[i][8]), 0u} : v2u{pk2(o[i][8], o[i][9]), pk2(o[i][10], o[i][11])};
    }
    BARRIER();
    {
      const int tok = 16 * w + t16, qt = tok >> 5, rr = tok & 31;
      float inv[2];
#pragma unroll
      for (int hd = 0; hd < 2; ++hd) {
        float ls = 0;
#pragma unroll
        for (int k = 0; k < 4; ++k) ls += *(LAS float*)(cb + ((hd * 4 + k) * 4 + qt) * 1536 + 1024 + (32 + rr) * 8);
        inv[hd] = __builtin_amdgcn_rcpf(ls);
      }
#pragma unroll
      for (int rnd = 0; rnd < 2; ++rnd) {
        const int ck = rnd ? 4 : R;
        v2u pk[2];
#pragma unroll
        for (int e = 0; e < 2; ++e) {
          const int gi = 2 * ck + e, hd = gi >= 5, pc = gi - 5 * hd;
          const int off = (hd * 16 + qt) * 1536 + (pc < 4 ? ((pc & 1) * 32 + rr) * 16 + (pc >> 1) * 8 : 1024 + rr * 8);
          f32x4 a = {0, 0, 0, 0};
#pragma unroll
          for (int k = 0; k < 4; ++k) a += unpk4(*(LAS v2u*)(cb + off + k * 6144));
          pk[e] = pk4(a * (hd ? inv[1] : inv[0]));
        }
        const int gc = hp * 5 + ck;
        st16(c.ws, (rnd == 0 || R == 0) ? O_OFF + ((8 * T + w) * 20 + (gc >> 2)) * 1024 + ((gc & 3) * 16 + t16) * 16 : OOB, v4u{pk[0].x, pk[0].y, pk[1].x, pk[1].y});
      }
    }
    DRAIN_BARRIER();
    set_flag(c, FK_P2);
    STAMP(6);
  }
  if (MK_STOP <= 2) return;
  const int rp = 4 * xc + (j >> 3), kh = w >> 2, mt = 4 * rp + (w & 3);
  const int blk0 = xc + 64 * (j >> 3);
  v2u xres[3];
  {
    const int i = threadIdx.x;
    float s1 = 0.f, s3 = 0.f;
    const unsigned o1 = i < 320 ? CS1_OFF + ((i / 80) * 5 * D + c.g * 80 + i % 80) * 4 : OOB;
    const unsigned o3 = i < 384 ? CS3_OFF + ((i / 96) * 5 * F + c.G * 96 + i % 96) * 4 : OOB;
#pragma unroll
    for (int k = 0; k < 5; ++k) {
      s1 += __uint_as_float(__builtin_amdgcn_raw_buffer_load_b32(c.ws, o1, k * D * 4, 16));
      s3 += __uint_as_float(__builtin_amdgcn_raw_buffer_load_b32(c.ws, o3, k * F * 4, 16));
    }
    const unsigned xo = (16 * mt + t16) * 1280 + c.g * 160 + R * 8;
    xres[0] = __builtin_amdgcn_raw_buffer_load_b64(rx, xo + kh * 64, 0, 0);
    xres[1] = __builtin_amdgcn_raw_buffer_load_b64(rx, xo + kh * 64 + 32, 0, 0);
    xres[2] = __builtin_amdgcn_raw_buffer_load_b64(rx, xo + 128, 0, 0);
    if (i < 320) *(LAS float*)(sm + L_CS1 + i * 4) = s1;
    if (i < 384) *(LAS float*)(sm + L_CS3 + i * 4) = s3;
    asm volatile("" : "+v"(xres[0]), "+v"(xres[1]), "+v"(xres[2]));
  }
  dma_batch<1>(c, 100, 150);
  if (w == 0) poll_flags(c, FK_P2, (t16 >> 1) + 8 * ((t16 & 1) + 2 * (rp >> 1)));
  BARRIER();
  STAMP(7);
  const unsigned vw0 = l16 + kh * 5120;
  v2u hq[3];
  {
    f32x4 a[5];
    unsigned va = O_OFF + (mt * 20 + kh * 10) * 1024 + l16, sa = 0, vw = vw0, dm = (0 + 2 * w) * 1024, ds = stream_src(c, 150 + 2 * w);
    GEMM_SMALL(P3_ASM, a, va, va, sa, vw, dm, ds);
    STAMP(8);
    pair_reduce(sm + 20 * 1024, w, l, a);
    dma_batch<0>(c, 190, 238);
    hq[0] = pk4(a[0] + unpk4(xres[0])); hq[1] = pk4(a[1] + unpk4(xres[1])); hq[2] = pk4(a[4] + unpk4(xres[2]));
    store_tile<true>(c, H_OFF, HST_OFF, mt, hq[0], hq[1], hq[2]);
    DRAIN_BARRIER();
    set_flag(c, FK_P3);
    dma_batch<1>(c, 166, 190);
    dma_batch<1>(c, 238, 250);
    if (w == 0) poll_flags(c, FK_P3, blk0 + 8 * (l & 7));
    BARRIER();
    STAMP(9);
  }
  if (MK_STOP <= 3) return;
  f32x4 keep[3];
  {
    f32x4 a[5];
    v2u s0, s1, s2, s3;
    unsigned va = H_OFF + (mt * 20 + kh * 10) * 1024 + l16, sa = 0, vw = vw0, dm = (100 + 2 * w) * 1024, ds = stream_src(c, 250 + 2 * w);
    const unsigned vst = HST_OFF + mt * 2048 + R * 512 + t16 * 8;
    asm volatile(P4_ASM
                 : [c0] "=&v"(a[0]), [c1] "=&v"(a[1]), [c2] "=&v"(a[2]), [c3] "=&v"(a[3]), [c4] "=&v"(a[4]), [vw] "+v"(vw), [sa] "+s"(sa), [dm0] "+s"(dm),
                   [ds0] "+s"(ds), [st0] "=&v"(s0), [st1] "=&v"(s1), [st2] "=&v"(s2), [st3] "=&v"(s3)
                 : [va0] "v"(va), [rs] "s"(c.ws), [l16] "v"(l16), [vst] "v"(vst)
                 : PS_CLOB, "scc", "memory");
    STAMP(10);
    pair_reduce(sm + 120 * 1024, w, l, a);
    dma_batch<0>(c, 290, 338);
    float mean, var;
    row_stats(s0, s1, s2, s3, mean, var);
    const float r1 = __builtin_amdgcn_rsqf(var + EPS);
    const float sc = r1 * __builtin_amdgcn_rsqf(var * r1 * r1 + EPS), sb = -mean * sc;
    const v2u g0 = pk4(gelu4(a[0] * sc + cs_read(sm, L_CS1 + (32 * kh + 4 * R) * 4, 320) * sb));
    const v2u g1 = pk4(gelu4(a[1] * sc + cs_read(sm, L_CS1 + (32 * kh + 16 + 4 * R) * 4, 320) * sb));
    v2u g4 = {0, 0};
    if (!kh) g4 = pk4(gelu4(a[4] * sc + cs_read(sm, L_CS1 + (64 + 4 * R) * 4, 320) * sb));
    store_tile<false>(c, G_OFF, 0, mt, g0, g1, g4);
#pragma unroll
    for (int i = 0; i < 3; ++i) keep[i] = (unpk4(hq[i]) - mean) * r1;
    DRAIN_BARRIER();
    set_flag(c, FK_P4);
    dma_batch<1>(c, 266, 290);
    dma_batch<1>(c, 338, 350);
    if (w == 0) {
      const int rn = min(max(rp + ((l >> 3) & 3) - 1, 0), 31), rq = min(rn, rp + 1);
      poll_flags(c, FK_P4, (rq >> 2) + 64 * (rq & 3) + 8 * (l & 7));
    }
    BARRIER();
    STAMP(11);
  }
  if (MK_STOP <= 4) return;
  {
    f32x4 a[5];
    unsigned va[2];
#pragma unroll
    for (int i = 0; i < 2; ++i) {
      const int tk = 16 * mt + t16 + (i ? kh + 1 : -(kh + 1));
      va[i] = (tk >= 0 && tk < S) ? G_OFF + ((tk >> 4) * 20 + kh * 10) * 1024 + (R * 16 + (tk & 15)) * 16 : OOB;
    }
    unsigned sa = 0, vw = vw0, dm = (50 + 2 * w) * 1024, ds = stream_src(c, 350 + 2 * w);
    GEMM_SMALL(P5_ASM, a, va[0], va[1], sa, vw, dm, ds);
    STAMP(12);
    pair_reduce(sm + 70 * 1024, w, l, a);
    dma_batch<0>(c, 390, 438);
    const v2u q0 = pk4(a[0] + keep[0]), q1 = pk4(a[1] + keep[1]), q4 = pk4(a[4] + keep[2]);
    store_tile<true>(c, X1_OFF, X1ST_OFF, mt, q0, q1, q4);
    keep[0] = unpk4(q0) + unpk4(hq[0]); keep[1] = unpk4(q1) + unpk4(hq[1]); keep[2] = unpk4(q4) + unpk4(hq[2]);
    DRAIN_BARRIER();
    set_flag(c, FK_P5);
    dma_batch<1>(c, 366, 390);
    dma_batch<1>(c, 438, 450);
    if (w == 0) poll_flags(c, FK_P5, xc + 128 * (j >> 4) + 8 * t16);
    BARRIER();
    STAMP(13);
  }
  if (MK_STOP <= 5) return;
  {
    const int m6 = 8 * (2 * xc + (j >> 4)) + w;
    f32x4 a[6];
    v2u s0, s1, s2, s3;
    unsigned va = X1_OFF + m6 * 20 * 1024 + l16, sa = 0, vw = l16, dm = (0 + 6 * w) * 1024, ds = stream_src(c, 450 + 6 * w);
    const unsigned vst = X1ST_OFF + m6 * 2048 + R * 512 + t16 * 8;
    asm volatile(P6_ASM
                 : [c0] "=&v"(a[0]), [c1] "=&v"(a[1]), [c2] "=&v"(a[2]), [c3] "=&v"(a[3]), [c4] "=&v"(a[4]), [c5] "=&v"(a[5]), [vw] "+v"(vw), [sa] "+s"(sa),
                   [dm0] "+s"(dm), [ds0] "+s"(ds), [st0] "=&v"(s0), [st1] "=&v"(s1), [st2] "=&v"(s2), [st3] "=&v"(s3)
                 : [va0] "v"(va), [rs] "s"(c.ws), [l16] "v"(l16), [vst] "v"(vst)
                 : P6_CLOB, "scc", "memory");
    STAMP(14);
    float mean, var;
    row_stats(s0, s1, s2, s3, mean, var);
    const float sc = __builtin_amdgcn_rsqf(var + EPS), sb = -mean * sc;
#pragma unroll
    for (int i = 0; i < 3; ++i) {
      const v2u u0 = pk4(gelu4(a[2 * i] * sc + cs_read(sm, L_CS3 + (32 * i + 4 * R) * 4, 384) * sb));
      const v2u u1 = pk4(gelu4(a[2 * i + 1] * sc + cs_read(sm, L_CS3 + (32 * i + 16 + 4 * R) * 4, 384) * sb));
      st16(c.ws, U_OFF + (m6 * 48 + 3 * c.G + i) * 1024 + ((2 * (R & 1) + (R >> 1)) * 16 + t16) * 16, swap_pair(u0, u1));
    }
    DRAIN_BARRIER();
    set_flag(c, FK_P6);
    dma_batch<1>(c, 498, 570);
    if (w == 0) poll_flags(c, FK_P6, xc + 128 * (j >> 4) + 8 * t16);
    BARRIER();
    STAMP(15);
  }
  if (MK_STOP <= 6) return;
  {
    if (b == 0 && threadIdx.x == 0) __hip_atomic_store((GAS unsigned*)c.wsp, (unsigned)c.tag, __ATOMIC_RELAXED, __HIP_MEMORY_SCOPE_AGENT);
    f32x4 a[5];
    unsigned va = U_OFF + (mt * 48 + kh * 24) * 1024 + l16, sa = 0, vw = vw0;
    unsigned dm0 = ((120 + 5 * w) % RING) * 1024, ds0 = stream_src(c, 570 + 5 * w), dm1 = (10 + 7 * w) * 1024, ds1 = stream_src(c, 610 + 7 * w);
    asm volatile(P7_ASM
                 : [c0] "=&v"(a[0]), [c1] "=&v"(a[1]), [c2] "=&v"(a[2]), [c3] "=&v"(a[3]), [c4] "=&v"(a[4]), [vw] "+v"(vw), [sa] "+s"(sa), [dm0] "+s"(dm0),
                   [ds0] "+s"(ds0), [dm1] "+s"(dm1), [ds1] "+s"(ds1)
                 : [va0] "v"(va), [rs] "s"(c.ws), [l16] "v"(l16)
                 : P7_CLOB, "scc", "memory");
    STAMP(16);
    pair_reduce(sm + 70 * 1024, w, l, a);
    const rsrc_t ro = mk_rsrc(p.out, S * D * 2);
    const unsigned oo = (16 * mt + t16) * 1280 + c.g * 160;
    __builtin_amdgcn_raw_buffer_store_b128(swap_pair(pk4(a[0] + keep[0]), pk4(a[1] + keep[1])), ro, oo + kh * 64 + (R & 1) * 32 + (R >> 1) * 16, 0, 0);
    if (!kh) __builtin_amdgcn_raw_buffer_store_b64(pk4(a[4] + keep[2]), ro, oo + 128 + R * 8, 0, 0);
    STAMP(17);
  }
}

extern "C" void kernel_launch(void* const* d_in, const int* in_sizes, int n_in, void* d_out, int out_size, void* d_ws, size_t ws_size, hipStream_t stream) {
  Params p;
  p.x = (const u16*)d_in[0];
  p.wqkv = (const u16*)d_in[1];
  p.wo = (const u16*)d_in[2];
  p.w1 = (const u16*)d_in[4];
  p.w2 = (const u16*)d_in[5];
  p.w3 = (const u16*)d_in[6];
  p.w4 = (const u16*)d_in[7];
  p.out = (u16*)d_out;
  p.ws = (unsigned char*)d_ws;
  (void)hipFuncSetAttribute((const void*)mk_fwd, hipFuncAttributeMaxDynamicSharedMemorySize, LDS_BYTES);
  mk_fwd<<<256, 512, LDS_BYTES, stream>>>(p);
}
```

```cpp
#include <hip/hip_runtime.h>

typedef unsigned short u16;
typedef unsigned long long u64;
typedef unsigned v4u __attribute__((ext_vector_type(4)));
typedef unsigned v2u __attribute__((ext_vector_type(2)));
typedef float f32x16 __attribute__((ext_vector_type(16)));
typedef float f32x4 __attribute__((ext_vector_type(4)));
typedef float f32x2 __attribute__((ext_vector_type(2)));
typedef __bf16 bf16x8 __attribute__((ext_vector_type(8)));
typedef __bf16 bf16x2 __attribute__((ext_vector_type(2)));
typedef short s16x4 __attribute__((ext_vector_type(4)));
#define LAS __attribute__((address_space(3)))
#define GAS __attribute__((address_space(1)))
#define CAS __attribute__((address_space(4)))
#define DEV __device__ __forceinline__
typedef __amdgpu_buffer_rsrc_t rsrc_t;

#define MK_STOP 99
#define MK_EXP 0
#define MK_STAMP 0

#define ATT_ASM \
  "s_nop 4\n" \
  "buffer_load_dwordx4 %[q160], %[l16], %[rs], %[sq] offen offset:0 sc1\n" \
  "buffer_load_dwordx4 %[q161], %[l16], %[rs], %[sq] offen offset:1024 sc1\n" \
  "buffer_load_dwordx4 %[q162], %[l16], %[rs], %[sq] offen offset:2048 sc1\n" \
  "buffer_load_dwordx4 %[q163], %[l16], %[rs], %[sq] offen offset:3072 sc1\n" \
  "buffer_load_dwordx4 %[k16a], %[l16], %[rs], %[so] offen offset:0 sc1\n" \
  "buffer_load_dwordx2 %[k8a], %[r8], %[rs], %[so] offen offset:1024 sc1\n" \
  "buffer_load_dwordx2 %[q80], %[r8], %[rs], %[sq8] offen offset:0 sc1\n" \
  "buffer_load_dwordx2 %[q81], %[r8], %[rs], %[sq8] offen offset:256 sc1\n" \
  "buffer_load_dwordx2 %[q82], %[r8], %[rs], %[sq8] offen offset:512 sc1\n" \
  "buffer_load_dwordx2 %[q83], %[r8], %[rs], %[sq8] offen offset:768 sc1\n" \
  "buffer_load_dwordx4 %[v0a], %[vvo], %[rs], %[so] offen offset:1280 sc1\n" \
  "buffer_load_dwordx4 %[v1a], %[vvo], %[rs], %[so] offen offset:2048 sc1\n" \
  "buffer_load_dwordx4 %[k16b], %[l16], %[rs], %[so] offen offset:2816 sc1\n" \
  "buffer_load_dwordx2 %[k8b], %[r8], %[rs], %[so] offen offset:3840 sc1\n" \
  "s_add_u32 %[so], %[so], 2816\n" \
  "s_waitcnt vmcnt(4)\n" \
  "v_mfma_f32_32x32x16_bf16 v[192:207], %[k16a], %[q160], 0\n" \
  "v_mfma_f32_32x32x8_bf16 v[192:207], %[k8a], %[q80], v[192:207]\n" \
  "v_mfma_f32_32x32x16_bf16 v[208:223], %[k16a], %[q161], 0\n" \
  "v_mfma_f32_32x32x8_bf16 v[208:223], %[k8a], %[q81], v[208:223]\n" \
  "s_nop 7\n" \
  "s_nop 3\n" \
  "v_exp_f32 v192, v192\n" \
  "v_exp_f32 v193, v193\n" \
  "s_branch LATT_skip%=\n" \
  "LATT_loop%=:\n" \
  "v_exp_f32 v192, v192\n" \
  "v_exp_f32 v193, v193\n" \
  "v_smfmac_f32_32x32x32_bf16 %[o3], %[v1b], v[248:255], %[idx]\n" \
  "LATT_skip%=:\n" \
  "buffer_load_dwordx4 %[v0b], %[vvo], %[rs], %[so] offen offset:1280 sc1\n" \
  "buffer_load_dwordx4 %[v1b], %[vvo], %[rs], %[so] offen offset:2048 sc1\n" \
  "v_exp_f32 v194, v194\n" \
  "v_exp_f32 v195, v195\n" \
  "v_exp_f32 v196, v196\n" \
  "v_exp_f32 v197, v197\n" \
  "v_mfma_f32_32x32x16_bf16 v[224:239], %[k16a], %[q162], 0\n" \
  "v_exp_f32 v198, v198\n" \
  "v_exp_f32 v199, v199\n" \
  "v_exp_f32 v200, v200\n" \
  "v_exp_f32 v201, v201\n" \
  "s_waitcnt vmcnt(4)\n" \
  "v_smfmac_f32_32x32x32_bf16 %[o0], %[v0a], v[192:199], %[idx]\n" \
  "v_exp_f32 v202, v202\n" \
  "v_exp_f32 v203, v203\n" \
  "v_exp_f32 v204, v204\n" \
  "v_exp_f32 v205, v205\n" \
  "v_mfma_f32_32x32x8_bf16 v[224:239], %[k8a], %[q82], v[224:239]\n" \
  "v_exp_f32 v206, v206\n" \
  "v_exp_f32 v207, v207\n" \
  "v_exp_f32 v208, v208\n" \
  "v_exp_f32 v209, v209\n" \
  "v_smfmac_f32_32x32x32_bf16 %[o0], %[v1a], v[200:207], %[idx]\n" \
  "v_exp_f32 v210, v210\n" \
  "v_exp_f32 v211, v211\n" \
  "v_exp_f32 v212, v212\n" \
  "v_exp_f32 v213, v213\n" \
  "v_mfma_f32_32x32x16_bf16 v[240:255], %[k16a], %[q163], 0\n" \
  "v_exp_f32 v214, v214\n" \
  "v_exp_f32 v215, v215\n" \
  "v_exp_f32 v216, v216\n" \
  "v_exp_f32 v217, v217\n" \
  "v_smfmac_f32_32x32x32_bf16 %[o1], %[v0a], v[208:215], %[idx]\n" \
  "v_exp_f32 v218, v218\n" \
  "v_exp_f32 v219, v219\n" \
  "v_exp_f32 v220, v220\n" \
  "v_exp_f32 v221, v221\n" \
  "v_mfma_f32_32x32x8_bf16 v[240:255], %[k8a], %[q83], v[240:255]\n" \
  "v_exp_f32 v222, v222\n" \
  "v_exp_f32 v223, v223\n" \
  "v_exp_f32 v224, v224\n" \
  "v_exp_f32 v225, v225\n" \
  "v_smfmac_f32_32x32x32_bf16 %[o1], %[v1a], v[216:223], %[idx]\n" \
  "v_exp_f32 v226, v226\n" \
  "v_exp_f32 v227, v227\n" \
  "v_exp_f32 v228, v228\n" \
  "v_exp_f32 v229, v229\n" \
  "s_waitcnt vmcnt(2)\n" \
  "v_mfma_f32_32x32x16_bf16 v[192:207], %[k16b], %[q160], 0\n" \
  "buffer_load_dwordx4 %[k16a], %[l16], %[rs], %[so] offen offset:2816 sc1\n" \
  "buffer_load_dwordx2 %[k8a], %[r8], %[rs], %[so] offen offset:3840 sc1\n" \
  "s_add_u32 %[so], %[so], 2816\n" \
  "v_exp_f32 v230, v230\n" \
  "v_exp_f32 v231, v231\n" \
  "v_exp_f32 v232, v232\n" \
  "v_exp_f32 v233, v233\n" \
  "v_smfmac_f32_32x32x32_bf16 %[o2], %[v0a], v[224:231], %[idx]\n" \
  "v_exp_f32 v234, v234\n" \
  "v_exp_f32 v235, v235\n" \
  "v_exp_f32 v236, v236\n" \
  "v_exp_f32 v237, v237\n" \
  "v_mfma_f32_32x32x8_bf16 v[192:207], %[k8b], %[q80], v[192:207]\n" \
  "v_exp_f32 v238, v238\n" \
  "v_exp_f32 v239, v239\n" \
  "v_exp_f32 v240, v240\n" \
  "v_exp_f32 v241, v241\n" \
  "v_smfmac_f32_32x32x32_bf16 %[o2], %[v1a], v[232:239], %[idx]\n" \
  "v_exp_f32 v242, v242\n" \
  "v_exp_f32 v243, v243\n" \
  "v_exp_f32 v244, v244\n" \
  "v_exp_f32 v245, v245\n" \
  "v_mfma_f32_32x32x16_bf16 v[208:223], %[k16b], %[q161], 0\n" \
  "v_exp_f32 v246, v246\n" \
  "v_exp_f32 v247, v247\n" \
  "v_exp_f32 v248, v248\n" \
  "v_exp_f32 v249, v249\n" \
  "v_smfmac_f32_32x32x32_bf16 %[o3], %[v0a], v[240:247], %[idx]\n" \
  "v_exp_f32 v250, v250\n" \
  "v_exp_f32 v251, v251\n" \
  "v_exp_f32 v252, v252\n" \
  "v_exp_f32 v253, v253\n" \
  "v_mfma_f32_32x32x8_bf16 v[208:223], %[k8b], %[q81], v[208:223]\n" \
  "v_exp_f32 v254, v254\n" \
  "v_exp_f32 v255, v255\n" \
  "v_exp_f32 v192, v192\n" \
  "v_exp_f32 v193, v193\n" \
  "v_smfmac_f32_32x32x32_bf16 %[o3], %[v1a], v[248:255], %[idx]\n" \
  "buffer_load_dwordx4 %[v0a], %[vvo], %[rs], %[so] offen offset:1280 sc1\n" \
  "buffer_load_dwordx4 %[v1a], %[vvo], %[rs], %[so] offen offset:2048 sc1\n" \
  "v_exp_f32 v194, v194\n" \
  "v_exp_f32 v195, v195\n" \
  "v_exp_f32 v196, v196\n" \
  "v_exp_f32 v197, v197\n" \
  "v_mfma_f32_32x32x16_bf16 v[224:239], %[k16b], %[q162], 0\n" \
  "v_exp_f32 v198, v198\n" \
  "v_exp_f32 v199, v199\n" \
  "v_exp_f32 v200, v200\n" \
  "v_exp_f32 v201, v201\n" \
  "s_waitcnt vmcnt(4)\n" \
  "v_smfmac_f32_32x32x32_bf16 %[o0], %[v0b], v[192:199], %[idx]\n" \
  "v_exp_f32 v202, v202\n" \
  "v_exp_f32 v203, v203\n" \
  "v_exp_f32 v204, v204\n" \
  "v_exp_f32 v205, v205\n" \
  "v_mfma_f32_32x32x8_bf16 v[224:239], %[k8b], %[q82], v[224:239]\n" \
  "v_exp_f32 v206, v206\n" \
  "v_exp_f32 v207, v207\n" \
  "v_exp_f32 v208, v208\n" \
  "v_exp_f32 v209, v209\n" \
  "v_smfmac_f32_32x32x32_bf16 %[o0], %[v1b], v[200:207], %[idx]\n" \
  "v_exp_f32 v210, v210\n" \
  "v_exp_f32 v211, v211\n" \
  "v_exp_f32 v212, v212\n" \
  "v_exp_f32 v213, v213\n" \
  "v_mfma_f32_32x32x16_bf16 v[240:255], %[k16b], %[q163], 0\n" \
  "v_exp_f32 v214, v214\n" \
  "v_exp_f32 v215, v215\n" \
  "v_exp_f32 v216, v216\n" \
  "v_exp_f32 v217, v217\n" \
  "v_smfmac_f32_32x32x32_bf16 %[o1], %[v0b], v[208:215], %[idx]\n" \
  "v_exp_f32 v218, v218\n" \
  "v_exp_f32 v219, v219\n" \
  "v_exp_f32 v220, v220\n" \
  "v_exp_f32 v221, v221\n" \
  "v_mfma_f32_32x32x8_bf16 v[240:255], %[k8b], %[q83], v[240:255]\n" \
  "v_exp_f32 v222, v222\n" \
  "v_exp_f32 v223, v223\n" \
  "v_exp_f32 v224, v224\n" \
  "v_exp_f32 v225, v225\n" \
  "v_smfmac_f32_32x32x32_bf16 %[o1], %[v1b], v[216:223], %[idx]\n" \
  "v_exp_f32 v226, v226\n" \
  "v_exp_f32 v227, v227\n" \
  "v_exp_f32 v228, v228\n" \
  "v_exp_f32 v229, v229\n" \
  "s_waitcnt vmcnt(2)\n" \
  "v_mfma_f32_32x32x16_bf16 v[192:207], %[k16a], %[q160], 0\n" \
  "buffer_load_dwordx4 %[k16b], %[l16], %[rs], %[so] offen offset:2816 sc1\n" \
  "buffer_load_dwordx2 %[k8b], %[r8], %[rs], %[so] offen offset:3840 sc1\n" \
  "s_add_u32 %[so], %[so], 2816\n" \
  "v_exp_f32 v230, v230\n" \
  "v_exp_f32 v231, v231\n" \
  "v_exp_f32 v232, v232\n" \
  "v_exp_f32 v233, v233\n" \
  "v_smfmac_f32_32x32x32_bf16 %[o2], %[v0b], v[224:231], %[idx]\n" \
  "v_exp_f32 v234, v234\n" \
  "v_exp_f32 v235, v235\n" \
  "v_exp_f32 v236, v236\n" \
  "v_exp_f32 v237, v237\n" \
  "v_mfma_f32_32x32x8_bf16 v[192:207], %[k8a], %[q80], v[192:207]\n" \
  "v_exp_f32 v238, v238\n" \
  "v_exp_f32 v239, v239\n" \
  "v_exp_f32 v240, v240\n" \
  "v_exp_f32 v241, v241\n" \
  "v_smfmac_f32_32x32x32_bf16 %[o2], %[v1b], v[232:239], %[idx]\n" \
  "v_exp_f32 v242, v242\n" \
  "v_exp_f32 v243, v243\n" \
  "v_exp_f32 v244, v244\n" \
  "v_exp_f32 v245, v245\n" \
  "v_mfma_f32_32x32x16_bf16 v[208:223], %[k16a], %[q161], 0\n" \
  "v_exp_f32 v246, v246\n" \
  "v_exp_f32 v247, v247\n" \
  "v_exp_f32 v248, v248\n" \
  "v_exp_f32 v249, v249\n" \
  "v_smfmac_f32_32x32x32_bf16 %[o3], %[v0b], v[240:247], %[idx]\n" \
  "v_exp_f32 v250, v250\n" \
  "v_exp_f32 v251, v251\n" \
  "v_exp_f32 v252, v252\n" \
  "v_exp_f32 v253, v253\n" \
  "v_mfma_f32_32x32x8_bf16 v[208:223], %[k8a], %[q81], v[208:223]\n" \
  "s_add_i32 %[cnt], %[cnt], -1\n" \
  "s_cmp_lg_u32 %[cnt], 0\n" \
  "v_exp_f32 v254, v254\n" \
  "v_exp_f32 v255, v255\n" \
  "s_cbranch_scc1 LATT_loop%=\n" \
  "s_nop 1\n" \
  "v_smfmac_f32_32x32x32_bf16 %[o3], %[v1b], v[248:255], %[idx]\n" \
  "s_waitcnt vmcnt(0)\n" \
  "s_nop 7\n" \
  "s_nop 3\n" \
  ""
#define ATT_CLOB "v192", "v193", "v194", "v195", "v196", "v197", "v198", "v199", "v200", "v201", "v202", "v203", "v204", "v205", "v206", "v207", "v208", "v209", "v210", "v211", "v212", "v213", "v214", "v215", "v216", "v217", "v218", "v219", "v220", "v221", "v222", "v223", "v224", "v225", "v226", "v227", "v228", "v229", "v230", "v231", "v232", "v233", "v234", "v235", "v236", "v237", "v238", "v239", "v240", "v241", "v242", "v243", "v244", "v245", "v246", "v247", "v248", "v249", "v250", "v251", "v252", "v253", "v254", "v255"
#define P3_ASM \
  "s_nop 4\n" \
  "buffer_load_dwordx4 v[64:67], %[va0], %[rs], %[sa] offen offset:0 sc1\n" \
  "buffer_load_dwordx4 v[68:71], %[va0], %[rs], %[sa] offen offset:1024 sc1\n" \
  "buffer_load_dwordx4 v[72:75], %[va0], %[rs], %[sa] offen offset:2048 sc1\n" \
  "buffer_load_dwordx4 v[76:79], %[va0], %[rs], %[sa] offen offset:3072 sc1\n" \
  "s_add_u32 %[sa], %[sa], 4096\n" \
  "buffer_load_dwordx4 v[80:83], %[va0], %[rs], %[sa] offen offset:0 sc1\n" \
  "buffer_load_dwordx4 v[84:87], %[va0], %[rs], %[sa] offen offset:1024 sc1\n" \
  "buffer_load_dwordx4 v[88:91], %[va0], %[rs], %[sa] offen offset:2048 sc1\n" \
  "buffer_load_dwordx4 v[92:95], %[va0], %[rs], %[sa] offen offset:3072 sc1\n" \
  "s_add_u32 %[sa], %[sa], 4096\n" \
  "buffer_load_dwordx4 v[96:99], %[va0], %[rs], %[sa] offen offset:0 sc1\n" \
  "buffer_load_dwordx4 v[100:103], %[va0], %[rs], %[sa] offen offset:1024 sc1\n" \
  "ds_read_b128 v[104:107], %[vw] offset:0\n" \
  "ds_read_b128 v[108:111], %[vw] offset:1024\n" \
  "ds_read_b128 v[112:115], %[vw] offset:2048\n" \
  "ds_read_b128 v[116:119], %[vw] offset:3072\n" \
  "ds_read_b128 v[120:123], %[vw] offset:4096\n" \
  "ds_read_b128 v[124:127], %[vw] offset:10240\n" \
  "ds_read_b128 v[128:131], %[vw] offset:11264\n" \
  "ds_read_b128 v[132:135], %[vw] offset:12288\n" \
  "s_waitcnt vmcnt(9)\n" \
  "ds_read_b128 v[136:139], %[vw] offset:13312\n" \
  "s_waitcnt lgkmcnt(8)\n" \
  "v_mfma_f32_16x16x32_bf16 %[c0], v[104:107], v[64:67], 0\n" \
  "ds_read_b128 v[140:143], %[vw] offset:14336\n" \
  "s_waitcnt lgkmcnt(8)\n" \
  "v_mfma_f32_16x16x32_bf16 %[c1], v[108:111], v[64:67], 0\n" \
  "ds_read_b128 v[104:107], %[vw] offset:20480\n" \
  "s_waitcnt lgkmcnt(8)\n" \
  "v_mfma_f32_16x16x32_bf16 %[c2], v[112:115], v[64:67], 0\n" \
  "ds_read_b128 v[108:111], %[vw] offset:21504\n" \
  "s_waitcnt lgkmcnt(8)\n" \
  "v_mfma_f32_16x16x32_bf16 %[c3], v[116:119], v[64:67], 0\n" \
  "ds_read_b128 v[112:115], %[vw] offset:22528\n" \
  "s_waitcnt lgkmcnt(8)\n" \
  "v_mfma_f32_16x16x32_bf16 %[c4], v[120:123], v[64:67], 0\n" \
  "s_waitcnt vmcnt(8)\n" \
  "ds_read_b128 v[116:119], %[vw] offset:23552\n" \
  "s_waitcnt lgkmcnt(8)\n" \
  "v_mfma_f32_16x16x32_bf16 %[c0], v[124:127], v[68:71], %[c0]\n" \
  "ds_read_b128 v[120:123], %[vw] offset:24576\n" \
  "s_waitcnt lgkmcnt(8)\n" \
  "v_mfma_f32_16x16x32_bf16 %[c1], v[128:131], v[68:71], %[c1]\n" \
  "ds_read_b128 v[124:127], %[vw] offset:30720\n" \
  "s_waitcnt lgkmcnt(8)\n" \
  "v_mfma_f32_16x16x32_bf16 %[c2], v[132:135], v[68:71], %[c2]\n" \
  "ds_read_b128 v[128:131], %[vw] offset:31744\n" \
  "s_waitcnt lgkmcnt(8)\n" \
  "v_mfma_f32_16x16x32_bf16 %[c3], v[136:139], v[68:71], %[c3]\n" \
  "ds_read_b128 v[132:135], %[vw] offset:32768\n" \
  "s_waitcnt lgkmcnt(8)\n" \
  "v_mfma_f32_16x16x32_bf16 %[c4], v[140:143], v[68:71], %[c4]\n" \
  "s_waitcnt vmcnt(7)\n" \
  "ds_read_b128 v[136:139], %[vw] offset:33792\n" \
  "s_waitcnt lgkmcnt(8)\n" \
  "v_mfma_f32_16x16x32_bf16 %[c0], v[104:107], v[72:75], %[c0]\n" \
  "ds_read_b128 v[140:143], %[vw] offset:34816\n" \
  "s_waitcnt lgkmcnt(8)\n" \
  "v_mfma_f32_16x16x32_bf16 %[c1], v[108:111], v[72:75], %[c1]\n" \
  "ds_read_b128 v[104:107], %[vw] offset:40960\n" \
  "s_waitcnt lgkmcnt(8)\n" \
  "v_mfma_f32_16x16x32_bf16 %[c2], v[112:115], v[72:75], %[c2]\n" \
  "ds_read_b128 v[108:111], %[vw] offset:41984\n" \
  "s_waitcnt lgkmcnt(8)\n" \
  "v_mfma_f32_16x16x32_bf16 %[c3], v[116:119], v[72:75], %[c3]\n" \
  "ds_read_b128 v[112:115], %[vw] offset:43008\n" \
  "s_waitcnt lgkmcnt(8)\n" \
  "v_mfma_f32_16x16x32_bf16 %[c4], v[120:123], v[72:75], %[c4]\n" \
  "s_waitcnt vmcnt(6)\n" \
  "ds_read_b128 v[116:119], %[vw] offset:44032\n" \
  "s_waitcnt lgkmcnt(8)\n" \
  "v_mfma_f32_16x16x32_bf16 %[c0], v[124:127], v[76:79], %[c0]\n" \
  "ds_read_b128 v[120:123], %[vw] offset:45056\n" \
  "s_waitcnt lgkmcnt(8)\n" \
  "v_mfma_f32_16x16x32_bf16 %[c1], v[128:131], v[76:79], %[c1]\n" \
  "ds_read_b128 v[124:127], %[vw] offset:51200\n" \
  "s_waitcnt lgkmcnt(8)\n" \
  "v_mfma_f32_16x16x32_bf16 %[c2], v[132:135], v[76:79], %[c2]\n" \
  "ds_read_b128 v[128:131], %[vw] offset:52224\n" \
  "s_waitcnt lgkmcnt(8)\n" \
  "v_mfma_f32_16x16x32_bf16 %[c3], v[136:139], v[76:79], %[c3]\n" \
  "ds_read_b128 v[132:135], %[vw] offset:53248\n" \
  "s_waitcnt lgkmcnt(8)\n" \
  "v_mfma_f32_16x16x32_bf16 %[c4], v[140:143], v[76:79], %[c4]\n" \
  "s_barrier\n" \
  "s_mov_b32 m0, %[dm0]\n" \
  "s_nop 0\n" \
  "buffer_load_dwordx4 %[l16], %[rs], %[ds0] offen offset:0 sc1 lds\n" \
  "buffer_load_dwordx4 %[l16], %[rs], %[ds0] offen offset:1024 sc1 lds\n" \
  "s_waitcnt vmcnt(7)\n" \
  "ds_read_b128 v[136:139], %[vw] offset:54272\n" \
  "s_waitcnt lgkmcnt(8)\n" \
  "v_mfma_f32_16x16x32_bf16 %[c0], v[104:107], v[80:83], %[c0]\n" \
  "ds_read_b128 v[140:143], %[vw] offset:55296\n" \
  "s_waitcnt lgkmcnt(8)\n" \
  "v_mfma_f32_16x16x32_bf16 %[c1], v[108:111], v[80:83], %[c1]\n" \
  "ds_read_b128 v[104:107], %[vw] offset:61440\n" \
  "s_waitcnt lgkmcnt(8)\n" \
  "v_mfma_f32_16x16x32_bf16 %[c2], v[112:115], v[80:83], %[c2]\n" \
  "ds_read_b128 v[108:111], %[vw] offset:62464\n" \
  "s_waitcnt lgkmcnt(8)\n" \
  "v_mfma_f32_16x16x32_bf16 %[c3], v[116:119], v[80:83], %[c3]\n" \
  "ds_read_b128 v[112:115], %[vw] offset:63488\n" \
  "s_waitcnt lgkmcnt(8)\n" \
  "v_mfma_f32_16x16x32_bf16 %[c4], v[120:123], v[80:83], %[c4]\n" \
  "s_waitcnt vmcnt(6)\n" \
  "ds_read_b128 v[116:119], %[vw] offset:64512\n" \
  "s_waitcnt lgkmcnt(8)\n" \
  "v_mfma_f32_16x16x32_bf16 %[c0], v[124:127], v[84:87], %[c0]\n" \
  "v_add_u32 %[vw], 65536, %[vw]\n" \
  "ds_read_b128 v[120:123], %[vw] offset:0\n" \
  "s_waitcnt lgkmcnt(8)\n" \
  "v_mfma_f32_16x16x32_bf16 %[c1], v[128:131], v[84:87], %[c1]\n" \
  "ds_read_b128 v[124:127], %[vw] offset:6144\n" \
  "s_waitcnt lgkmcnt(8)\n" \
  "v_mfma_f32_16x16x32_bf16 %[c2], v[132:135], v[84:87], %[c2]\n" \
  "ds_read_b128 v[128:131], %[vw] offset:7168\n" \
  "s_waitcnt lgkmcnt(8)\n" \
  "v_mfma_f32_16x16x32_bf16 %[c3], v[136:139], v[84:87], %[c3]\n" \
  "ds_read_b128 v[132:135], %[vw] offset:8192\n" \
  "s_waitcnt lgkmcnt(8)\n" \
  "v_mfma_f32_16x16x32_bf16 %[c4], v[140:143], v[84:87], %[c4]\n" \
  "s_waitcnt vmcnt(5)\n" \
  "ds_read_b128 v[136:139], %[vw] offset:9216\n" \
  "s_waitcnt lgkmcnt(8)\n" \
  "v_mfma_f32_16x16x32_bf16 %[c0], v[104:107], v[88:91], %[c0]\n" \
  "ds_read_b128 v[140:143], %[vw] offset:10240\n" \
  "s_waitcnt lgkmcnt(8)\n" \
  "v_mfma_f32_16x16x32_bf16 %[c1], v[108:111], v[88:91], %[c1]\n" \
  "ds_read_b128 v[104:107], %[vw] offset:16384\n" \
  "s_waitcnt lgkmcnt(8)\n" \
  "v_mfma_f32_16x16x32_bf16 %[c2], v[112:115], v[88:91], %[c2]\n" \
  "ds_read_b128 v[108:111], %[vw] offset:17408\n" \
  "s_waitcnt lgkmcnt(8)\n" \
  "v_mfma_f32_16x16x32_bf16 %[c3], v[116:119], v[88:91], %[c3]\n" \
  "ds_read_b128 v[112:115], %[vw] offset:18432\n" \
  "s_waitcnt lgkmcnt(8)\n" \
  "v_mfma_f32_16x16x32_bf16 %[c4], v[120:123], v[88:91], %[c4]\n" \
  "s_waitcnt vmcnt(4)\n" \
  "ds_read_b128 v[116:119], %[vw] offset:19456\n" \
  "s_waitcnt lgkmcnt(8)\n" \
  "v_mfma_f32_16x16x32_bf16 %[c0], v[124:127], v[92:95], %[c0]\n" \
  "ds_read_b128 v[120:123], %[vw] offset:20480\n" \
  "s_waitcnt lgkmcnt(8)\n" \
  "v_mfma_f32_16x16x32_bf16 %[c1], v[128:131], v[92:95], %[c1]\n" \
  "ds_read_b128 v[124:127], %[vw] offset:26624\n" \
  "s_waitcnt lgkmcnt(8)\n" \
  "v_mfma_f32_16x16x32_bf16 %[c2], v[132:135], v[92:95], %[c2]\n" \
  "ds_read_b128 v[128:131], %[vw] offset:27648\n" \
  "s_waitcnt lgkmcnt(8)\n" \
  "v_mfma_f32_16x16x32_bf16 %[c3], v[136:139], v[92:95], %[c3]\n" \
  "ds_read_b128 v[132:135], %[vw] offset:28672\n" \
  "s_waitcnt lgkmcnt(8)\n" \
  "v_mfma_f32_16x16x32_bf16 %[c4], v[140:143], v[92:95], %[c4]\n" \
  "s_waitcnt vmcnt(3)\n" \
  "ds_read_b128 v[136:139], %[vw] offset:29696\n" \
  "s_waitcnt lgkmcnt(8)\n" \
  "v_mfma_f32_16x16x32_bf16 %[c0], v[104:107], v[96:99], %[c0]\n" \
  "ds_read_b128 v[140:143], %[vw] offset:30720\n" \
  "s_waitcnt lgkmcnt(8)\n" \
  "v_mfma_f32_16x16x32_bf16 %[c1], v[108:111], v[96:99], %[c1]\n" \
  "s_waitcnt lgkmcnt(7)\n" \
  "v_mfma_f32_16x16x32_bf16 %[c2], v[112:115], v[96:99], %[c2]\n" \
  "s_waitcnt lgkmcnt(6)\n" \
  "v_mfma_f32_16x16x32_bf16 %[c3], v[116:119], v[96:99], %[c3]\n" \
  "s_waitcnt lgkmcnt(5)\n" \
  "v_mfma_f32_16x16x32_bf16 %[c4], v[120:123], v[96:99], %[c4]\n" \
  "s_waitcnt vmcnt(2)\n" \
  "s_waitcnt lgkmcnt(4)\n" \
  "v_mfma_f32_16x16x32_bf16 %[c0], v[124:127], v[100:103], %[c0]\n" \
  "s_waitcnt lgkmcnt(3)\n" \
  "v_mfma_f32_16x16x32_bf16 %[c1], v[128:131], v[100:103], %[c1]\n" \
  "s_waitcnt lgkmcnt(2)\n" \
  "v_mfma_f32_16x16x32_bf16 %[c2], v[132:135], v[100:103], %[c2]\n" \
  "s_waitcnt lgkmcnt(1)\n" \
  "v_mfma_f32_16x16x32_bf16 %[c3], v[136:139], v[100:103], %[c3]\n" \
  "s_waitcnt lgkmcnt(0)\n" \
  "v_mfma_f32_16x16x32_bf16 %[c4], v[140:143], v[100:103], %[c4]\n" \
  "s_nop 7\n" \
  ""
#define P4_ASM \
  "s_nop 4\n" \
  "buffer_load_dwordx4 v[64:67], %[va0], %[rs], %[sa] offen offset:0 sc1\n" \
  "buffer_load_dwordx4 v[68:71], %[va0], %[rs], %[sa] offen offset:1024 sc1\n" \
  "buffer_load_dwordx4 v[72:75], %[va0], %[rs], %[sa] offen offset:2048 sc1\n" \
  "buffer_load_dwordx4 v[76:79], %[va0], %[rs], %[sa] offen offset:3072 sc1\n" \
  "s_add_u32 %[sa], %[sa], 4096\n" \
  "buffer_load_dwordx4 v[80:83], %[va0], %[rs], %[sa] offen offset:0 sc1\n" \
  "buffer_load_dwordx4 v[84:87], %[va0], %[rs], %[sa] offen offset:1024 sc1\n" \
  "buffer_load_dwordx4 v[88:91], %[va0], %[rs], %[sa] offen offset:2048 sc1\n" \
  "buffer_load_dwordx4 v[92:95], %[va0], %[rs], %[sa] offen offset:3072 sc1\n" \
  "s_add_u32 %[sa], %[sa], 4096\n" \
  "buffer_load_dwordx4 v[96:99], %[va0], %[rs], %[sa] offen offset:0 sc1\n" \
  "buffer_load_dwordx4 v[100:103], %[va0], %[rs], %[sa] offen offset:1024 sc1\n" \
  "buffer_load_dwordx2 %[st0], %[vst], %[rs], 0 offen offset:0 sc1\n" \
  "buffer_load_dwordx2 %[st1], %[vst], %[rs], 0 offen offset:128 sc1\n" \
  "buffer_load_dwordx2 %[st2], %[vst], %[rs], 0 offen offset:256 sc1\n" \
  "buffer_load_dwordx2 %[st3], %[vst], %[rs], 0 offen offset:384 sc1\n" \
  "v_add_u32 %[vw], 98304, %[vw]\n" \
  "ds_read_b128 v[104:107], %[vw] offset:4096\n" \
  "ds_read_b128 v[108:111], %[vw] offset:5120\n" \
  "ds_read_b128 v[112:115], %[vw] offset:6144\n" \
  "ds_read_b128 v[116:119], %[vw] offset:7168\n" \
  "ds_read_b128 v[120:123], %[vw] offset:8192\n" \
  "ds_read_b128 v[124:127], %[vw] offset:14336\n" \
  "ds_read_b128 v[128:131], %[vw] offset:15360\n" \
  "ds_read_b128 v[132:135], %[vw] offset:16384\n" \
  "s_waitcnt vmcnt(13)\n" \
  "ds_read_b128 v[136:139], %[vw] offset:17408\n" \
  "s_waitcnt lgkmcnt(8)\n" \
  "v_mfma_f32_16x16x32_bf16 %[c0], v[104:107], v[64:67], 0\n" \
  "ds_read_b128 v[140:143], %[vw] offset:18432\n" \
  "s_waitcnt lgkmcnt(8)\n" \
  "v_mfma_f32_16x16x32_bf16 %[c1], v[108:111], v[64:67], 0\n" \
  "ds_read_b128 v[104:107], %[vw] offset:24576\n" \
  "s_waitcnt lgkmcnt(8)\n" \
  "v_mfma_f32_16x16x32_bf16 %[c2], v[112:115], v[64:67], 0\n" \
  "ds_read_b128 v[108:111], %[vw] offset:25600\n" \
  "s_waitcnt lgkmcnt(8)\n" \
  "v_mfma_f32_16x16x32_bf16 %[c3], v[116:119], v[64:67], 0\n" \
  "ds_read_b128 v[112:115], %[vw] offset:26624\n" \
  "s_waitcnt lgkmcnt(8)\n" \
  "v_mfma_f32_16x16x32_bf16 %[c4], v[120:123], v[64:67], 0\n" \
  "s_waitcnt vmcnt(12)\n" \
  "ds_read_b128 v[116:119], %[vw] offset:27648\n" \
  "s_waitcnt lgkmcnt(8)\n" \
  "v_mfma_f32_16x16x32_bf16 %[c0], v[124:127], v[68:71], %[c0]\n" \
  "ds_read_b128 v[120:123], %[vw] offset:28672\n" \
  "s_waitcnt lgkmcnt(8)\n" \
  "v_mfma_f32_16x16x32_bf16 %[c1], v[128:131], v[68:71], %[c1]\n" \
  "ds_read_b128 v[124:127], %[vw] offset:34816\n" \
  "s_waitcnt lgkmcnt(8)\n" \
  "v_mfma_f32_16x16x32_bf16 %[c2], v[132:135], v[68:71], %[c2]\n" \
  "ds_read_b128 v[128:131], %[vw] offset:35840\n" \
  "s_waitcnt lgkmcnt(8)\n" \
  "v_mfma_f32_16x16x32_bf16 %[c3], v[136:139], v[68:71], %[c3]\n" \
  "ds_read_b128 v[132:135], %[vw] offset:36864\n" \
  "s_waitcnt lgkmcnt(8)\n" \
  "v_mfma_f32_16x16x32_bf16 %[c4], v[140:143], v[68:71], %[c4]\n" \
  "s_waitcnt vmcnt(11)\n" \
  "ds_read_b128 v[136:139], %[vw] offset:37888\n" \
  "s_waitcnt lgkmcnt(8)\n" \
  "v_mfma_f32_16x16x32_bf16 %[c0], v[104:107], v[72:75], %[c0]\n" \
  "ds_read_b128 v[140:143], %[vw] offset:38912\n" \
  "s_waitcnt lgkmcnt(8)\n" \
  "v_mfma_f32_16x16x32_bf16 %[c1], v[108:111], v[72:75], %[c1]\n" \
  "ds_read_b128 v[104:107], %[vw] offset:45056\n" \
  "s_waitcnt lgkmcnt(8)\n" \
  "v_mfma_f32_16x16x32_bf16 %[c2], v[112:115], v[72:75], %[c2]\n" \
  "ds_read_b128 v[108:111], %[vw] offset:46080\n" \
  "s_waitcnt lgkmcnt(8)\n" \
  "v_mfma_f32_16x16x32_bf16 %[c3], v[116:119], v[72:75], %[c3]\n" \
  "ds_read_b128 v[112:115], %[vw] offset:47104\n" \
  "s_waitcnt lgkmcnt(8)\n" \
  "v_mfma_f32_16x16x32_bf16 %[c4], v[120:123], v[72:75], %[c4]\n" \
  "s_waitcnt vmcnt(10)\n" \
  "ds_read_b128 v[116:119], %[vw] offset:48128\n" \
  "s_waitcnt lgkmcnt(8)\n" \
  "v_mfma_f32_16x16x32_bf16 %[c0], v[124:127], v[76:79], %[c0]\n" \
  "ds_read_b128 v[120:123], %[vw] offset:49152\n" \
  "s_waitcnt lgkmcnt(8)\n" \
  "v_mfma_f32_16x16x32_bf16 %[c1], v[128:131], v[76:79], %[c1]\n" \
  "v_add_u32 %[vw], 4294868992, %[vw]\n" \
  "ds_read_b128 v[124:127], %[vw] offset:0\n" \
  "s_waitcnt lgkmcnt(8)\n" \
  "v_mfma_f32_16x16x32_bf16 %[c2], v[132:135], v[76:79], %[c2]\n" \
  "ds_read_b128 v[128:131], %[vw] offset:1024\n" \
  "s_waitcnt lgkmcnt(8)\n" \
  "v_mfma_f32_16x16x32_bf16 %[c3], v[136:139], v[76:79], %[c3]\n" \
  "ds_read_b128 v[132:135], %[vw] offset:2048\n" \
  "s_waitcnt lgkmcnt(8)\n" \
  "v_mfma_f32_16x16x32_bf16 %[c4], v[140:143], v[76:79], %[c4]\n" \
  "s_barrier\n" \
  "s_mov_b32 m0, %[dm0]\n" \
  "s_nop 0\n" \
  "buffer_load_dwordx4 %[l16], %[rs], %[ds0] offen offset:0 sc1 lds\n" \
  "buffer_load_dwordx4 %[l16], %[rs], %[ds0] offen offset:1024 sc1 lds\n" \
  "s_waitcnt vmcnt(11)\n" \
  "ds_read_b128 v[136:139], %[vw] offset:3072\n" \
  "s_waitcnt lgkmcnt(8)\n" \
  "v_mfma_f32_16x16x32_bf16 %[c0], v[104:107], v[80:83], %[c0]\n" \
  "ds_read_b128 v[140:143], %[vw] offset:4096\n" \
  "s_waitcnt lgkmcnt(8)\n" \
  "v_mfma_f32_16x16x32_bf16 %[c1], v[108:111], v[80:83], %[c1]\n" \
  "ds_read_b128 v[104:107], %[vw] offset:10240\n" \
  "s_waitcnt lgkmcnt(8)\n" \
  "v_mfma_f32_16x16x32_bf16 %[c2], v[112:115], v[80:83], %[c2]\n" \
  "ds_read_b128 v[108:111], %[vw] offset:11264\n" \
  "s_waitcnt lgkmcnt(8)\n" \
  "v_mfma_f32_16x16x32_bf16 %[c3], v[116:119], v[80:83], %[c3]\n" \
  "ds_read_b128 v[112:115], %[vw] offset:12288\n" \
  "s_waitcnt lgkmcnt(8)\n" \
  "v_mfma_f32_16x16x32_bf16 %[c4], v[120:123], v[80:83], %[c4]\n" \
  "s_waitcnt vmcnt(10)\n" \
  "ds_read_b128 v[116:119], %[vw] offset:13312\n" \
  "s_waitcnt lgkmcnt(8)\n" \
  "v_mfma_f32_16x16x32_bf16 %[c0], v[124:127], v[84:87], %[c0]\n" \
  "ds_read_b128 v[120:123], %[vw] offset:14336\n" \
  "s_waitcnt lgkmcnt(8)\n" \
  "v_mfma_f32_16x16x32_bf16 %[c1], v[128:131], v[84:87], %[c1]\n" \
  "ds_read_b128 v[124:127], %[vw] offset:20480\n" \
  "s_waitcnt lgkmcnt(8)\n" \
  "v_mfma_f32_16x16x32_bf16 %[c2], v[132:135], v[84:87], %[c2]\n" \
  "ds_read_b128 v[128:131], %[vw] offset:21504\n" \
  "s_waitcnt lgkmcnt(8)\n" \
  "v_mfma_f32_16x16x32_bf16 %[c3], v[136:139], v[84:87], %[c3]\n" \
  "ds_read_b128 v[132:135], %[vw] offset:22528\n" \
  "s_waitcnt lgkmcnt(8)\n" \
  "v_mfma_f32_16x16x32_bf16 %[c4], v[140:143], v[84:87], %[c4]\n" \
  "s_waitcnt vmcnt(9)\n" \
  "ds_read_b128 v[136:139], %[vw] offset:23552\n" \
  "s_waitcnt lgkmcnt(8)\n" \
  "v_mfma_f32_16x16x32_bf16 %[c0], v[104:107], v[88:91], %[c0]\n" \
  "ds_read_b128 v[140:143], %[vw] offset:24576\n" \
  "s_waitcnt lgkmcnt(8)\n" \
  "v_mfma_f32_16x16x32_bf16 %[c1], v[108:111], v[88:91], %[c1]\n" \
  "ds_read_b128 v[104:107], %[vw] offset:30720\n" \
  "s_waitcnt lgkmcnt(8)\n" \
  "v_mfma_f32_16x16x32_bf16 %[c2], v[112:115], v[88:91], %[c2]\n" \
  "ds_read_b128 v[108:111], %[vw] offset:31744\n" \
  "s_waitcnt lgkmcnt(8)\n" \
  "v_mfma_f32_16x16x32_bf16 %[c3], v[116:119], v[88:91], %[c3]\n" \
  "ds_read_b128 v[112:115], %[vw] offset:32768\n" \
  "s_waitcnt lgkmcnt(8)\n" \
  "v_mfma_f32_16x16x32_bf16 %[c4], v[120:123], v[88:91], %[c4]\n" \
  "s_waitcnt vmcnt(8)\n" \
  "ds_read_b128 v[116:119], %[vw] offset:33792\n" \
  "s_waitcnt lgkmcnt(8)\n" \
  "v_mfma_f32_16x16x32_bf16 %[c0], v[124:127], v[92:95], %[c0]\n" \
  "ds_read_b128 v[120:123], %[vw] offset:34816\n" \
  "s_waitcnt lgkmcnt(8)\n" \
  "v_mfma_f32_16x16x32_bf16 %[c1], v[128:131], v[92:95], %[c1]\n" \
  "ds_read_b128 v[124:127], %[vw] offset:40960\n" \
  "s_waitcnt lgkmcnt(8)\n" \
  "v_mfma_f32_16x16x32_bf16 %[c2], v[132:135], v[92:95], %[c2]\n" \
  "ds_read_b128 v[128:131], %[vw] offset:41984\n" \
  "s_waitcnt lgkmcnt(8)\n" \
  "v_mfma_f32_16x16x32_bf16 %[c3], v[136:139], v[92:95], %[c3]\n" \
  "ds_read_b128 v[132:135], %[vw] offset:43008\n" \
  "s_waitcnt lgkmcnt(8)\n" \
  "v_mfma_f32_16x16x32_bf16 %[c4], v[140:143], v[92:95], %[c4]\n" \
  "s_waitcnt vmcnt(7)\n" \
  "ds_read_b128 v[136:139], %[vw] offset:44032\n" \
  "s_waitcnt lgkmcnt(8)\n" \
  "v_mfma_f32_16x16x32_bf16 %[c0], v[104:107], v[96:99], %[c0]\n" \
  "ds_read_b128 v[140:143], %[vw] offset:45056\n" \
  "s_waitcnt lgkmcnt(8)\n" \
  "v_mfma_f32_16x16x32_bf16 %[c1], v[108:111], v[96:99], %[c1]\n" \
  "s_waitcnt lgkmcnt(7)\n" \
  "v_mfma_f32_16x16x32_bf16 %[c2], v[112:115], v[96:99], %[c2]\n" \
  "s_waitcnt lgkmcnt(6)\n" \
  "v_mfma_f32_16x16x32_bf16 %[c3], v[116:119], v[96:99], %[c3]\n" \
  "s_waitcnt lgkmcnt(5)\n" \
  "v_mfma_f32_16x16x32_bf16 %[c4], v[120:123], v[96:99], %[c4]\n" \
  "s_waitcnt vmcnt(6)\n" \
  "s_waitcnt lgkmcnt(4)\n" \
  "v_mfma_f32_16x16x32_bf16 %[c0], v[124:127], v[100:103], %[c0]\n" \
  "s_waitcnt lgkmcnt(3)\n" \
  "v_mfma_f32_16x16x32_bf16 %[c1], v[128:131], v[100:103], %[c1]\n" \
  "s_waitcnt lgkmcnt(2)\n" \
  "v_mfma_f32_16x16x32_bf16 %[c2], v[132:135], v[100:103], %[c2]\n" \
  "s_waitcnt lgkmcnt(1)\n" \
  "v_mfma_f32_16x16x32_bf16 %[c3], v[136:139], v[100:103], %[c3]\n" \
  "s_waitcnt lgkmcnt(0)\n" \
  "v_mfma_f32_16x16x32_bf16 %[c4], v[140:143], v[100:103], %[c4]\n" \
  "s_waitcnt vmcnt(2)\n" \
  "s_nop 7\n" \
  ""
#define P5_ASM \
  "s_nop 4\n" \
  "buffer_load_dwordx4 v[64:67], %[va0], %[rs], %[sa] offen offset:0 sc1\n" \
  "buffer_load_dwordx4 v[68:71], %[va0], %[rs], %[sa] offen offset:1024 sc1\n" \
  "buffer_load_dwordx4 v[72:75], %[va0], %[rs], %[sa] offen offset:2048 sc1\n" \
  "buffer_load_dwordx4 v[76:79], %[va0], %[rs], %[sa] offen offset:3072 sc1\n" \
  "s_add_u32 %[sa], %[sa], 4096\n" \
  "buffer_load_dwordx4 v[80:83], %[va0], %[rs], %[sa] offen offset:0 sc1\n" \
  "buffer_load_dwordx4 v[84:87], %[va1], %[rs], %[sa] offen offset:1024 sc1\n" \
  "buffer_load_dwordx4 v[88:91], %[va1], %[rs], %[sa] offen offset:2048 sc1\n" \
  "buffer_load_dwordx4 v[92:95], %[va1], %[rs], %[sa] offen offset:3072 sc1\n" \
  "s_add_u32 %[sa], %[sa], 4096\n" \
  "buffer_load_dwordx4 v[96:99], %[va1], %[rs], %[sa] offen offset:0 sc1\n" \
  "buffer_load_dwordx4 v[100:103], %[va1], %[rs], %[sa] offen offset:1024 sc1\n" \
  "ds_read_b128 v[104:107], %[vw] offset:51200\n" \
  "ds_read_b128 v[108:111], %[vw] offset:52224\n" \
  "ds_read_b128 v[112:115], %[vw] offset:53248\n" \
  "ds_read_b128 v[116:119], %[vw] offset:54272\n" \
  "ds_read_b128 v[120:123], %[vw] offset:55296\n" \
  "ds_read_b128 v[124:127], %[vw] offset:61440\n" \
  "ds_read_b128 v[128:131], %[vw] offset:62464\n" \
  "ds_read_b128 v[132:135], %[vw] offset:63488\n" \
  "s_waitcnt vmcnt(9)\n" \
  "ds_read_b128 v[136:139], %[vw] offset:64512\n" \
  "s_waitcnt lgkmcnt(8)\n" \
  "v_mfma_f32_16x16x32_bf16 %[c0], v[104:107], v[64:67], 0\n" \
  "v_add_u32 %[vw], 65536, %[vw]\n" \
  "ds_read_b128 v[140:143], %[vw] offset:0\n" \
  "s_waitcnt lgkmcnt(8)\n" \
  "v_mfma_f32_16x16x32_bf16 %[c1], v[108:111], v[64:67], 0\n" \
  "ds_read_b128 v[104:107], %[vw] offset:6144\n" \
  "s_waitcnt lgkmcnt(8)\n" \
  "v_mfma_f32_16x16x32_bf16 %[c2], v[112:115], v[64:67], 0\n" \
  "ds_read_b128 v[108:111], %[vw] offset:7168\n" \
  "s_waitcnt lgkmcnt(8)\n" \
  "v_mfma_f32_16x16x32_bf16 %[c3], v[116:119], v[64:67], 0\n" \
  "ds_read_b128 v[112:115], %[vw] offset:8192\n" \
  "s_waitcnt lgkmcnt(8)\n" \
  "v_mfma_f32_16x16x32_bf16 %[c4], v[120:123], v[64:67], 0\n" \
  "s_waitcnt vmcnt(8)\n" \
  "ds_read_b128 v[116:119], %[vw] offset:9216\n" \
  "s_waitcnt lgkmcnt(8)\n" \
  "v_mfma_f32_16x16x32_bf16 %[c0], v[124:127], v[68:71], %[c0]\n" \
  "ds_read_b128 v[120:123], %[vw] offset:10240\n" \
  "s_waitcnt lgkmcnt(8)\n" \
  "v_mfma_f32_16x16x32_bf16 %[c1], v[128:131], v[68:71], %[c1]\n" \
  "ds_read_b128 v[124:127], %[vw] offset:16384\n" \
  "s_waitcnt lgkmcnt(8)\n" \
  "v_mfma_f32_16x16x32_bf16 %[c2], v[132:135], v[68:71], %[c2]\n" \
  "ds_read_b128 v[128:131], %[vw] offset:17408\n" \
  "s_waitcnt lgkmcnt(8)\n" \
  "v_mfma_f32_16x16x32_bf16 %[c3], v[136:139], v[68:71], %[c3]\n" \
  "ds_read_b128 v[132:135], %[vw] offset:18432\n" \
  "s_waitcnt lgkmcnt(8)\n" \
  "v_mfma_f32_16x16x32_bf16 %[c4], v[140:143], v[68:71], %[c4]\n" \
  "s_waitcnt vmcnt(7)\n" \
  "ds_read_b128 v[136:139], %[vw] offset:19456\n" \
  "s_waitcnt lgkmcnt(8)\n" \
  "v_mfma_f32_16x16x32_bf16 %[c0], v[104:107], v[72:75], %[c0]\n" \
  "ds_read_b128 v[140:143], %[vw] offset:20480\n" \
  "s_waitcnt lgkmcnt(8)\n" \
  "v_mfma_f32_16x16x32_bf16 %[c1], v[108:111], v[72:75], %[c1]\n" \
  "ds_read_b128 v[104:107], %[vw] offset:26624\n" \
  "s_waitcnt lgkmcnt(8)\n" \
  "v_mfma_f32_16x16x32_bf16 %[c2], v[112:115], v[72:75], %[c2]\n" \
  "ds_read_b128 v[108:111], %[vw] offset:27648\n" \
  "s_waitcnt lgkmcnt(8)\n" \
  "v_mfma_f32_16x16x32_bf16 %[c3], v[116:119], v[72:75], %[c3]\n" \
  "ds_read_b128 v[112:115], %[vw] offset:28672\n" \
  "s_waitcnt lgkmcnt(8)\n" \
  "v_mfma_f32_16x16x32_bf16 %[c4], v[120:123], v[72:75], %[c4]\n" \
  "s_waitcnt vmcnt(6)\n" \
  "ds_read_b128 v[116:119], %[vw] offset:29696\n" \
  "s_waitcnt lgkmcnt(8)\n" \
  "v_mfma_f32_16x16x32_bf16 %[c0], v[124:127], v[76:79], %[c0]\n" \
  "ds_read_b128 v[120:123], %[vw] offset:30720\n" \
  "s_waitcnt lgkmcnt(8)\n" \
  "v_mfma_f32_16x16x32_bf16 %[c1], v[128:131], v[76:79], %[c1]\n" \
  "ds_read_b128 v[124:127], %[vw] offset:36864\n" \
  "s_waitcnt lgkmcnt(8)\n" \
  "v_mfma_f32_16x16x32_bf16 %[c2], v[132:135], v[76:79], %[c2]\n" \
  "ds_read_b128 v[128:131], %[vw] offset:37888\n" \
  "s_waitcnt lgkmcnt(8)\n" \
  "v_mfma_f32_16x16x32_bf16 %[c3], v[136:139], v[76:79], %[c3]\n" \
  "ds_read_b128 v[132:135], %[vw] offset:38912\n" \
  "s_waitcnt lgkmcnt(8)\n" \
  "v_mfma_f32_16x16x32_bf16 %[c4], v[140:143], v[76:79], %[c4]\n" \
  "s_barrier\n" \
  "s_mov_b32 m0, %[dm0]\n" \
  "s_nop 0\n" \
  "buffer_load_dwordx4 %[l16], %[rs], %[ds0] offen offset:0 sc1 lds\n" \
  "buffer_load_dwordx4 %[l16], %[rs], %[ds0] offen offset:1024 sc1 lds\n" \
  "s_waitcnt vmcnt(7)\n" \
  "ds_read_b128 v[136:139], %[vw] offset:39936\n" \
  "s_waitcnt lgkmcnt(8)\n" \
  "v_mfma_f32_16x16x32_bf16 %[c0], v[104:107], v[80:83], %[c0]\n" \
  "ds_read_b128 v[140:143], %[vw] offset:40960\n" \
  "s_waitcnt lgkmcnt(8)\n" \
  "v_mfma_f32_16x16x32_bf16 %[c1], v[108:111], v[80:83], %[c1]\n" \
  "ds_read_b128 v[104:107], %[vw] offset:47104\n" \
  "s_waitcnt lgkmcnt(8)\n" \
  "v_mfma_f32_16x16x32_bf16 %[c2], v[112:115], v[80:83], %[c2]\n" \
  "ds_read_b128 v[108:111], %[vw] offset:48128\n" \
  "s_waitcnt lgkmcnt(8)\n" \
  "v_mfma_f32_16x16x32_bf16 %[c3], v[116:119], v[80:83], %[c3]\n" \
  "ds_read_b128 v[112:115], %[vw] offset:49152\n" \
  "s_waitcnt lgkmcnt(8)\n" \
  "v_mfma_f32_16x16x32_bf16 %[c4], v[120:123], v[80:83], %[c4]\n" \
  "s_waitcnt vmcnt(6)\n" \
  "ds_read_b128 v[116:119], %[vw] offset:50176\n" \
  "s_waitcnt lgkmcnt(8)\n" \
  "v_mfma_f32_16x16x32_bf16 %[c0], v[124:127], v[84:87], %[c0]\n" \
  "ds_read_b128 v[120:123], %[vw] offset:51200\n" \
  "s_waitcnt lgkmcnt(8)\n" \
  "v_mfma_f32_16x16x32_bf16 %[c1], v[128:131], v[84:87], %[c1]\n" \
  "ds_read_b128 v[124:127], %[vw] offset:57344\n" \
  "s_waitcnt lgkmcnt(8)\n" \
  "v_mfma_f32_16x16x32_bf16 %[c2], v[132:135], v[84:87], %[c2]\n" \
  "ds_read_b128 v[128:131], %[vw] offset:58368\n" \
  "s_waitcnt lgkmcnt(8)\n" \
  "v_mfma_f32_16x16x32_bf16 %[c3], v[136:139], v[84:87], %[c3]\n" \
  "ds_read_b128 v[132:135], %[vw] offset:59392\n" \
  "s_waitcnt lgkmcnt(8)\n" \
  "v_mfma_f32_16x16x32_bf16 %[c4], v[140:143], v[84:87], %[c4]\n" \
  "s_waitcnt vmcnt(5)\n" \
  "ds_read_b128 v[136:139], %[vw] offset:60416\n" \
  "s_waitcnt lgkmcnt(8)\n" \
  "v_mfma_f32_16x16x32_bf16 %[c0], v[104:107], v[88:91], %[c0]\n" \
  "ds_read_b128 v[140:143], %[vw] offset:61440\n" \
  "s_waitcnt lgkmcnt(8)\n" \
  "v_mfma_f32_16x16x32_bf16 %[c1], v[108:111], v[88:91], %[c1]\n" \
  "v_add_u32 %[vw], 65536, %[vw]\n" \
  "ds_read_b128 v[104:107], %[vw] offset:2048\n" \
  "s_waitcnt lgkmcnt(8)\n" \
  "v_mfma_f32_16x16x32_bf16 %[c2], v[112:115], v[88:91], %[c2]\n" \
  "ds_read_b128 v[108:111], %[vw] offset:3072\n" \
  "s_waitcnt lgkmcnt(8)\n" \
  "v_mfma_f32_16x16x32_bf16 %[c3], v[116:119], v[88:91], %[c3]\n" \
  "ds_read_b128 v[112:115], %[vw] offset:4096\n" \
  "s_waitcnt lgkmcnt(8)\n" \
  "v_mfma_f32_16x16x32_bf16 %[c4], v[120:123], v[88:91], %[c4]\n" \
  "s_waitcnt vmcnt(4)\n" \
  "ds_read_b128 v[116:119], %[vw] offset:5120\n" \
  "s_waitcnt lgkmcnt(8)\n" \
  "v_mfma_f32_16x16x32_bf16 %[c0], v[124:127], v[92:95], %[c0]\n" \
  "ds_read_b128 v[120:123], %[vw] offset:6144\n" \
  "s_waitcnt lgkmcnt(8)\n" \
  "v_mfma_f32_16x16x32_bf16 %[c1], v[128:131], v[92:95], %[c1]\n" \
  "ds_read_b128 v[124:127], %[vw] offset:12288\n" \
  "s_waitcnt lgkmcnt(8)\n" \
  "v_mfma_f32_16x16x32_bf16 %[c2], v[132:135], v[92:95], %[c2]\n" \
  "ds_read_b128 v[128:131], %[vw] offset:13312\n" \
  "s_waitcnt lgkmcnt(8)\n" \
  "v_mfma_f32_16x16x32_bf16 %[c3], v[136:139], v[92:95], %[c3]\n" \
  "ds_read_b128 v[132:135], %[vw] offset:14336\n" \
  "s_waitcnt lgkmcnt(8)\n" \
  "v_mfma_f32_16x16x32_bf16 %[c4], v[140:143], v[92:95], %[c4]\n" \
  "s_waitcnt vmcnt(3)\n" \
  "ds_read_b128 v[136:139], %[vw] offset:15360\n" \
  "s_waitcnt lgkmcnt(8)\n" \
  "v_mfma_f32_16x16x32_bf16 %[c0], v[104:107], v[96:99], %[c0]\n" \
  "ds_read_b128 v[140:143], %[vw] offset:16384\n" \
  "s_waitcnt lgkmcnt(8)\n" \
  "v_mfma_f32_16x16x32_bf16 %[c1], v[108:111], v[96:99], %[c1]\n" \
  "s_waitcnt lgkmcnt(7)\n" \
  "v_mfma_f32_16x16x32_bf16 %[c2], v[112:115], v[96:99], %[c2]\n" \
  "s_waitcnt lgkmcnt(6)\n" \
  "v_mfma_f32_16x16x32_bf16 %[c3], v[116:119], v[96:99], %[c3]\n" \
  "s_waitcnt lgkmcnt(5)\n" \
  "v_mfma_f32_16x16x32_bf16 %[c4], v[120:123], v[96:99], %[c4]\n" \
  "s_waitcnt vmcnt(2)\n" \
  "s_waitcnt lgkmcnt(4)\n" \
  "v_mfma_f32_16x16x32_bf16 %[c0], v[124:127], v[100:103], %[c0]\n" \
  "s_waitcnt lgkmcnt(3)\n" \
  "v_mfma_f32_16x16x32_bf16 %[c1], v[128:131], v[100:103], %[c1]\n" \
  "s_waitcnt lgkmcnt(2)\n" \
  "v_mfma_f32_16x16x32_bf16 %[c2], v[132:135], v[100:103], %[c2]\n" \
  "s_waitcnt lgkmcnt(1)\n" \
  "v_mfma_f32_16x16x32_bf16 %[c3], v[136:139], v[100:103], %[c3]\n" \
  "s_waitcnt lgkmcnt(0)\n" \
  "v_mfma_f32_16x16x32_bf16 %[c4], v[140:143], v[100:103], %[c4]\n" \
  "s_nop 7\n" \
  ""
#define PS_CLOB "v64", "v65", "v66", "v67", "v68", "v69", "v70", "v71", "v72", "v73", "v74", "v75", "v76", "v77", "v78", "v79", "v80", "v81", "v82", "v83", "v84", "v85", "v86", "v87", "v88", "v89", "v90", "v91", "v92", "v93", "v94", "v95", "v96", "v97", "v98", "v99", "v100", "v101", "v102", "v103", "v104", "v105", "v106", "v107", "v108", "v109", "v110", "v111", "v112", "v113", "v114", "v115", "v116", "v117", "v118", "v119", "v120", "v121", "v122", "v123", "v124", "v125", "v126", "v127", "v128", "v129", "v130", "v131", "v132", "v133", "v134", "v135", "v136", "v137", "v138", "v139", "v140", "v141", "v142", "v143"
#define P6_ASM \
  "s_nop 4\n" \
  "buffer_load_dwordx4 v[64:67], %[va0], %[rs], %[sa] offen offset:0 sc1\n" \
  "buffer_load_dwordx4 v[68:71], %[va0], %[rs], %[sa] offen offset:1024 sc1\n" \
  "buffer_load_dwordx4 v[72:75], %[va0], %[rs], %[sa] offen offset:2048 sc1\n" \
  "buffer_load_dwordx4 v[76:79], %[va0], %[rs], %[sa] offen offset:3072 sc1\n" \
  "s_add_u32 %[sa], %[sa], 4096\n" \
  "buffer_load_dwordx4 v[80:83], %[va0], %[rs], %[sa] offen offset:0 sc1\n" \
  "buffer_load_dwordx4 v[84:87], %[va0], %[rs], %[sa] offen offset:1024 sc1\n" \
  "buffer_load_dwordx4 v[88:91], %[va0], %[rs], %[sa] offen offset:2048 sc1\n" \
  "buffer_load_dwordx4 v[92:95], %[va0], %[rs], %[sa] offen offset:3072 sc1\n" \
  "s_add_u32 %[sa], %[sa], 4096\n" \
  "buffer_load_dwordx4 v[96:99], %[va0], %[rs], %[sa] offen offset:0 sc1\n" \
  "buffer_load_dwordx4 v[100:103], %[va0], %[rs], %[sa] offen offset:1024 sc1\n" \
  "buffer_load_dwordx4 v[104:107], %[va0], %[rs], %[sa] offen offset:2048 sc1\n" \
  "buffer_load_dwordx4 v[108:111], %[va0], %[rs], %[sa] offen offset:3072 sc1\n" \
  "s_add_u32 %[sa], %[sa], 4096\n" \
  "buffer_load_dwordx4 v[112:115], %[va0], %[rs], %[sa] offen offset:0 sc1\n" \
  "buffer_load_dwordx4 v[116:119], %[va0], %[rs], %[sa] offen offset:1024 sc1\n" \
  "buffer_load_dwordx4 v[120:123], %[va0], %[rs], %[sa] offen offset:2048 sc1\n" \
  "buffer_load_dwordx4 v[124:127], %[va0], %[rs], %[sa] offen offset:3072 sc1\n" \
  "s_add_u32 %[sa], %[sa], 4096\n" \
  "buffer_load_dwordx4 v[128:131], %[va0], %[rs], %[sa] offen offset:0 sc1\n" \
  "buffer_load_dwordx4 v[132:135], %[va0], %[rs], %[sa] offen offset:1024 sc1\n" \
  "buffer_load_dwordx4 v[136:139], %[va0], %[rs], %[sa] offen offset:2048 sc1\n" \
  "buffer_load_dwordx4 v[140:143], %[va0], %[rs], %[sa] offen offset:3072 sc1\n" \
  "buffer_load_dwordx2 %[st0], %[vst], %[rs], 0 offen offset:0 sc1\n" \
  "buffer_load_dwordx2 %[st1], %[vst], %[rs], 0 offen offset:128 sc1\n" \
  "buffer_load_dwordx2 %[st2], %[vst], %[rs], 0 offen offset:256 sc1\n" \
  "buffer_load_dwordx2 %[st3], %[vst], %[rs], 0 offen offset:384 sc1\n" \
  "ds_read_b128 v[144:147], %[vw] offset:0\n" \
  "ds_read_b128 v[148:151], %[vw] offset:1024\n" \
  "ds_read_b128 v[152:155], %[vw] offset:2048\n" \
  "ds_read_b128 v[156:159], %[vw] offset:3072\n" \
  "ds_read_b128 v[160:163], %[vw] offset:4096\n" \
  "ds_read_b128 v[164:167], %[vw] offset:5120\n" \
  "ds_read_b128 v[168:171], %[vw] offset:6144\n" \
  "ds_read_b128 v[172:175], %[vw] offset:7168\n" \
  "s_waitcnt vmcnt(23)\n" \
  "ds_read_b128 v[176:179], %[vw] offset:8192\n" \
  "s_waitcnt lgkmcnt(8)\n" \
  "v_mfma_f32_16x16x32_bf16 %[c0], v[144:147], v[64:67], 0\n" \
  "ds_read_b128 v[180:183], %[vw] offset:9216\n" \
  "s_waitcnt lgkmcnt(8)\n" \
  "v_mfma_f32_16x16x32_bf16 %[c1], v[148:151], v[64:67], 0\n" \
  "ds_read_b128 v[144:147], %[vw] offset:10240\n" \
  "s_waitcnt lgkmcnt(8)\n" \
  "v_mfma_f32_16x16x32_bf16 %[c2], v[152:155], v[64:67], 0\n" \
  "ds_read_b128 v[148:151], %[vw] offset:11264\n" \
  "s_waitcnt lgkmcnt(8)\n" \
  "v_mfma_f32_16x16x32_bf16 %[c3], v[156:159], v[64:67], 0\n" \
  "ds_read_b128 v[152:155], %[vw] offset:12288\n" \
  "s_waitcnt lgkmcnt(8)\n" \
  "v_mfma_f32_16x16x32_bf16 %[c4], v[160:163], v[64:67], 0\n" \
  "ds_read_b128 v[156:159], %[vw] offset:13312\n" \
  "s_waitcnt lgkmcnt(8)\n" \
  "v_mfma_f32_16x16x32_bf16 %[c5], v[164:167], v[64:67], 0\n" \
  "s_waitcnt vmcnt(22)\n" \
  "ds_read_b128 v[160:163], %[vw] offset:14336\n" \
  "s_waitcnt lgkmcnt(8)\n" \
  "v_mfma_f32_16x16x32_bf16 %[c0], v[168:171], v[68:71], %[c0]\n" \
  "ds_read_b128 v[164:167], %[vw] offset:15360\n" \
  "s_waitcnt lgkmcnt(8)\n" \
  "v_mfma_f32_16x16x32_bf16 %[c1], v[172:175], v[68:71], %[c1]\n" \
  "ds_read_b128 v[168:171], %[vw] offset:16384\n" \
  "s_waitcnt lgkmcnt(8)\n" \
  "v_mfma_f32_16x16x32_bf16 %[c2], v[176:179], v[68:71], %[c2]\n" \
  "ds_read_b128 v[172:175], %[vw] offset:17408\n" \
  "s_waitcnt lgkmcnt(8)\n" \
  "v_mfma_f32_16x16x32_bf16 %[c3], v[180:183], v[68:71], %[c3]\n" \
  "ds_read_b128 v[176:179], %[vw] offset:18432\n" \
  "s_waitcnt lgkmcnt(8)\n" \
  "v_mfma_f32_16x16x32_bf16 %[c4], v[144:147], v[68:71], %[c4]\n" \
  "ds_read_b128 v[180:183], %[vw] offset:19456\n" \
  "s_waitcnt lgkmcnt(8)\n" \
  "v_mfma_f32_16x16x32_bf16 %[c5], v[148:151], v[68:71], %[c5]\n" \
  "s_waitcnt vmcnt(21)\n" \
  "ds_read_b128 v[144:147], %[vw] offset:20480\n" \
  "s_waitcnt lgkmcnt(8)\n" \
  "v_mfma_f32_16x16x32_bf16 %[c0], v[152:155], v[72:75], %[c0]\n" \
  "ds_read_b128 v[148:151], %[vw] offset:21504\n" \
  "s_waitcnt lgkmcnt(8)\n" \
  "v_mfma_f32_16x16x32_bf16 %[c1], v[156:159], v[72:75], %[c1]\n" \
  "ds_read_b128 v[152:155], %[vw] offset:22528\n" \
  "s_waitcnt lgkmcnt(8)\n" \
  "v_mfma_f32_16x16x32_bf16 %[c2], v[160:163], v[72:75], %[c2]\n" \
  "ds_read_b128 v[156:159], %[vw] offset:23552\n" \
  "s_waitcnt lgkmcnt(8)\n" \
  "v_mfma_f32_16x16x32_bf16 %[c3], v[164:167], v[72:75], %[c3]\n" \
  "ds_read_b128 v[160:163], %[vw] offset:24576\n" \
  "s_waitcnt lgkmcnt(8)\n" \
  "v_mfma_f32_16x16x32_bf16 %[c4], v[168:171], v[72:75], %[c4]\n" \
  "ds_read_b128 v[164:167], %[vw] offset:25600\n" \
  "s_waitcnt lgkmcnt(8)\n" \
  "v_mfma_f32_16x16x32_bf16 %[c5], v[172:175], v[72:75], %[c5]\n" \
  "s_waitcnt vmcnt(20)\n" \
  "ds_read_b128 v[168:171], %[vw] offset:26624\n" \
  "s_waitcnt lgkmcnt(8)\n" \
  "v_mfma_f32_16x16x32_bf16 %[c0], v[176:179], v[76:79], %[c0]\n" \
  "ds_read_b128 v[172:175], %[vw] offset:27648\n" \
  "s_waitcnt lgkmcnt(8)\n" \
  "v_mfma_f32_16x16x32_bf16 %[c1], v[180:183], v[76:79], %[c1]\n" \
  "ds_read_b128 v[176:179], %[vw] offset:28672\n" \
  "s_waitcnt lgkmcnt(8)\n" \
  "v_mfma_f32_16x16x32_bf16 %[c2], v[144:147], v[76:79], %[c2]\n" \
  "ds_read_b128 v[180:183], %[vw] offset:29696\n" \
  "s_waitcnt lgkmcnt(8)\n" \
  "v_mfma_f32_16x16x32_bf16 %[c3], v[148:151], v[76:79], %[c3]\n" \
  "ds_read_b128 v[144:147], %[vw] offset:30720\n" \
  "s_waitcnt lgkmcnt(8)\n" \
  "v_mfma_f32_16x16x32_bf16 %[c4], v[152:155], v[76:79], %[c4]\n" \
  "ds_read_b128 v[148:151], %[vw] offset:31744\n" \
  "s_waitcnt lgkmcnt(8)\n" \
  "v_mfma_f32_16x16x32_bf16 %[c5], v[156:159], v[76:79], %[c5]\n" \
  "s_waitcnt vmcnt(19)\n" \
  "ds_read_b128 v[152:155], %[vw] offset:32768\n" \
  "s_waitcnt lgkmcnt(8)\n" \
  "v_mfma_f32_16x16x32_bf16 %[c0], v[160:163], v[80:83], %[c0]\n" \
  "ds_read_b128 v[156:159], %[vw] offset:33792\n" \
  "s_waitcnt lgkmcnt(8)\n" \
  "v_mfma_f32_16x16x32_bf16 %[c1], v[164:167], v[80:83], %[c1]\n" \
  "ds_read_b128 v[160:163], %[vw] offset:34816\n" \
  "s_waitcnt lgkmcnt(8)\n" \
  "v_mfma_f32_16x16x32_bf16 %[c2], v[168:171], v[80:83], %[c2]\n" \
  "ds_read_b128 v[164:167], %[vw] offset:35840\n" \
  "s_waitcnt lgkmcnt(8)\n" \
  "v_mfma_f32_16x16x32_bf16 %[c3], v[172:175], v[80:83], %[c3]\n" \
  "ds_read_b128 v[168:171], %[vw] offset:36864\n" \
  "s_waitcnt lgkmcnt(8)\n" \
  "v_mfma_f32_16x16x32_bf16 %[c4], v[176:179], v[80:83], %[c4]\n" \
  "ds_read_b128 v[172:175], %[vw] offset:37888\n" \
  "s_waitcnt lgkmcnt(8)\n" \
  "v_mfma_f32_16x16x32_bf16 %[c5], v[180:183], v[80:83], %[c5]\n" \
  "s_waitcnt vmcnt(18)\n" \
  "ds_read_b128 v[176:179], %[vw] offset:38912\n" \
  "s_waitcnt lgkmcnt(8)\n" \
  "v_mfma_f32_16x16x32_bf16 %[c0], v[144:147], v[84:87], %[c0]\n" \
  "ds_read_b128 v[180:183], %[vw] offset:39936\n" \
  "s_waitcnt lgkmcnt(8)\n" \
  "v_mfma_f32_16x16x32_bf16 %[c1], v[148:151], v[84:87], %[c1]\n" \
  "ds_read_b128 v[144:147], %[vw] offset:40960\n" \
  "s_waitcnt lgkmcnt(8)\n" \
  "v_mfma_f32_16x16x32_bf16 %[c2], v[152:155], v[84:87], %[c2]\n" \
  "ds_read_b128 v[148:151], %[vw] offset:41984\n" \
  "s_waitcnt lgkmcnt(8)\n" \
  "v_mfma_f32_16x16x32_bf16 %[c3], v[156:159], v[84:87], %[c3]\n" \
  "ds_read_b128 v[152:155], %[vw] offset:43008\n" \
  "s_waitcnt lgkmcnt(8)\n" \
  "v_mfma_f32_16x16x32_bf16 %[c4], v[160:163], v[84:87], %[c4]\n" \
  "ds_read_b128 v[156:159], %[vw] offset:44032\n" \
  "s_waitcnt lgkmcnt(8)\n" \
  "v_mfma_f32_16x16x32_bf16 %[c5], v[164:167], v[84:87], %[c5]\n" \
  "s_waitcnt vmcnt(17)\n" \
  "ds_read_b128 v[160:163], %[vw] offset:45056\n" \
  "s_waitcnt lgkmcnt(8)\n" \
  "v_mfma_f32_16x16x32_bf16 %[c0], v[168:171], v[88:91], %[c0]\n" \
  "ds_read_b128 v[164:167], %[vw] offset:46080\n" \
  "s_waitcnt lgkmcnt(8)\n" \
  "v_mfma_f32_16x16x32_bf16 %[c1], v[172:175], v[88:91], %[c1]\n" \
  "ds_read_b128 v[168:171], %[vw] offset:47104\n" \
  "s_waitcnt lgkmcnt(8)\n" \
  "v_mfma_f32_16x16x32_bf16 %[c2], v[176:179], v[88:91], %[c2]\n" \
  "ds_read_b128 v[172:175], %[vw] offset:48128\n" \
  "s_waitcnt lgkmcnt(8)\n" \
  "v_mfma_f32_16x16x32_bf16 %[c3], v[180:183], v[88:91], %[c3]\n" \
  "ds_read_b128 v[176:179], %[vw] offset:49152\n" \
  "s_waitcnt lgkmcnt(8)\n" \
  "v_mfma_f32_16x16x32_bf16 %[c4], v[144:147], v[88:91], %[c4]\n" \
  "ds_read_b128 v[180:183], %[vw] offset:50176\n" \
  "s_waitcnt lgkmcnt(8)\n" \
  "v_mfma_f32_16x16x32_bf16 %[c5], v[148:151], v[88:91], %[c5]\n" \
  "s_waitcnt vmcnt(16)\n" \
  "ds_read_b128 v[144:147], %[vw] offset:51200\n" \
  "s_waitcnt lgkmcnt(8)\n" \
  "v_mfma_f32_16x16x32_bf16 %[c0], v[152:155], v[92:95], %[c0]\n" \
  "ds_read_b128 v[148:151], %[vw] offset:52224\n" \
  "s_waitcnt lgkmcnt(8)\n" \
  "v_mfma_f32_16x16x32_bf16 %[c1], v[156:159], v[92:95], %[c1]\n" \
  "ds_read_b128 v[152:155], %[vw] offset:53248\n" \
  "s_waitcnt lgkmcnt(8)\n" \
  "v_mfma_f32_16x16x32_bf16 %[c2], v[160:163], v[92:95], %[c2]\n" \
  "ds_read_b128 v[156:159], %[vw] offset:54272\n" \
  "s_waitcnt lgkmcnt(8)\n" \
  "v_mfma_f32_16x16x32_bf16 %[c3], v[164:167], v[92:95], %[c3]\n" \
  "ds_read_b128 v[160:163], %[vw] offset:55296\n" \
  "s_waitcnt lgkmcnt(8)\n" \
  "v_mfma_f32_16x16x32_bf16 %[c4], v[168:171], v[92:95], %[c4]\n" \
  "ds_read_b128 v[164:167], %[vw] offset:56320\n" \
  "s_waitcnt lgkmcnt(8)\n" \
  "v_mfma_f32_16x16x32_bf16 %[c5], v[172:175], v[92:95], %[c5]\n" \
  "s_barrier\n" \
  "s_mov_b32 m0, %[dm0]\n" \
  "s_nop 0\n" \
  "buffer_load_dwordx4 %[l16], %[rs], %[ds0] offen offset:0 sc1 lds\n" \
  "buffer_load_dwordx4 %[l16], %[rs], %[ds0] offen offset:1024 sc1 lds\n" \
  "buffer_load_dwordx4 %[l16], %[rs], %[ds0] offen offset:2048 sc1 lds\n" \
  "buffer_load_dwordx4 %[l16], %[rs], %[ds0] offen offset:3072 sc1 lds\n" \
  "s_add_u32 %[dm0], %[dm0], 4096\n" \
  "s_add_u32 %[ds0], %[ds0], 4096\n" \
  "s_mov_b32 m0, %[dm0]\n" \
  "s_nop 0\n" \
  "buffer_load_dwordx4 %[l16], %[rs], %[ds0] offen offset:0 sc1 lds\n" \
  "buffer_load_dwordx4 %[l16], %[rs], %[ds0] offen offset:1024 sc1 lds\n" \
  "s_waitcnt vmcnt(21)\n" \
  "ds_read_b128 v[168:171], %[vw] offset:57344\n" \
  "s_waitcnt lgkmcnt(8)\n" \
  "v_mfma_f32_16x16x32_bf16 %[c0], v[176:179], v[96:99], %[c0]\n" \
  "ds_read_b128 v[172:175], %[vw] offset:58368\n" \
  "s_waitcnt lgkmcnt(8)\n" \
  "v_mfma_f32_16x16x32_bf16 %[c1], v[180:183], v[96:99], %[c1]\n" \
  "ds_read_b128 v[176:179], %[vw] offset:59392\n" \
  "s_waitcnt lgkmcnt(8)\n" \
  "v_mfma_f32_16x16x32_bf16 %[c2], v[144:147], v[96:99], %[c2]\n" \
  "ds_read_b128 v[180:183], %[vw] offset:60416\n" \
  "s_waitcnt lgkmcnt(8)\n" \
  "v_mfma_f32_16x16x32_bf16 %[c3], v[148:151], v[96:99], %[c3]\n" \
  "ds_read_b128 v[144:147], %[vw] offset:61440\n" \
  "s_waitcnt lgkmcnt(8)\n" \
  "v_mfma_f32_16x16x32_bf16 %[c4], v[152:155], v[96:99], %[c4]\n" \
  "ds_read_b128 v[148:151], %[vw] offset:62464\n" \
  "s_waitcnt lgkmcnt(8)\n" \
  "v_mfma_f32_16x16x32_bf16 %[c5], v[156:159], v[96:99], %[c5]\n" \
  "s_waitcnt vmcnt(20)\n" \
  "ds_read_b128 v[152:155], %[vw] offset:63488\n" \
  "s_waitcnt lgkmcnt(8)\n" \
  "v_mfma_f32_16x16x32_bf16 %[c0], v[160:163], v[100:103], %[c0]\n" \
  "ds_read_b128 v[156:159], %[vw] offset:64512\n" \
  "s_waitcnt lgkmcnt(8)\n" \
  "v_mfma_f32_16x16x32_bf16 %[c1], v[164:167], v[100:103], %[c1]\n" \
  "v_add_u32 %[vw], 65536, %[vw]\n" \
  "ds_read_b128 v[160:163], %[vw] offset:0\n" \
  "s_waitcnt lgkmcnt(8)\n" \
  "v_mfma_f32_16x16x32_bf16 %[c2], v[168:171], v[100:103], %[c2]\n" \
  "ds_read_b128 v[164:167], %[vw] offset:1024\n" \
  "s_waitcnt lgkmcnt(8)\n" \
  "v_mfma_f32_16x16x32_bf16 %[c3], v[172:175], v[100:103], %[c3]\n" \
  "ds_read_b128 v[168:171], %[vw] offset:2048\n" \
  "s_waitcnt lgkmcnt(8)\n" \
  "v_mfma_f32_16x16x32_bf16 %[c4], v[176:179], v[100:103], %[c4]\n" \
  "ds_read_b128 v[172:175], %[vw] offset:3072\n" \
  "s_waitcnt lgkmcnt(8)\n" \
  "v_mfma_f32_16x16x32_bf16 %[c5], v[180:183], v[100:103], %[c5]\n" \
  "s_waitcnt vmcnt(19)\n" \
  "ds_read_b128 v[176:179], %[vw] offset:4096\n" \
  "s_waitcnt lgkmcnt(8)\n" \
  "v_mfma_f32_16x16x32_bf16 %[c0], v[144:147], v[104:107], %[c0]\n" \
  "ds_read_b128 v[180:183], %[vw] offset:5120\n" \
  "s_waitcnt lgkmcnt(8)\n" \
  "v_mfma_f32_16x16x32_bf16 %[c1], v[148:151], v[104:107], %[c1]\n" \
  "ds_read_b128 v[144:147], %[vw] offset:6144\n" \
  "s_waitcnt lgkmcnt(8)\n" \
  "v_mfma_f32_16x16x32_bf16 %[c2], v[152:155], v[104:107], %[c2]\n" \
  "ds_read_b128 v[148:151], %[vw] offset:7168\n" \
  "s_waitcnt lgkmcnt(8)\n" \
  "v_mfma_f32_16x16x32_bf16 %[c3], v[156:159], v[104:107], %[c3]\n" \
  "ds_read_b128 v[152:155], %[vw] offset:8192\n" \
  "s_waitcnt lgkmcnt(8)\n" \
  "v_mfma_f32_16x16x32_bf16 %[c4], v[160:163], v[104:107], %[c4]\n" \
  "ds_read_b128 v[156:159], %[vw] offset:9216\n" \
  "s_waitcnt lgkmcnt(8)\n" \
  "v_mfma_f32_16x16x32_bf16 %[c5], v[164:167], v[104:107], %[c5]\n" \
  "s_waitcnt vmcnt(18)\n" \
  "ds_read_b128 v[160:163], %[vw] offset:10240\n" \
  "s_waitcnt lgkmcnt(8)\n" \
  "v_mfma_f32_16x16x32_bf16 %[c0], v[168:171], v[108:111], %[c0]\n" \
  "ds_read_b128 v[164:167], %[vw] offset:11264\n" \
  "s_waitcnt lgkmcnt(8)\n" \
  "v_mfma_f32_16x16x32_bf16 %[c1], v[172:175], v[108:111], %[c1]\n" \
  "ds_read_b128 v[168:171], %[vw] offset:12288\n" \
  "s_waitcnt lgkmcnt(8)\n" \
  "v_mfma_f32_16x16x32_bf16 %[c2], v[176:179], v[108:111], %[c2]\n" \
  "ds_read_b128 v[172:175], %[vw] offset:13312\n" \
  "s_waitcnt lgkmcnt(8)\n" \
  "v_mfma_f32_16x16x32_bf16 %[c3], v[180:183], v[108:111], %[c3]\n" \
  "ds_read_b128 v[176:179], %[vw] offset:14336\n" \
  "s_waitcnt lgkmcnt(8)\n" \
  "v_mfma_f32_16x16x32_bf16 %[c4], v[144:147], v[108:111], %[c4]\n" \
  "ds_read_b128 v[180:183], %[vw] offset:15360\n" \
  "s_waitcnt lgkmcnt(8)\n" \
  "v_mfma_f32_16x16x32_bf16 %[c5], v[148:151], v[108:111], %[c5]\n" \
  "s_waitcnt vmcnt(17)\n" \
  "ds_read_b128 v[144:147], %[vw] offset:16384\n" \
  "s_waitcnt lgkmcnt(8)\n" \
  "v_mfma_f32_16x16x32_bf16 %[c0], v[152:155], v[112:115], %[c0]\n" \
  "ds_read_b128 v[148:151], %[vw] offset:17408\n" \
  "s_waitcnt lgkmcnt(8)\n" \
  "v_mfma_f32_16x16x32_bf16 %[c1], v[156:159], v[112:115], %[c1]\n" \
  "ds_read_b128 v[152:155], %[vw] offset:18432\n" \
  "s_waitcnt lgkmcnt(8)\n" \
  "v_mfma_f32_16x16x32_bf16 %[c2], v[160:163], v[112:115], %[c2]\n" \
  "ds_read_b128 v[156:159], %[vw] offset:19456\n" \
  "s_waitcnt lgkmcnt(8)\n" \
  "v_mfma_f32_16x16x32_bf16 %[c3], v[164:167], v[112:115], %[c3]\n" \
  "ds_read_b128 v[160:163], %[vw] offset:20480\n" \
  "s_waitcnt lgkmcnt(8)\n" \
  "v_mfma_f32_16x16x32_bf16 %[c4], v[168:171], v[112:115], %[c4]\n" \
  "ds_read_b128 v[164:167], %[vw] offset:21504\n" \
  "s_waitcnt lgkmcnt(8)\n" \
  "v_mfma_f32_16x16x32_bf16 %[c5], v[172:175], v[112:115], %[c5]\n" \
  "s_waitcnt vmcnt(16)\n" \
  "ds_read_b128 v[168:171], %[vw] offset:22528\n" \
  "s_waitcnt lgkmcnt(8)\n" \
  "v_mfma_f32_16x16x32_bf16 %[c0], v[176:179], v[116:119], %[c0]\n" \
  "ds_read_b128 v[172:175], %[vw] offset:23552\n" \
  "s_waitcnt lgkmcnt(8)\n" \
  "v_mfma_f32_16x16x32_bf16 %[c1], v[180:183], v[116:119], %[c1]\n" \
  "ds_read_b128 v[176:179], %[vw] offset:24576\n" \
  "s_waitcnt lgkmcnt(8)\n" \
  "v_mfma_f32_16x16x32_bf16 %[c2], v[144:147], v[116:119], %[c2]\n" \
  "ds_read_b128 v[180:183], %[vw] offset:25600\n" \
  "s_waitcnt lgkmcnt(8)\n" \
  "v_mfma_f32_16x16x32_bf16 %[c3], v[148:151], v[116:119], %[c3]\n" \
  "ds_read_b128 v[144:147], %[vw] offset:26624\n" \
  "s_waitcnt lgkmcnt(8)\n" \
  "v_mfma_f32_16x16x32_bf16 %[c4], v[152:155], v[116:119], %[c4]\n" \
  "ds_read_b128 v[148:151], %[vw] offset:27648\n" \
  "s_waitcnt lgkmcnt(8)\n" \
  "v_mfma_f32_16x16x32_bf16 %[c5], v[156:159], v[116:119], %[c5]\n" \
  "s_waitcnt vmcnt(15)\n" \
  "ds_read_b128 v[152:155], %[vw] offset:28672\n" \
  "s_waitcnt lgkmcnt(8)\n" \
  "v_mfma_f32_16x16x32_bf16 %[c0], v[160:163], v[120:123], %[c0]\n" \
  "ds_read_b128 v[156:159], %[vw] offset:29696\n" \
  "s_waitcnt lgkmcnt(8)\n" \
  "v_mfma_f32_16x16x32_bf16 %[c1], v[164:167], v[120:123], %[c1]\n" \
  "ds_read_b128 v[160:163], %[vw] offset:30720\n" \
  "s_waitcnt lgkmcnt(8)\n" \
  "v_mfma_f32_16x16x32_bf16 %[c2], v[168:171], v[120:123], %[c2]\n" \
  "ds_read_b128 v[164:167], %[vw] offset:31744\n" \
  "s_waitcnt lgkmcnt(8)\n" \
  "v_mfma_f32_16x16x32_bf16 %[c3], v[172:175], v[120:123], %[c3]\n" \
  "ds_read_b128 v[168:171], %[vw] offset:32768\n" \
  "s_waitcnt lgkmcnt(8)\n" \
  "v_mfma_f32_16x16x32_bf16 %[c4], v[176:179], v[120:123], %[c4]\n" \
  "ds_read_b128 v[172:175], %[vw] offset:33792\n" \
  "s_waitcnt lgkmcnt(8)\n" \
  "v_mfma_f32_16x16x32_bf16 %[c5], v[180:183], v[120:123], %[c5]\n" \
  "s_waitcnt vmcnt(14)\n" \
  "ds_read_b128 v[176:179], %[vw] offset:34816\n" \
  "s_waitcnt lgkmcnt(8)\n" \
  "v_mfma_f32_16x16x32_bf16 %[c0], v[144:147], v[124:127], %[c0]\n" \
  "ds_read_b128 v[180:183], %[vw] offset:35840\n" \
  "s_waitcnt lgkmcnt(8)\n" \
  "v_mfma_f32_16x16x32_bf16 %[c1], v[148:151], v[124:127], %[c1]\n" \
  "ds_read_b128 v[144:147], %[vw] offset:36864\n" \
  "s_waitcnt lgkmcnt(8)\n" \
  "v_mfma_f32_16x16x32_bf16 %[c2], v[152:155], v[124:127], %[c2]\n" \
  "ds_read_b128 v[148:151], %[vw] offset:37888\n" \
  "s_waitcnt lgkmcnt(8)\n" \
  "v_mfma_f32_16x16x32_bf16 %[c3], v[156:159], v[124:127], %[c3]\n" \
  "ds_read_b128 v[152:155], %[vw] offset:38912\n" \
  "s_waitcnt lgkmcnt(8)\n" \
  "v_mfma_f32_16x16x32_bf16 %[c4], v[160:163], v[124:127], %[c4]\n" \
  "ds_read_b128 v[156:159], %[vw] offset:39936\n" \
  "s_waitcnt lgkmcnt(8)\n" \
  "v_mfma_f32_16x16x32_bf16 %[c5], v[164:167], v[124:127], %[c5]\n" \
  "s_waitcnt vmcnt(13)\n" \
  "ds_read_b128 v[160:163], %[vw] offset:40960\n" \
  "s_waitcnt lgkmcnt(8)\n" \
  "v_mfma_f32_16x16x32_bf16 %[c0], v[168:171], v[128:131], %[c0]\n" \
  "ds_read_b128 v[164:167], %[vw] offset:41984\n" \
  "s_waitcnt lgkmcnt(8)\n" \
  "v_mfma_f32_16x16x32_bf16 %[c1], v[172:175], v[128:131], %[c1]\n" \
  "ds_read_b128 v[168:171], %[vw] offset:43008\n" \
  "s_waitcnt lgkmcnt(8)\n" \
  "v_mfma_f32_16x16x32_bf16 %[c2], v[176:179], v[128:131], %[c2]\n" \
  "ds_read_b128 v[172:175], %[vw] offset:44032\n" \
  "s_waitcnt lgkmcnt(8)\n" \
  "v_mfma_f32_16x16x32_bf16 %[c3], v[180:183], v[128:131], %[c3]\n" \
  "ds_read_b128 v[176:179], %[vw] offset:45056\n" \
  "s_waitcnt lgkmcnt(8)\n" \
  "v_mfma_f32_16x16x32_bf16 %[c4], v[144:147], v[128:131], %[c4]\n" \
  "ds_read_b128 v[180:183], %[vw] offset:46080\n" \
  "s_waitcnt lgkmcnt(8)\n" \
  "v_mfma_f32_16x16x32_bf16 %[c5], v[148:151], v[128:131], %[c5]\n" \
  "s_waitcnt vmcnt(12)\n" \
  "ds_read_b128 v[144:147], %[vw] offset:47104\n" \
  "s_waitcnt lgkmcnt(8)\n" \
  "v_mfma_f32_16x16x32_bf16 %[c0], v[152:155], v[132:135], %[c0]\n" \
  "ds_read_b128 v[148:151], %[vw] offset:48128\n" \
  "s_waitcnt lgkmcnt(8)\n" \
  "v_mfma_f32_16x16x32_bf16 %[c1], v[156:159], v[132:135], %[c1]\n" \
  "ds_read_b128 v[152:155], %[vw] offset:49152\n" \
  "s_waitcnt lgkmcnt(8)\n" \
  "v_mfma_f32_16x16x32_bf16 %[c2], v[160:163], v[132:135], %[c2]\n" \
  "ds_read_b128 v[156:159], %[vw] offset:50176\n" \
  "s_waitcnt lgkmcnt(8)\n" \
  "v_mfma_f32_16x16x32_bf16 %[c3], v[164:167], v[132:135], %[c3]\n" \
  "ds_read_b128 v[160:163], %[vw] offset:51200\n" \
  "s_waitcnt lgkmcnt(8)\n" \
  "v_mfma_f32_16x16x32_bf16 %[c4], v[168:171], v[132:135], %[c4]\n" \
  "ds_read_b128 v[164:167], %[vw] offset:52224\n" \
  "s_waitcnt lgkmcnt(8)\n" \
  "v_mfma_f32_16x16x32_bf16 %[c5], v[172:175], v[132:135], %[c5]\n" \
  "s_waitcnt vmcnt(11)\n" \
  "ds_read_b128 v[168:171], %[vw] offset:53248\n" \
  "s_waitcnt lgkmcnt(8)\n" \
  "v_mfma_f32_16x16x32_bf16 %[c0], v[176:179], v[136:139], %[c0]\n" \
  "ds_read_b128 v[172:175], %[vw] offset:54272\n" \
  "s_waitcnt lgkmcnt(8)\n" \
  "v_mfma_f32_16x16x32_bf16 %[c1], v[180:183], v[136:139], %[c1]\n" \
  "ds_read_b128 v[176:179], %[vw] offset:55296\n" \
  "s_waitcnt lgkmcnt(8)\n" \
  "v_mfma_f32_16x16x32_bf16 %[c2], v[144:147], v[136:139], %[c2]\n" \
  "ds_read_b128 v[180:183], %[vw] offset:56320\n" \
  "s_waitcnt lgkmcnt(8)\n" \
  "v_mfma_f32_16x16x32_bf16 %[c3], v[148:151], v[136:139], %[c3]\n" \
  "s_waitcnt lgkmcnt(7)\n" \
  "v_mfma_f32_16x16x32_bf16 %[c4], v[152:155], v[136:139], %[c4]\n" \
  "s_waitcnt lgkmcnt(6)\n" \
  "v_mfma_f32_16x16x32_bf16 %[c5], v[156:159], v[136:139], %[c5]\n" \
  "s_waitcnt vmcnt(10)\n" \
  "s_waitcnt lgkmcnt(5)\n" \
  "v_mfma_f32_16x16x32_bf16 %[c0], v[160:163], v[140:143], %[c0]\n" \
  "s_waitcnt lgkmcnt(4)\n" \
  "v_mfma_f32_16x16x32_bf16 %[c1], v[164:167], v[140:143], %[c1]\n" \
  "s_waitcnt lgkmcnt(3)\n" \
  "v_mfma_f32_16x16x32_bf16 %[c2], v[168:171], v[140:143], %[c2]\n" \
  "s_waitcnt lgkmcnt(2)\n" \
  "v_mfma_f32_16x16x32_bf16 %[c3], v[172:175], v[140:143], %[c3]\n" \
  "s_waitcnt lgkmcnt(1)\n" \
  "v_mfma_f32_16x16x32_bf16 %[c4], v[176:179], v[140:143], %[c4]\n" \
  "s_waitcnt lgkmcnt(0)\n" \
  "v_mfma_f32_16x16x32_bf16 %[c5], v[180:183], v[140:143], %[c5]\n" \
  "s_waitcnt vmcnt(6)\n" \
  "s_nop 7\n" \
  ""
#define P6_CLOB "v64", "v65", "v66", "v67", "v68", "v69", "v70", "v71", "v72", "v73", "v74", "v75", "v76", "v77", "v78", "v79", "v80", "v81", "v82", "v83", "v84", "v85", "v86", "v87", "v88", "v89", "v90", "v91", "v92", "v93", "v94", "v95", "v96", "v97", "v98", "v99", "v100", "v101", "v102", "v103", "v104", "v105", "v106", "v107", "v108", "v109", "v110", "v111", "v112", "v113", "v114", "v115", "v116", "v117", "v118", "v119", "v120", "v121", "v122", "v123", "v124", "v125", "v126", "v127", "v128", "v129", "v130", "v131", "v132", "v133", "v134", "v135", "v136", "v137", "v138", "v139", "v140", "v141", "v142", "v143", "v144", "v145", "v146", "v147", "v148", "v149", "v150", "v151", "v152", "v153", "v154", "v155", "v156", "v157", "v158", "v159", "v160", "v161", "v162", "v163", "v164", "v165", "v166", "v167", "v168", "v169", "v170", "v171", "v172", "v173", "v174", "v175", "v176", "v177", "v178", "v179", "v180", "v181", "v182", "v183"
#define P7_ASM \
  "s_nop 4\n" \
  "buffer_load_dwordx4 v[64:67], %[va0], %[rs], %[sa] offen offset:0 sc1\n" \
  "buffer_load_dwordx4 v[68:71], %[va0], %[rs], %[sa] offen offset:1024 sc1\n" \
  "buffer_load_dwordx4 v[72:75], %[va0], %[rs], %[sa] offen offset:2048 sc1\n" \
  "buffer_load_dwordx4 v[76:79], %[va0], %[rs], %[sa] offen offset:3072 sc1\n" \
  "s_add_u32 %[sa], %[sa], 4096\n" \
  "buffer_load_dwordx4 v[80:83], %[va0], %[rs], %[sa] offen offset:0 sc1\n" \
  "buffer_load_dwordx4 v[84:87], %[va0], %[rs], %[sa] offen offset:1024 sc1\n" \
  "buffer_load_dwordx4 v[88:91], %[va0], %[rs], %[sa] offen offset:2048 sc1\n" \
  "buffer_load_dwordx4 v[92:95], %[va0], %[rs], %[sa] offen offset:3072 sc1\n" \
  "s_add_u32 %[sa], %[sa], 4096\n" \
  "buffer_load_dwordx4 v[96:99], %[va0], %[rs], %[sa] offen offset:0 sc1\n" \
  "buffer_load_dwordx4 v[100:103], %[va0], %[rs], %[sa] offen offset:1024 sc1\n" \
  "buffer_load_dwordx4 v[104:107], %[va0], %[rs], %[sa] offen offset:2048 sc1\n" \
  "buffer_load_dwordx4 v[108:111], %[va0], %[rs], %[sa] offen offset:3072 sc1\n" \
  "s_add_u32 %[sa], %[sa], 4096\n" \
  "buffer_load_dwordx4 v[112:115], %[va0], %[rs], %[sa] offen offset:0 sc1\n" \
  "buffer_load_dwordx4 v[116:119], %[va0], %[rs], %[sa] offen offset:1024 sc1\n" \
  "buffer_load_dwordx4 v[120:123], %[va0], %[rs], %[sa] offen offset:2048 sc1\n" \
  "buffer_load_dwordx4 v[124:127], %[va0], %[rs], %[sa] offen offset:3072 sc1\n" \
  "s_add_u32 %[sa], %[sa], 4096\n" \
  "buffer_load_dwordx4 v[128:131], %[va0], %[rs], %[sa] offen offset:0 sc1\n" \
  "buffer_load_dwordx4 v[132:135], %[va0], %[rs], %[sa] offen offset:1024 sc1\n" \
  "buffer_load_dwordx4 v[136:139], %[va0], %[rs], %[sa] offen offset:2048 sc1\n" \
  "buffer_load_dwordx4 v[140:143], %[va0], %[rs], %[sa] offen offset:3072 sc1\n" \
  "s_add_u32 %[sa], %[sa], 4096\n" \
  "buffer_load_dwordx4 v[144:147], %[va0], %[rs], %[sa] offen offset:0 sc1\n" \
  "buffer_load_dwordx4 v[148:151], %[va0], %[rs], %[sa] offen offset:1024 sc1\n" \
  "buffer_load_dwordx4 v[152:155], %[va0], %[rs], %[sa] offen offset:2048 sc1\n" \
  "buffer_load_dwordx4 v[156:159], %[va0], %[rs], %[sa] offen offset:3072 sc1\n" \
  "v_add_u32 %[vw], 98304, %[vw]\n" \
  "ds_read_b128 v[160:163], %[vw] offset:24576\n" \
  "ds_read_b128 v[164:167], %[vw] offset:25600\n" \
  "ds_read_b128 v[168:171], %[vw] offset:26624\n" \
  "ds_read_b128 v[172:175], %[vw] offset:27648\n" \
  "ds_read_b128 v[176:179], %[vw] offset:28672\n" \
  "ds_read_b128 v[180:183], %[vw] offset:34816\n" \
  "ds_read_b128 v[184:187], %[vw] offset:35840\n" \
  "ds_read_b128 v[188:191], %[vw] offset:36864\n" \
  "s_waitcnt vmcnt(23)\n" \
  "ds_read_b128 v[192:195], %[vw] offset:37888\n" \
  "s_waitcnt lgkmcnt(8)\n" \
  "v_mfma_f32_16x16x32_bf16 %[c0], v[160:163], v[64:67], 0\n" \
  "ds_read_b128 v[196:199], %[vw] offset:38912\n" \
  "s_waitcnt lgkmcnt(8)\n" \
  "v_mfma_f32_16x16x32_bf16 %[c1], v[164:167], v[64:67], 0\n" \
  "ds_read_b128 v[160:163], %[vw] offset:45056\n" \
  "s_waitcnt lgkmcnt(8)\n" \
  "v_mfma_f32_16x16x32_bf16 %[c2], v[168:171], v[64:67], 0\n" \
  "ds_read_b128 v[164:167], %[vw] offset:46080\n" \
  "s_waitcnt lgkmcnt(8)\n" \
  "v_mfma_f32_16x16x32_bf16 %[c3], v[172:175], v[64:67], 0\n" \
  "ds_read_b128 v[168:171], %[vw] offset:47104\n" \
  "s_waitcnt lgkmcnt(8)\n" \
  "v_mfma_f32_16x16x32_bf16 %[c4], v[176:179], v[64:67], 0\n" \
  "s_waitcnt vmcnt(22)\n" \
  "ds_read_b128 v[172:175], %[vw] offset:48128\n" \
  "s_waitcnt lgkmcnt(8)\n" \
  "v_mfma_f32_16x16x32_bf16 %[c0], v[180:183], v[68:71], %[c0]\n" \
  "ds_read_b128 v[176:179], %[vw] offset:49152\n" \
  "s_waitcnt lgkmcnt(8)\n" \
  "v_mfma_f32_16x16x32_bf16 %[c1], v[184:187], v[68:71], %[c1]\n" \
  "v_add_u32 %[vw], 4294868992, %[vw]\n" \
  "ds_read_b128 v[180:183], %[vw] offset:0\n" \
  "s_waitcnt lgkmcnt(8)\n" \
  "v_mfma_f32_16x16x32_bf16 %[c2], v[188:191], v[68:71], %[c2]\n" \
  "ds_read_b128 v[184:187], %[vw] offset:1024\n" \
  "s_waitcnt lgkmcnt(8)\n" \
  "v_mfma_f32_16x16x32_bf16 %[c3], v[192:195], v[68:71], %[c3]\n" \
  "ds_read_b128 v[188:191], %[vw] offset:2048\n" \
  "s_waitcnt lgkmcnt(8)\n" \
  "v_mfma_f32_16x16x32_bf16 %[c4], v[196:199], v[68:71], %[c4]\n" \
  "s_waitcnt vmcnt(21)\n" \
  "ds_read_b128 v[192:195], %[vw] offset:3072\n" \
  "s_waitcnt lgkmcnt(8)\n" \
  "v_mfma_f32_16x16x32_bf16 %[c0], v[160:163], v[72:75], %[c0]\n" \
  "ds_read_b128 v[196:199], %[vw] offset:4096\n" \
  "s_waitcnt lgkmcnt(8)\n" \
  "v_mfma_f32_16x16x32_bf16 %[c1], v[164:167], v[72:75], %[c1]\n" \
  "ds_read_b128 v[160:163], %[vw] offset:10240\n" \
  "s_waitcnt lgkmcnt(8)\n" \
  "v_mfma_f32_16x16x32_bf16 %[c2], v[168:171], v[72:75], %[c2]\n" \
  "ds_read_b128 v[164:167], %[vw] offset:11264\n" \
  "s_waitcnt lgkmcnt(8)\n" \
  "v_mfma_f32_16x16x32_bf16 %[c3], v[172:175], v[72:75], %[c3]\n" \
  "ds_read_b128 v[168:171], %[vw] offset:12288\n" \
  "s_waitcnt lgkmcnt(8)\n" \
  "v_mfma_f32_16x16x32_bf16 %[c4], v[176:179], v[72:75], %[c4]\n" \
  "s_waitcnt vmcnt(20)\n" \
  "ds_read_b128 v[172:175], %[vw] offset:13312\n" \
  "s_waitcnt lgkmcnt(8)\n" \
  "v_mfma_f32_16x16x32_bf16 %[c0], v[180:183], v[76:79], %[c0]\n" \
  "ds_read_b128 v[176:179], %[vw] offset:14336\n" \
  "s_waitcnt lgkmcnt(8)\n" \
  "v_mfma_f32_16x16x32_bf16 %[c1], v[184:187], v[76:79], %[c1]\n" \
  "ds_read_b128 v[180:183], %[vw] offset:20480\n" \
  "s_waitcnt lgkmcnt(8)\n" \
  "v_mfma_f32_16x16x32_bf16 %[c2], v[188:191], v[76:79], %[c2]\n" \
  "ds_read_b128 v[184:187], %[vw] offset:21504\n" \
  "s_waitcnt lgkmcnt(8)\n" \
  "v_mfma_f32_16x16x32_bf16 %[c3], v[192:195], v[76:79], %[c3]\n" \
  "ds_read_b128 v[188:191], %[vw] offset:22528\n" \
  "s_waitcnt lgkmcnt(8)\n" \
  "v_mfma_f32_16x16x32_bf16 %[c4], v[196:199], v[76:79], %[c4]\n" \
  "s_barrier\n" \
  "s_mov_b32 m0, %[dm0]\n" \
  "s_nop 0\n" \
  "buffer_load_dwordx4 %[l16], %[rs], %[ds0] offen offset:0 sc1 lds\n" \
  "buffer_load_dwordx4 %[l16], %[rs], %[ds0] offen offset:1024 sc1 lds\n" \
  "buffer_load_dwordx4 %[l16], %[rs], %[ds0] offen offset:2048 sc1 lds\n" \
  "buffer_load_dwordx4 %[l16], %[rs], %[ds0] offen offset:3072 sc1 lds\n" \
  "s_add_u32 %[dm0], %[dm0], 4096\n" \
  "s_add_u32 %[ds0], %[ds0], 4096\n" \
  "s_mov_b32 m0, %[dm0]\n" \
  "s_nop 0\n" \
  "buffer_load_dwordx4 %[l16], %[rs], %[ds0] offen offset:0 sc1 lds\n" \
  "s_waitcnt vmcnt(24)\n" \
  "ds_read_b128 v[192:195], %[vw] offset:23552\n" \
  "s_waitcnt lgkmcnt(8)\n" \
  "v_mfma_f32_16x16x32_bf16 %[c0], v[160:163], v[80:83], %[c0]\n" \
  "ds_read_b128 v[196:199], %[vw] offset:24576\n" \
  "s_waitcnt lgkmcnt(8)\n" \
  "v_mfma_f32_16x16x32_bf16 %[c1], v[164:167], v[80:83], %[c1]\n" \
  "ds_read_b128 v[160:163], %[vw] offset:30720\n" \
  "s_waitcnt lgkmcnt(8)\n" \
  "v_mfma_f32_16x16x32_bf16 %[c2], v[168:171], v[80:83], %[c2]\n" \
  "ds_read_b128 v[164:167], %[vw] offset:31744\n" \
  "s_waitcnt lgkmcnt(8)\n" \
  "v_mfma_f32_16x16x32_bf16 %[c3], v[172:175], v[80:83], %[c3]\n" \
  "ds_read_b128 v[168:171], %[vw] offset:32768\n" \
  "s_waitcnt lgkmcnt(8)\n" \
  "v_mfma_f32_16x16x32_bf16 %[c4], v[176:179], v[80:83], %[c4]\n" \
  "s_waitcnt vmcnt(23)\n" \
  "ds_read_b128 v[172:175], %[vw] offset:33792\n" \
  "s_waitcnt lgkmcnt(8)\n" \
  "v_mfma_f32_16x16x32_bf16 %[c0], v[180:183], v[84:87], %[c0]\n" \
  "ds_read_b128 v[176:179], %[vw] offset:34816\n" \
  "s_waitcnt lgkmcnt(8)\n" \
  "v_mfma_f32_16x16x32_bf16 %[c1], v[184:187], v[84:87], %[c1]\n" \
  "ds_read_b128 v[180:183], %[vw] offset:40960\n" \
  "s_waitcnt lgkmcnt(8)\n" \
  "v_mfma_f32_16x16x32_bf16 %[c2], v[188:191], v[84:87], %[c2]\n" \
  "ds_read_b128 v[184:187], %[vw] offset:41984\n" \
  "s_waitcnt lgkmcnt(8)\n" \
  "v_mfma_f32_16x16x32_bf16 %[c3], v[192:195], v[84:87], %[c3]\n" \
  "ds_read_b128 v[188:191], %[vw] offset:43008\n" \
  "s_waitcnt lgkmcnt(8)\n" \
  "v_mfma_f32_16x16x32_bf16 %[c4], v[196:199], v[84:87], %[c4]\n" \
  "s_waitcnt vmcnt(22)\n" \
  "ds_read_b128 v[192:195], %[vw] offset:44032\n" \
  "s_waitcnt lgkmcnt(8)\n" \
  "v_mfma_f32_16x16x32_bf16 %[c0], v[160:163], v[88:91], %[c0]\n" \
  "ds_read_b128 v[196:199], %[vw] offset:45056\n" \
  "s_waitcnt lgkmcnt(8)\n" \
  "v_mfma_f32_16x16x32_bf16 %[c1], v[164:167], v[88:91], %[c1]\n" \
  "ds_read_b128 v[160:163], %[vw] offset:51200\n" \
  "s_waitcnt lgkmcnt(8)\n" \
  "v_mfma_f32_16x16x32_bf16 %[c2], v[168:171], v[88:91], %[c2]\n" \
  "ds_read_b128 v[164:167], %[vw] offset:52224\n" \
  "s_waitcnt lgkmcnt(8)\n" \
  "v_mfma_f32_16x16x32_bf16 %[c3], v[172:175], v[88:91], %[c3]\n" \
  "ds_read_b128 v[168:171], %[vw] offset:53248\n" \
  "s_waitcnt lgkmcnt(8)\n" \
  "v_mfma_f32_16x16x32_bf16 %[c4], v[176:179], v[88:91], %[c4]\n" \
  "s_waitcnt vmcnt(21)\n" \
  "ds_read_b128 v[172:175], %[vw] offset:54272\n" \
  "s_waitcnt lgkmcnt(8)\n" \
  "v_mfma_f32_16x16x32_bf16 %[c0], v[180:183], v[92:95], %[c0]\n" \
  "ds_read_b128 v[176:179], %[vw] offset:55296\n" \
  "s_waitcnt lgkmcnt(8)\n" \
  "v_mfma_f32_16x16x32_bf16 %[c1], v[184:187], v[92:95], %[c1]\n" \
  "ds_read_b128 v[180:183], %[vw] offset:61440\n" \
  "s_waitcnt lgkmcnt(8)\n" \
  "v_mfma_f32_16x16x32_bf16 %[c2], v[188:191], v[92:95], %[c2]\n" \
  "ds_read_b128 v[184:187], %[vw] offset:62464\n" \
  "s_waitcnt lgkmcnt(8)\n" \
  "v_mfma_f32_16x16x32_bf16 %[c3], v[192:195], v[92:95], %[c3]\n" \
  "ds_read_b128 v[188:191], %[vw] offset:63488\n" \
  "s_waitcnt lgkmcnt(8)\n" \
  "v_mfma_f32_16x16x32_bf16 %[c4], v[196:199], v[92:95], %[c4]\n" \
  "s_waitcnt vmcnt(20)\n" \
  "ds_read_b128 v[192:195], %[vw] offset:64512\n" \
  "s_waitcnt lgkmcnt(8)\n" \
  "v_mfma_f32_16x16x32_bf16 %[c0], v[160:163], v[96:99], %[c0]\n" \
  "v_add_u32 %[vw], 65536, %[vw]\n" \
  "ds_read_b128 v[196:199], %[vw] offset:0\n" \
  "s_waitcnt lgkmcnt(8)\n" \
  "v_mfma_f32_16x16x32_bf16 %[c1], v[164:167], v[96:99], %[c1]\n" \
  "ds_read_b128 v[160:163], %[vw] offset:6144\n" \
  "s_waitcnt lgkmcnt(8)\n" \
  "v_mfma_f32_16x16x32_bf16 %[c2], v[168:171], v[96:99], %[c2]\n" \
  "ds_read_b128 v[164:167], %[vw] offset:7168\n" \
  "s_waitcnt lgkmcnt(8)\n" \
  "v_mfma_f32_16x16x32_bf16 %[c3], v[172:175], v[96:99], %[c3]\n" \
  "ds_read_b128 v[168:171], %[vw] offset:8192\n" \
  "s_waitcnt lgkmcnt(8)\n" \
  "v_mfma_f32_16x16x32_bf16 %[c4], v[176:179], v[96:99], %[c4]\n" \
  "s_waitcnt vmcnt(19)\n" \
  "ds_read_b128 v[172:175], %[vw] offset:9216\n" \
  "s_waitcnt lgkmcnt(8)\n" \
  "v_mfma_f32_16x16x32_bf16 %[c0], v[180:183], v[100:103], %[c0]\n" \
  "ds_read_b128 v[176:179], %[vw] offset:10240\n" \
  "s_waitcnt lgkmcnt(8)\n" \
  "v_mfma_f32_16x16x32_bf16 %[c1], v[184:187], v[100:103], %[c1]\n" \
  "ds_read_b128 v[180:183], %[vw] offset:16384\n" \
  "s_waitcnt lgkmcnt(8)\n" \
  "v_mfma_f32_16x16x32_bf16 %[c2], v[188:191], v[100:103], %[c2]\n" \
  "ds_read_b128 v[184:187], %[vw] offset:17408\n" \
  "s_waitcnt lgkmcnt(8)\n" \
  "v_mfma_f32_16x16x32_bf16 %[c3], v[192:195], v[100:103], %[c3]\n" \
  "ds_read_b128 v[188:191], %[vw] offset:18432\n" \
  "s_waitcnt lgkmcnt(8)\n" \
  "v_mfma_f32_16x16x32_bf16 %[c4], v[196:199], v[100:103], %[c4]\n" \
  "s_barrier\n" \
  "s_mov_b32 m0, %[dm1]\n" \
  "s_nop 0\n" \
  "buffer_load_dwordx4 %[l16], %[rs], %[ds1] offen offset:0 sc1 lds\n" \
  "buffer_load_dwordx4 %[l16], %[rs], %[ds1] offen offset:1024 sc1 lds\n" \
  "buffer_load_dwordx4 %[l16], %[rs], %[ds1] offen offset:2048 sc1 lds\n" \
  "buffer_load_dwordx4 %[l16], %[rs], %[ds1] offen offset:3072 sc1 lds\n" \
  "s_add_u32 %[dm1], %[dm1], 4096\n" \
  "s_add_u32 %[ds1], %[ds1], 4096\n" \
  "s_mov_b32 m0, %[dm1]\n" \
  "s_nop 0\n" \
  "buffer_load_dwordx4 %[l16], %[rs], %[ds1] offen offset:0 sc1 lds\n" \
  "buffer_load_dwordx4 %[l16], %[rs], %[ds1] offen offset:1024 sc1 lds\n" \
  "buffer_load_dwordx4 %[l16], %[rs], %[ds1] offen offset:2048 sc1 lds\n" \
  "s_waitcnt vmcnt(25)\n" \
  "ds_read_b128 v[192:195], %[vw] offset:19456\n" \
  "s_waitcnt lgkmcnt(8)\n" \
  "v_mfma_f32_16x16x32_bf16 %[c0], v[160:163], v[104:107], %[c0]\n" \
  "ds_read_b128 v[196:199], %[vw] offset:20480\n" \
  "s_waitcnt lgkmcnt(8)\n" \
  "v_mfma_f32_16x16x32_bf16 %[c1], v[164:167], v[104:107], %[c1]\n" \
  "ds_read_b128 v[160:163], %[vw] offset:26624\n" \
  "s_waitcnt lgkmcnt(8)\n" \
  "v_mfma_f32_16x16x32_bf16 %[c2], v[168:171], v[104:107], %[c2]\n" \
  "ds_read_b128 v[164:167], %[vw] offset:27648\n" \
  "s_waitcnt lgkmcnt(8)\n" \
  "v_mfma_f32_16x16x32_bf16 %[c3], v[172:175], v[104:107], %[c3]\n" \
  "ds_read_b128 v[168:171], %[vw] offset:28672\n" \
  "s_waitcnt lgkmcnt(8)\n" \
  "v_mfma_f32_16x16x32_bf16 %[c4], v[176:179], v[104:107], %[c4]\n" \
  "s_waitcnt vmcnt(24)\n" \
  "ds_read_b128 v[172:175], %[vw] offset:29696\n" \
  "s_waitcnt lgkmcnt(8)\n" \
  "v_mfma_f32_16x16x32_bf16 %[c0], v[180:183], v[108:111], %[c0]\n" \
  "ds_read_b128 v[176:179], %[vw] offset:30720\n" \
  "s_waitcnt lgkmcnt(8)\n" \
  "v_mfma_f32_16x16x32_bf16 %[c1], v[184:187], v[108:111], %[c1]\n" \
  "ds_read_b128 v[180:183], %[vw] offset:36864\n" \
  "s_waitcnt lgkmcnt(8)\n" \
  "v_mfma_f32_16x16x32_bf16 %[c2], v[188:191], v[108:111], %[c2]\n" \
  "ds_read_b128 v[184:187], %[vw] offset:37888\n" \
  "s_waitcnt lgkmcnt(8)\n" \
  "v_mfma_f32_16x16x32_bf16 %[c3], v[192:195], v[108:111], %[c3]\n" \
  "ds_read_b128 v[188:191], %[vw] offset:38912\n" \
  "s_waitcnt lgkmcnt(8)\n" \
  "v_mfma_f32_16x16x32_bf16 %[c4], v[196:199], v[108:111], %[c4]\n" \
  "s_waitcnt vmcnt(23)\n" \
  "ds_read_b128 v[192:195], %[vw] offset:39936\n" \
  "s_waitcnt lgkmcnt(8)\n" \
  "v_mfma_f32_16x16x32_bf16 %[c0], v[160:163], v[112:115], %[c0]\n" \
  "ds_read_b128 v[196:199], %[vw] offset:40960\n" \
  "s_waitcnt lgkmcnt(8)\n" \
  "v_mfma_f32_16x16x32_bf16 %[c1], v[164:167], v[112:115], %[c1]\n" \
  "ds_read_b128 v[160:163], %[vw] offset:47104\n" \
  "s_waitcnt lgkmcnt(8)\n" \
  "v_mfma_f32_16x16x32_bf16 %[c2], v[168:171], v[112:115], %[c2]\n" \
  "ds_read_b128 v[164:167], %[vw] offset:48128\n" \
  "s_waitcnt lgkmcnt(8)\n" \
  "v_mfma_f32_16x16x32_bf16 %[c3], v[172:175], v[112:115], %[c3]\n" \
  "ds_read_b128 v[168:171], %[vw] offset:49152\n" \
  "s_waitcnt lgkmcnt(8)\n" \
  "v_mfma_f32_16x16x32_bf16 %[c4], v[176:179], v[112:115], %[c4]\n" \
  "s_waitcnt vmcnt(22)\n" \
  "ds_read_b128 v[172:175], %[vw] offset:50176\n" \
  "s_waitcnt lgkmcnt(8)\n" \
  "v_mfma_f32_16x16x32_bf16 %[c0], v[180:183], v[116:119], %[c0]\n" \
  "ds_read_b128 v[176:179], %[vw] offset:51200\n" \
  "s_waitcnt lgkmcnt(8)\n" \
  "v_mfma_f32_16x16x32_bf16 %[c1], v[184:187], v[116:119], %[c1]\n" \
  "s_waitcnt vmcnt(7)\n" \
  "s_barrier\n" \
  "ds_read_b128 v[180:183], %[vw] offset:57344\n" \
  "s_waitcnt lgkmcnt(8)\n" \
  "v_mfma_f32_16x16x32_bf16 %[c2], v[188:191], v[116:119], %[c2]\n" \
  "ds_read_b128 v[184:187], %[vw] offset:58368\n" \
  "s_waitcnt lgkmcnt(8)\n" \
  "v_mfma_f32_16x16x32_bf16 %[c3], v[192:195], v[116:119], %[c3]\n" \
  "ds_read_b128 v[188:191], %[vw] offset:59392\n" \
  "s_waitcnt lgkmcnt(8)\n" \
  "v_mfma_f32_16x16x32_bf16 %[c4], v[196:199], v[116:119], %[c4]\n" \
  "s_waitcnt vmcnt(21)\n" \
  "ds_read_b128 v[192:195], %[vw] offset:60416\n" \
  "s_waitcnt lgkmcnt(8)\n" \
  "v_mfma_f32_16x16x32_bf16 %[c0], v[160:163], v[120:123], %[c0]\n" \
  "ds_read_b128 v[196:199], %[vw] offset:61440\n" \
  "s_waitcnt lgkmcnt(8)\n" \
  "v_mfma_f32_16x16x32_bf16 %[c1], v[164:167], v[120:123], %[c1]\n" \
  "v_add_u32 %[vw], 65536, %[vw]\n" \
  "ds_read_b128 v[160:163], %[vw] offset:2048\n" \
  "s_waitcnt lgkmcnt(8)\n" \
  "v_mfma_f32_16x16x32_bf16 %[c2], v[168:171], v[120:123], %[c2]\n" \
  "ds_read_b128 v[164:167], %[vw] offset:3072\n" \
  "s_waitcnt lgkmcnt(8)\n" \
  "v_mfma_f32_16x16x32_bf16 %[c3], v[172:175], v[120:123], %[c3]\n" \
  "ds_read_b128 v[168:171], %[vw] offset:4096\n" \
  "s_waitcnt lgkmcnt(8)\n" \
  "v_mfma_f32_16x16x32_bf16 %[c4], v[176:179], v[120:123], %[c4]\n" \
  "s_waitcnt vmcnt(20)\n" \
  "ds_read_b128 v[172:175], %[vw] offset:5120\n" \
  "s_waitcnt lgkmcnt(8)\n" \
  "v_mfma_f32_16x16x32_bf16 %[c0], v[180:183], v[124:127], %[c0]\n" \
  "ds_read_b128 v[176:179], %[vw] offset:6144\n" \
  "s_waitcnt lgkmcnt(8)\n" \
  "v_mfma_f32_16x16x32_bf16 %[c1], v[184:187], v[124:127], %[c1]\n" \
  "ds_read_b128 v[180:183], %[vw] offset:12288\n" \
  "s_waitcnt lgkmcnt(8)\n" \
  "v_mfma_f32_16x16x32_bf16 %[c2], v[188:191], v[124:127], %[c2]\n" \
  "ds_read_b128 v[184:187], %[vw] offset:13312\n" \
  "s_waitcnt lgkmcnt(8)\n" \
  "v_mfma_f32_16x16x32_bf16 %[c3], v[192:195], v[124:127], %[c3]\n" \
  "ds_read_b128 v[188:191], %[vw] offset:14336\n" \
  "s_waitcnt lgkmcnt(8)\n" \
  "v_mfma_f32_16x16x32_bf16 %[c4], v[196:199], v[124:127], %[c4]\n" \
  "s_waitcnt vmcnt(19)\n" \
  "ds_read_b128 v[192:195], %[vw] offset:15360\n" \
  "s_waitcnt lgkmcnt(8)\n" \
  "v_mfma_f32_16x16x32_bf16 %[c0], v[160:163], v[128:131], %[c0]\n" \
  "ds_read_b128 v[196:199], %[vw] offset:16384\n" \
  "s_waitcnt lgkmcnt(8)\n" \
  "v_mfma_f32_16x16x32_bf16 %[c1], v[164:167], v[128:131], %[c1]\n" \
  "v_add_u32 %[vw], 4294836224, %[vw]\n" \
  "ds_read_b128 v[160:163], %[vw] offset:0\n" \
  "s_waitcnt lgkmcnt(8)\n" \
  "v_mfma_f32_16x16x32_bf16 %[c2], v[168:171], v[128:131], %[c2]\n" \
  "ds_read_b128 v[164:167], %[vw] offset:1024\n" \
  "s_waitcnt lgkmcnt(8)\n" \
  "v_mfma_f32_16x16x32_bf16 %[c3], v[172:175], v[128:131], %[c3]\n" \
  "ds_read_b128 v[168:171], %[vw] offset:2048\n" \
  "s_waitcnt lgkmcnt(8)\n" \
  "v_mfma_f32_16x16x32_bf16 %[c4], v[176:179], v[128:131], %[c4]\n" \
  "s_waitcnt vmcnt(18)\n" \
  "ds_read_b128 v[172:175], %[vw] offset:3072\n" \
  "s_waitcnt lgkmcnt(8)\n" \
  "v_mfma_f32_16x16x32_bf16 %[c0], v[180:183], v[132:135], %[c0]\n" \
  "ds_read_b128 v[176:179], %[vw] offset:4096\n" \
  "s_waitcnt lgkmcnt(8)\n" \
  "v_mfma_f32_16x16x32_bf16 %[c1], v[184:187], v[132:135], %[c1]\n" \
  "s_waitcnt vmcnt(0)\n" \
  "s_barrier\n" \
  "ds_read_b128 v[180:183], %[vw] offset:10240\n" \
  "s_waitcnt lgkmcnt(8)\n" \
  "v_mfma_f32_16x16x32_bf16 %[c2], v[188:191], v[132:135], %[c2]\n" \
  "ds_read_b128 v[184:187], %[vw] offset:11264\n" \
  "s_waitcnt lgkmcnt(8)\n" \
  "v_mfma_f32_16x16x32_bf16 %[c3], v[192:195], v[132:135], %[c3]\n" \
  "ds_read_b128 v[188:191], %[vw] offset:12288\n" \
  "s_waitcnt lgkmcnt(8)\n" \
  "v_mfma_f32_16x16x32_bf16 %[c4], v[196:199], v[132:135], %[c4]\n" \
  "s_waitcnt vmcnt(17)\n" \
  "ds_read_b128 v[192:195], %[vw] offset:13312\n" \
  "s_waitcnt lgkmcnt(8)\n" \
  "v_mfma_f32_16x16x32_bf16 %[c0], v[160:163], v[136:139], %[c0]\n" \
  "ds_read_b128 v[196:199], %[vw] offset:14336\n" \
  "s_waitcnt lgkmcnt(8)\n" \
  "v_mfma_f32_16x16x32_bf16 %[c1], v[164:167], v[136:139], %[c1]\n" \
  "ds_read_b128 v[160:163], %[vw] offset:20480\n" \
  "s_waitcnt lgkmcnt(8)\n" \
  "v_mfma_f32_16x16x32_bf16 %[c2], v[168:171], v[136:139], %[c2]\n" \
  "ds_read_b128 v[164:167], %[vw] offset:21504\n" \
  "s_waitcnt lgkmcnt(8)\n" \
  "v_mfma_f32_16x16x32_bf16 %[c3], v[172:175], v[136:139], %[c3]\n" \
  "ds_read_b128 v[168:171], %[vw] offset:22528\n" \
  "s_waitcnt lgkmcnt(8)\n" \
  "v_mfma_f32_16x16x32_bf16 %[c4], v[176:179], v[136:139], %[c4]\n" \
  "s_waitcnt vmcnt(16)\n" \
  "ds_read_b128 v[172:175], %[vw] offset:23552\n" \
  "s_waitcnt lgkmcnt(8)\n" \
  "v_mfma_f32_16x16x32_bf16 %[c0], v[180:183], v[140:143], %[c0]\n" \
  "ds_read_b128 v[176:179], %[vw] offset:24576\n" \
  "s_waitcnt lgkmcnt(8)\n" \
  "v_mfma_f32_16x16x32_bf16 %[c1], v[184:187], v[140:143], %[c1]\n" \
  "ds_read_b128 v[180:183], %[vw] offset:30720\n" \
  "s_waitcnt lgkmcnt(8)\n" \
  "v_mfma_f32_16x16x32_bf16 %[c2], v[188:191], v[140:143], %[c2]\n" \
  "ds_read_b128 v[184:187], %[vw] offset:31744\n" \
  "s_waitcnt lgkmcnt(8)\n" \
  "v_mfma_f32_16x16x32_bf16 %[c3], v[192:195], v[140:143], %[c3]\n" \
  "ds_read_b128 v[188:191], %[vw] offset:32768\n" \
  "s_waitcnt lgkmcnt(8)\n" \
  "v_mfma_f32_16x16x32_bf16 %[c4], v[196:199], v[140:143], %[c4]\n" \
  "s_waitcnt vmcnt(15)\n" \
  "ds_read_b128 v[192:195], %[vw] offset:33792\n" \
  "s_waitcnt lgkmcnt(8)\n" \
  "v_mfma_f32_16x16x32_bf16 %[c0], v[160:163], v[144:147], %[c0]\n" \
  "ds_read_b128 v[196:199], %[vw] offset:34816\n" \
  "s_waitcnt lgkmcnt(8)\n" \
  "v_mfma_f32_16x16x32_bf16 %[c1], v[164:167], v[144:147], %[c1]\n" \
  "ds_read_b128 v[160:163], %[vw] offset:40960\n" \
  "s_waitcnt lgkmcnt(8)\n" \
  "v_mfma_f32_16x16x32_bf16 %[c2], v[168:171], v[144:147], %[c2]\n" \
  "ds_read_b128 v[164:167], %[vw] offset:41984\n" \
  "s_waitcnt lgkmcnt(8)\n" \
  "v_mfma_f32_16x16x32_bf16 %[c3], v[172:175], v[144:147], %[c3]\n" \
  "ds_read_b128 v[168:171], %[vw] offset:43008\n" \
  "s_waitcnt lgkmcnt(8)\n" \
  "v_mfma_f32_16x16x32_bf16 %[c4], v[176:179], v[144:147], %[c4]\n" \
  "s_waitcnt vmcnt(14)\n" \
  "ds_read_b128 v[172:175], %[vw] offset:44032\n" \
  "s_waitcnt lgkmcnt(8)\n" \
  "v_mfma_f32_16x16x32_bf16 %[c0], v[180:183], v[148:151], %[c0]\n" \
  "ds_read_b128 v[176:179], %[vw] offset:45056\n" \
  "s_waitcnt lgkmcnt(8)\n" \
  "v_mfma_f32_16x16x32_bf16 %[c1], v[184:187], v[148:151], %[c1]\n" \
  "ds_read_b128 v[180:183], %[vw] offset:51200\n" \
  "s_waitcnt lgkmcnt(8)\n" \
  "v_mfma_f32_16x16x32_bf16 %[c2], v[188:191], v[148:151], %[c2]\n" \
  "ds_read_b128 v[184:187], %[vw] offset:52224\n" \
  "s_waitcnt lgkmcnt(8)\n" \
  "v_mfma_f32_16x16x32_bf16 %[c3], v[192:195], v[148:151], %[c3]\n" \
  "ds_read_b128 v[188:191], %[vw] offset:53248\n" \
  "s_waitcnt lgkmcnt(8)\n" \
  "v_mfma_f32_16x16x32_bf16 %[c4], v[196:199], v[148:151], %[c4]\n" \
  "s_waitcnt vmcnt(13)\n" \
  "ds_read_b128 v[192:195], %[vw] offset:54272\n" \
  "s_waitcnt lgkmcnt(8)\n" \
  "v_mfma_f32_16x16x32_bf16 %[c0], v[160:163], v[152:155], %[c0]\n" \
  "ds_read_b128 v[196:199], %[vw] offset:55296\n" \
  "s_waitcnt lgkmcnt(8)\n" \
  "v_mfma_f32_16x16x32_bf16 %[c1], v[164:167], v[152:155], %[c1]\n" \
  "s_waitcnt lgkmcnt(7)\n" \
  "v_mfma_f32_16x16x32_bf16 %[c2], v[168:171], v[152:155], %[c2]\n" \
  "s_waitcnt lgkmcnt(6)\n" \
  "v_mfma_f32_16x16x32_bf16 %[c3], v[172:175], v[152:155], %[c3]\n" \
  "s_waitcnt lgkmcnt(5)\n" \
  "v_mfma_f32_16x16x32_bf16 %[c4], v[176:179], v[152:155], %[c4]\n" \
  "s_waitcnt vmcnt(12)\n" \
  "s_waitcnt lgkmcnt(4)\n" \
  "v_mfma_f32_16x16x32_bf16 %[c0], v[180:183], v[156:159], %[c0]\n" \
  "s_waitcnt lgkmcnt(3)\n" \
  "v_mfma_f32_16x16x32_bf16 %[c1], v[184:187], v[156:159], %[c1]\n" \
  "s_waitcnt lgkmcnt(2)\n" \
  "v_mfma_f32_16x16x32_bf16 %[c2], v[188:191], v[156:159], %[c2]\n" \
  "s_waitcnt lgkmcnt(1)\n" \
  "v_mfma_f32_16x16x32_bf16 %[c3], v[192:195], v[156:159], %[c3]\n" \
  "s_waitcnt lgkmcnt(0)\n" \
  "v_mfma_f32_16x16x32_bf16 %[c4], v[196:199], v[156:159], %[c4]\n" \
  "s_nop 7\n" \
  ""
#define P7_CLOB "v64", "v65", "v66", "v67", "v68", "v69", "v70", "v71", "v72", "v73", "v74", "v75", "v76", "v77", "v78", "v79", "v80", "v81", "v82", "v83", "v84", "v85", "v86", "v87", "v88", "v89", "v90", "v91", "v92", "v93", "v94", "v95", "v96", "v97", "v98", "v99", "v100", "v101", "v102", "v103", "v104", "v105", "v106", "v107", "v108", "v109", "v110", "v111", "v112", "v113", "v114", "v115", "v116", "v117", "v118", "v119", "v120", "v121", "v122", "v123", "v124", "v125", "v126", "v127", "v128", "v129", "v130", "v131", "v132", "v133", "v134", "v135", "v136", "v137", "v138", "v139", "v140", "v141", "v142", "v143", "v144", "v145", "v146", "v147", "v148", "v149", "v150", "v151", "v152", "v153", "v154", "v155", "v156", "v157", "v158", "v159", "v160", "v161", "v162", "v163", "v164", "v165", "v166", "v167", "v168", "v169", "v170", "v171", "v172", "v173", "v174", "v175", "v176", "v177", "v178", "v179", "v180", "v181", "v182", "v183", "v184", "v185", "v186", "v187", "v188", "v189", "v190", "v191", "v192", "v193", "v194", "v195", "v196", "v197", "v198", "v199"

constexpr int S = 2048, D = 640, F = 1536;
constexpr float EPS = 1e-5f;
constexpr unsigned OOB = 0x80000000u;
constexpr int FLG_OFF = 4096;
constexpr int XST_OFF = FLG_OFF + 7 * 256 * 256;
constexpr int HST_OFF = XST_OFF + S * 16;
constexpr int X1ST_OFF = HST_OFF + 128 * 2048;
constexpr int CS1_OFF = X1ST_OFF + 128 * 2048;
constexpr int CS3_OFF = CS1_OFF + 20 * D * 4;
constexpr int Q16_OFF = CS3_OFF + 20 * F * 4;
constexpr int Q8_OFF = Q16_OFF + 32 * 64 * 1024;
constexpr int KV_OFF = Q8_OFF + 32 * 64 * 256;
constexpr int O_OFF = KV_OFF + (32 * 64 + 4) * 2816;
constexpr int H_OFF = O_OFF + S * D * 2;
constexpr int G_OFF = H_OFF + S * D * 2;
constexpr int X1_OFF = G_OFF + S * D * 2;
constexpr int U_OFF = X1_OFF + S * D * 2;
constexpr int WG_OFF = U_OFF + S * F * 2;
constexpr int WG_STR = 548 * 1024;
constexpr int W3_OFF = WG_OFF + 8 * WG_STR;
constexpr int DBG_OFF = W3_OFF + 16 * 120 * 1024;
constexpr int WS_END = DBG_OFF + 256 * 8 * 128;
enum { FK_P1 = 0, FK_R = 1, FK_P2 = 2, FK_P3 = 3, FK_P4 = 4, FK_P5 = 5, FK_P6 = 6 };
constexpr int LDS_BYTES = 163840;
constexpr int RING = 150;
constexpr int MISC = RING * 1024;
constexpr int L_CS1 = MISC, L_CS3 = MISC + 4 * 80 * 4, L_VOTE = L_CS3 + 4 * 96 * 4, L_MAX = L_VOTE + 64;
constexpr unsigned SPIN_CAP = 1u << 17;

struct Params {
  const u16 *x, *wqkv, *wo, *w1, *w2, *w3, *w4;
  u16* out;
  unsigned char* ws;
};

DEV rsrc_t mk_rsrc(const void* p, int bytes) { return __builtin_amdgcn_make_buffer_rsrc((void*)p, 0, bytes, 0x00020000); }
DEV float bflo(unsigned v) { return __uint_as_float(v << 16); }
DEV float bfhi(unsigned v) { return __uint_as_float(v & 0xffff0000u); }
DEV unsigned pk2(float a, float b) {
  bf16x2 r = {(__bf16)a, (__bf16)b};
  return __builtin_bit_cast(unsigned, r);
}
DEV float xsum16(float v) { return v + __shfl_xor(v, 16); }
DEV float quadsum(float v) { v += __shfl_xor(v, 16); return v + __shfl_xor(v, 32); }
DEV float dot1(unsigned v, float c) { return __builtin_amdgcn_fdot2_f32_bf16(__builtin_bit_cast(bf16x2, v), __builtin_bit_cast(bf16x2, 0x3f803f80u), c, false); }
DEV f32x4 mfma16(v4u a, v4u b, f32x4 c) { return __builtin_amdgcn_mfma_f32_16x16x32_bf16(__builtin_bit_cast(bf16x8, a), __builtin_bit_cast(bf16x8, b), c, 0, 0, 0); }
DEV v2u trrd(LAS char* p) { return __builtin_bit_cast(v2u, __builtin_amdgcn_ds_read_tr16_b64_v4i16((LAS s16x4*)p)); }
DEV void st16(rsrc_t rs, unsigned vo, v4u v) { __builtin_amdgcn_raw_buffer_store_b128(v, rs, vo, 0, 16); }
DEV void st8(rsrc_t rs, unsigned vo, v2u v) { __builtin_amdgcn_raw_buffer_store_b64(v, rs, vo, 0, 16); }
DEV v4u swap_pair(v2u a, v2u b) {
  auto r0 = __builtin_amdgcn_permlane16_swap(a.x, b.x, false, false);
  auto r1 = __builtin_amdgcn_permlane16_swap(a.y, b.y, false, false);
  return v4u{r0[0], r1[0], r0[1], r1[1]};
}
DEV v2u pk4(f32x4 v) { return v2u{pk2(v.x, v.y), pk2(v.z, v.w)}; }
DEV f32x4 unpk4(v2u v) { return f32x4{bflo(v.x), bfhi(v.x), bflo(v.y), bfhi(v.y)}; }
DEV float gelu1(float t) {
  const float c0 = -2.f * 0.7978845608028654f * 1.4426950408889634f, c1 = c0 * 0.044715f;
  float e = __builtin_amdgcn_exp2f(t * __builtin_fmaf(t * t, c1, c0));
  return t * __builtin_amdgcn_rcpf(1.f + e);
}
DEV f32x4 gelu4(f32x4 t) { return f32x4{gelu1(t.x), gelu1(t.y), gelu1(t.z), gelu1(t.w)}; }
template <bool DEVSCOPE> DEV void dma1(rsrc_t rs, unsigned vo, unsigned so, unsigned m0v) {
  if (DEVSCOPE) asm volatile("s_mov_b32 m0, %2\n\ts_nop 3\n\tbuffer_load_dwordx4 %0, %1, %3 offen sc1 lds" ::"v"(vo), "s"(rs), "s"(m0v), "s"(so) : "memory");
  else asm volatile("s_mov_b32 m0, %2\n\ts_nop 3\n\tbuffer_load_dwordx4 %0, %1, %3 offen lds" ::"v"(vo), "s"(rs), "s"(m0v), "s"(so) : "memory");
}
#define WAIT_VM(n) asm volatile("s_waitcnt vmcnt(" #n ")" ::: "memory")
#define BARRIER() asm volatile("s_waitcnt lgkmcnt(0)\n\ts_barrier" ::: "memory")
#define DRAIN_BARRIER() asm volatile("s_waitcnt vmcnt(0) lgkmcnt(0)\n\ts_barrier" ::: "memory")

struct Ctx {
  rsrc_t ws;
  GAS unsigned char* wsp;
  u64 tag;
  int b, w, l, g, G;
};
DEV unsigned stream_src(const Ctx& c, int n) {
  return n < 300 ? WG_OFF + c.g * WG_STR + n * 1024 : n < 420 ? W3_OFF + (c.G * 120 + n - 300) * 1024 : WG_OFF + c.g * WG_STR + (n - 120) * 1024;
}
template <int W0> DEV void dma_batch(const Ctx& c, int n0, int n1) {
  if (c.w >= W0)
    for (int n = n0 + c.w - W0; n < n1; n += 8 - W0) dma1<true>(c.ws, c.l * 16, stream_src(c, n), (n % RING) * 1024);
}
DEV void set_flag(const Ctx& c, int kind) {
  if (threadIdx.x == 0) __hip_atomic_store((GAS u64*)(c.wsp + FLG_OFF + (kind * 256 + c.b) * 256), c.tag, __ATOMIC_RELAXED, __HIP_MEMORY_SCOPE_AGENT);
}
DEV void poll_flags(const Ctx& c, int kind, int blk) {
  GAS u64* f = (GAS u64*)(c.wsp + FLG_OFF + (kind * 256 + blk) * 256);
  for (unsigned spins = 0; spins < SPIN_CAP; ++spins)
    if (__all(__hip_atomic_load(f, __ATOMIC_RELAXED, __HIP_MEMORY_SCOPE_AGENT) == c.tag)) break;
  __builtin_amdgcn_fence(__ATOMIC_ACQUIRE, "wavefront");
}
#define STAMP(i) do {} while (0)
#define FINISH() do {} while (0)

template <bool OA, bool OB>
DEV void p1_loop(LAS char* sm, rsrc_t rx, rsrc_t rw, rsrc_t rws, unsigned xvo, unsigned wvo0, unsigned wvo1, unsigned stvo, int w,
                 unsigned xa0, unsigned xa1, unsigned wa, unsigned wb, f32x4 (&acc)[2][4], float (&cs)[2], v4u& st) {
#pragma unroll 1
  for (int kt = 0; kt < 10; ++kt) {
    if (kt == 0) WAIT_VM(17); else if (kt < 4) WAIT_VM(13); else if (kt < 8) WAIT_VM(8); else if (kt == 8) WAIT_VM(5); else WAIT_VM(1);
    asm volatile("s_barrier" ::: "memory");
    if (kt >= 1 && kt <= 6 && !(MK_EXP & 1)) {
      const unsigned sl = ((kt - 1) & 3) * 32768, k4 = kt + 3;
      dma1<false>(rx, xvo, k4 * 128, sl + w * 2048);
      dma1<false>(rx, xvo, k4 * 128 + 10240, sl + w * 2048 + 1024);
      dma1<false>(rw, wvo0, k4 * (64 * 3840), sl + 16384 + w * 2048);
      dma1<false>(rw, wvo1, k4 * (64 * 3840), sl + 16384 + w * 2048 + 1024);
    }
    if (kt == 7) asm volatile("buffer_load_dwordx4 %0, %1, %2, 0 offen sc1" : "=v"(st) : "v"(stvo), "s"(rws) : "memory");
    LAS char* base = sm + (kt & 3) * 32768;
#pragma unroll
    for (int s = 0; s < ((MK_EXP & 2) ? 0 : 2); ++s) {
      v4u xf[4];
#pragma unroll
      for (int m = 0; m < 4; ++m) xf[m] = *(LAS v4u*)(base + (s ? xa1 : xa0) + m * 2048);
      v2u a0 = trrd(base + wa + s * 8192), a1 = trrd(base + wa + s * 8192 + 2048);
      v2u b0 = trrd(base + wb + s * 8192), b1 = trrd(base + wb + s * 8192 + 2048);
      v4u fa = {a0.x, a0.y, a1.x, a1.y}, fb = {b0.x, b0.y, b1.x, b1.y};
#pragma unroll
      for (int m = 0; m < 4; ++m) {
        acc[0][m] = OA ? mfma16(fa, xf[m], acc[0][m]) : mfma16(xf[m], fa, acc[0][m]);
        acc[1][m] = OB ? mfma16(fb, xf[m], acc[1][m]) : mfma16(xf[m], fb, acc[1][m]);
      }
      if (!(MK_EXP & 4)) cs[0] = dot1(fa.w, dot1(fa.z, dot1(fa.y, dot1(fa.x, cs[0]))));
      if (!(MK_EXP & 4)) cs[1] = dot1(fb.w, dot1(fb.z, dot1(fb.y, dot1(fb.x, cs[1]))));
    }
    asm volatile("s_waitcnt lgkmcnt(0)" ::: "memory");
  }
}

DEV void pair_reduce(LAS char* scr, int w, int l, f32x4 (&c)[5]) {
  const int kh = w >> 2;
  LAS char* mine = scr + (w & 3) * 5120 + l * 16;
  *(LAS f32x4*)(mine + kh * 2048) = c[2];
  *(LAS f32x4*)(mine + kh * 2048 + 1024) = c[3];
  if (kh) *(LAS f32x4*)(mine + 4096) = c[4];
  BARRIER();
  c[0] += *(LAS f32x4*)(mine + (kh ^ 1) * 2048);
  c[1] += *(LAS f32x4*)(mine + (kh ^ 1) * 2048 + 1024);
  if (!kh) c[4] += *(LAS f32x4*)(mine + 4096);
}
template <bool STATS> DEV void store_tile(const Ctx& c, int buf, int stbuf, int mt, v2u a, v2u b, v2u e) {
  const int kh = c.w >> 2, R = c.l >> 4, t = c.l & 15;
  const int cc = 10 * c.g + 4 * kh + 2 * (R & 1) + (R >> 1);
  st16(c.ws, buf + (mt * 20 + (cc >> 2)) * 1024 + ((cc & 3) * 16 + t) * 16, swap_pair(a, b));
  if (!kh) {
    const int c4 = 10 * c.g + 8 + (R >> 1);
    st8(c.ws, buf + (mt * 20 + (c4 >> 2)) * 1024 + ((c4 & 3) * 16 + t) * 16 + (R & 1) * 8, e);
  }
  if (STATS) {
    f32x4 fa = unpk4(a), fb = unpk4(b), fe = unpk4(kh ? v2u{0, 0} : e);
    f32x4 s4 = fa + fb + fe, q4 = fa * fa + fb * fb + fe * fe;
    float s = quadsum((s4.x + s4.y) + (s4.z + s4.w)), q = quadsum((q4.x + q4.y) + (q4.z + q4.w));
    st8(c.ws, R == 0 ? stbuf + mt * 2048 + (2 * c.g + kh) * 128 + t * 8 : OOB, v2u{__float_as_uint(s), __float_as_uint(q)});
  }
}
DEV void row_stats(v2u s0, v2u s1, v2u s2, v2u s3, float& mean, float& var) {
  float s = (__uint_as_float(s0.x) + __uint_as_float(s1.x)) + (__uint_as_float(s2.x) + __uint_as_float(s3.x));
  float q = (__uint_as_float(s0.y) + __uint_as_float(s1.y)) + (__uint_as_float(s2.y) + __uint_as_float(s3.y));
  mean = quadsum(s) * (1.f / D);
  var = fmaxf(quadsum(q) * (1.f / D) - mean * mean, 0.f);
}
DEV f32x4 cs_read(LAS char* sm, int off, int stride) {
  return (*(LAS f32x4*)(sm + off) + *(LAS f32x4*)(sm + off + stride)) + (*(LAS f32x4*)(sm + off + 2 * stride) + *(LAS f32x4*)(sm + off + 3 * stride));
}

#define GEMM_SMALL(NAME, C, VA0, VA1, SA, VW, DM, DS)                                                                            \
  asm volatile(NAME : [c0] "=&v"(C[0]), [c1] "=&v"(C[1]), [c2] "=&v"(C[2]), [c3] "=&v"(C[3]), [c4] "=&v"(C[4]), [vw] "+v"(VW),   \
               [sa] "+s"(SA), [dm0] "+s"(DM), [ds0] "+s"(DS)                                                                     \
               : [va0] "v"(VA0), [va1] "v"(VA1), [rs] "s"(c.ws), [l16] "v"(l16) : PS_CLOB, "scc", "memory")

__global__ void __launch_bounds__(512, 2) mk_fwd(Params p) {
  extern __shared__ __attribute__((aligned(16))) char smem_[];
  LAS char* sm = (LAS char*)smem_;
  const int b = blockIdx.x, w = __builtin_amdgcn_readfirstlane(threadIdx.x >> 6), l = threadIdx.x & 63;
  const int xc = b & 7, j = b >> 3;
  STAMP(0);
  Ctx c;
  c.ws = mk_rsrc(p.ws, WS_END);
  c.wsp = (GAS unsigned char*)p.ws;
  c.b = b; c.w = w; c.l = l; c.g = j & 7; c.G = j & 15;
  const unsigned l16 = l * 16;
  const int R = l >> 4, t16 = l & 15;
  const rsrc_t rx = mk_rsrc(p.x, S * D * 2);

  {
    const int T = 8 * (xc & 1) + (j & 7), hp = 4 * (xc >> 1) + (j >> 3);
    const rsrc_t rw = mk_rsrc(p.wqkv, D * 3 * D * 2);
    v4u xr0, xr1;
    {
      const unsigned vo = l < 40 ? (8 * b + w) * 1280 + l * 32 : OOB;
      asm volatile("buffer_load_dwordx4 %0, %2, %3, 0 offen\n\tbuffer_load_dwordx4 %1, %2, %3, 0 offen offset:16" : "=&v"(xr0), "=&v"(xr1) : "v"(vo), "s"(rx) : "memory");
    }
    const unsigned xvo = (128 * T + 16 * w + (l >> 3)) * 1280 + (((l & 7) ^ ((l >> 3) & 7)) << 4);
    unsigned wvo[2];
#pragma unroll
    for (int i = 0; i < 2; ++i) {
      const int k = 32 * (w >> 2) + 16 * ((w >> 1) & 1) + 8 * i + 4 * (w & 1) + R;
      const int ch = t16 ^ ((4 * i + R) << 1);
      const int ten = ch / 5;
      wvo[i] = ch == 15 ? OOB : k * 3840 + ten * 1280 + 80 * hp + (ch - 5 * ten) * 16;
    }
#pragma unroll
    for (int kt = 0; kt < 4; ++kt) {
      dma1<false>(rx, xvo, kt * 128, kt * 32768 + w * 2048);
      dma1<false>(rx, xvo, kt * 128 + 10240, kt * 32768 + w * 2048 + 1024);
      dma1<false>(rw, wvo[0], kt * (64 * 3840), kt * 32768 + 16384 + w * 2048);
      dma1<false>(rw, wvo[1], kt * (64 * 3840), kt * 32768 + 16384 + w * 2048 + 1024);
    }
    const int fam = b >> 5, u0 = 5 * (b & 31);
#pragma unroll
    for (int i = 0; i < 4; ++i) {
      const int ins = w * 4 + i, u = u0 + ins / 6, pc = ins % 6;
      const bool is3 = u >= 108;
      const u16* src = u < 20 ? p.wo : u < 40 ? p.w1 : u < 60 ? p.w2 : u < 108 ? p.w4 : p.w3;
      const int ks = u < 60 ? u % 20 : u < 108 ? u - 60 : (u - 108) % 20;
      const int cidx = pc * 64 + l;
      const int row = is3 ? cidx / 12 : cidx / 10, cc = cidx - row * (is3 ? 12 : 10);
      const unsigned vo = (ins < 30 && u < 148 && row < 32) ? (is3 ? (32 * ks + row) * 3072 + (2 * fam + (u - 108) / 20) * 192 + cc * 16 : (32 * ks + row) * 1280 + fam * 160 + cc * 16) : OOB;
      dma1<false>(mk_rsrc(src, u < 60 ? D * D * 2 : D * F * 2), vo, 0, 131072 + ins * 1024);
    }
    const unsigned E = *(const CAS unsigned*)p.ws;
    const unsigned tlo = E * 0x9E3779B1u + 0x7F4A7C15u, thi = tlo * 0x85EBCA6Bu + 0xC2B2AE35u;
    c.tag = ((u64)thi << 32) | tlo;
    {
      asm volatile("s_waitcnt vmcnt(20)" : "+v"(xr0), "+v"(xr1)::"memory");
      float v[16];
#pragma unroll
      for (int i = 0; i < 4; ++i) { v[2 * i] = bflo(xr0[i]); v[2 * i + 1] = bfhi(xr0[i]); v[8 + 2 * i] = bflo(xr1[i]); v[9 + 2 * i] = bfhi(xr1[i]); }
      float s = 0;
#pragma unroll
      for (int i = 0; i < 16; ++i) s += v[i];
#pragma unroll
      for (int o = 1; o < 64; o <<= 1) s += __shfl_xor(s, o);
      const float mean = s * (1.f / D);
      float q = 0;
#pragma unroll
      for (int i = 0; i < 16; ++i) q += (v[i] - mean) * (v[i] - mean);
      q = l < 40 ? q : 0.f;
#pragma unroll
      for (int o = 1; o < 64; o <<= 1) q += __shfl_xor(q, o);
      const float rstd = __builtin_amdgcn_rsqf(q * (1.f / D) + EPS);
      const v4u gr = {__float_as_uint(mean), tlo, __float_as_uint(rstd), tlo};
      const unsigned vo = l == 0 ? XST_OFF + (8 * b + w) * 16 : OOB;
      asm volatile("buffer_store_dwordx4 %0, %1, %2, 0 offen sc1\n\ts_nop 1" ::"v"(gr), "v"(vo), "s"(c.ws) : "memory");
    }
    STAMP(1);
    const int wc = w & 3, th = w >> 2;
    const int row0 = 64 * th + t16;
    const unsigned xa0 = row0 * 128 + ((R ^ (row0 & 7)) << 4), xa1 = xa0 ^ 64;
    unsigned wad[2];
    {
      const int q4 = t16 >> 2, pp = l & 3, r7 = 4 * (R & 1) + q4;
#pragma unroll
      for (int i = 0; i < 2; ++i) {
        const int tile = 2 * wc + i;
        const int B = tile < 4 ? 40 * tile + 8 * pp : tile == 4 ? 32 + 40 * pp : 32 * tile + 8 * pp;
        wad[i] = 16384 + (16 * (R >> 1) + r7) * 256 + (((B >> 4) ^ (r7 << 1)) << 4) + (B & 8);
      }
    }
    f32x4 acc[2][4];
#pragma unroll
    for (int i = 0; i < 2; ++i)
#pragma unroll
      for (int m = 0; m < 4; ++m) acc[i][m] = f32x4{0, 0, 0, 0};
    float cs[2] = {0.f, 0.f};
    v4u st;
    const unsigned stvo = XST_OFF + (128 * T + 64 * th + l) * 16;
    if (wc < 2) p1_loop<true, true>(sm, rx, rw, c.ws, xvo, wvo[0], wvo[1], stvo, w, xa0, xa1, wad[0], wad[1], acc, cs, st);
    else if (wc == 2) p1_loop<true, false>(sm, rx, rw, c.ws, xvo, wvo[0], wvo[1], stvo, w, xa0, xa1, wad[0], wad[1], acc, cs, st);
    else p1_loop<false, false>(sm, rx, rw, c.ws, xvo, wvo[0], wvo[1], stvo, w, xa0, xa1, wad[0], wad[1], acc, cs, st);
    asm volatile("s_waitcnt vmcnt(0)" : "+v"(st)::"memory");
    STAMP(2);
    for (unsigned spins = 0; spins < SPIN_CAP && !__all(st.y == tlo && st.w == tlo); ++spins) {
      asm volatile("" ::: "memory");
      st = __builtin_amdgcn_raw_buffer_load_b128(c.ws, stvo, 0, 16);
    }
    LAS char* scr = sm + 65536 + w * 1024;
    {
      const float rs = __uint_as_float(st.z);
      *(LAS f32x2*)(scr + l * 8) = f32x2{rs, -__uint_as_float(st.x) * rs};
      cs[0] = quadsum(cs[0]); cs[1] = quadsum(cs[1]);
      *(LAS float*)(scr + 512 + (R & 1) * 64 + t16 * 4) = (R & 1) ? cs[1] : cs[0];
    }
    const float QS = 0.22360679774997896f * 1.4426950408889634f;
    const int tb = 4 * T + 2 * th;
    if (wc < 2) {
      const f32x4 ca = *(LAS f32x4*)(scr + 512 + R * 16), cb = *(LAS f32x4*)(scr + 576 + R * 16);
      const int h = 2 * hp + (R & 1);
#pragma unroll
      for (int m = 0; m < 4; ++m) {
        f32x2 ab = *(LAS f32x2*)(scr + (16 * m + t16) * 8);
        if (wc == 0) ab *= QS;
        const v4u o = swap_pair(pk4(acc[0][m] * ab.x + ca * ab.y), pk4(acc[1][m] * ab.x + cb * ab.y));
        const int rec = h * 64 + tb + (m >> 1);
        st16(c.ws, (wc == 0 ? Q16_OFF + rec * 1024 : KV_OFF + rec * 2816) + ((R >> 1) * 32 + 16 * (m & 1) + t16) * 16, o);
      }
    } else {
      if (wc == 2) {
        const f32x4 ca = *(LAS f32x4*)(scr + 512 + R * 16);
        const int h = 2 * hp + (R & 1);
#pragma unroll
        for (int m = 0; m < 4; ++m) {
          f32x2 ab = *(LAS f32x2*)(scr + (16 * m + t16) * 8);
          if (R < 2) ab *= QS;
          const int rec = h * 64 + tb + (m >> 1);
          st8(c.ws, (R < 2 ? Q8_OFF + rec * 256 : KV_OFF + rec * 2816 + 1024) + (16 * (m & 1) + t16) * 8, pk4(acc[0][m] * ab.x + ca * ab.y));
        }
      }
#pragma unroll
      for (int i = 0; i < 2; ++i) {
        if (i == 0 && wc == 2) continue;
        const int vc = 16 * (2 * wc + i - 5) + t16;
        const int hd = vc < 40 ? vc / 20 : vc - 40, d = vc < 40 ? vc - 20 * hd : 20;
#pragma unroll
        for (int u = 0; u < 2; ++u) {
          v2u pk[2];
#pragma unroll
          for (int mm = 0; mm < 2; ++mm) {
            const int m = 2 * u + mm;
            const f32x4 p01 = *(LAS f32x4*)(scr + (16 * m + 4 * R) * 8), p23 = *(LAS f32x4*)(scr + (16 * m + 4 * R) * 8 + 16);
            pk[mm] = pk4(acc[i][m] * f32x4{p01.x, p01.z, p23.x, p23.z} + f32x4{p01.y, p01.w, p23.y, p23.w} * cs[i]);
          }
          v4u o = swap_pair(pk[0], pk[1]);
          if (vc >= 40) o = v4u{0x3f803f80u, 0x3f803f80u, 0x3f803f80u, 0x3f803f80u};
          st16(c.ws, vc < 42 ? KV_OFF + ((2 * hp + hd) * 64 + tb + u) * 2816 + 1280 + ((2 * (R & 1) + (R >> 1)) * 24 + d) * 16 : OOB, o);
        }
      }
    }
    DRAIN_BARRIER();
    set_flag(c, FK_P1);
    STAMP(3);
#pragma unroll 1
    for (int f = w; f < 30; f += 8) {
      const int u = u0 + f / 6, nt = f % 6;
      const bool is3 = u >= 108;
      if (u >= 148 || (nt == 5 && !is3)) continue;
      const int rb = is3 ? 192 : 160;
      LAS char* a = sm + 131072 + (f / 6) * 6144 + (8 * R + (t16 >> 2)) * rb + nt * 32 + (l & 3) * 8;
      const v2u f0 = trrd(a), f1 = trrd(a + 4 * rb);
      int dst;
      if (is3) dst = W3_OFF + ((2 * fam + (u - 108) / 20) * 120 + ((u - 108) % 20) * 6 + nt) * 1024;
      else {
        const int half = u < 60 ? 10 : 24, ks = u < 60 ? u % 20 : u - 60, kh = ks >= half, ds = ks - kh * half;
        dst = WG_OFF + fam * WG_STR + ((u < 60 ? (u / 20) * 100 : 300) + ds * 10 + kh * 5 + ((kh && nt < 4) ? nt ^ 2 : nt)) * 1024;
      }
      st16(c.ws, dst + l16, v4u{f0.x, f0.y, f1.x, f1.y});
      if (is3 || (u >= 20 && u < 40)) {
        const float s = quadsum(dot1(f1.y, dot1(f1.x, dot1(f0.y, dot1(f0.x, 0.f)))));
        const int co = is3 ? CS3_OFF + (((u - 108) % 20) * F + (2 * fam + (u - 108) / 20) * 96 + nt * 16 + t16) * 4 : CS1_OFF + ((u - 20) * D + fam * 80 + nt * 16 + t16) * 4;
        __builtin_amdgcn_raw_buffer_store_b32(__float_as_uint(s), c.ws, R == 0 ? co : OOB, 0, 16);
      }
    }
  }
  {
    const int hp = 2 * xc + (j & 1), T = j >> 1;
    if (w == 0) poll_flags(c, FK_P1, (((hp >> 2) << 1) | (t16 >> 3)) + 8 * (((hp & 3) << 3) | (t16 & 7)));
    DRAIN_BARRIER();
    set_flag(c, FK_R);
    STAMP(4);
    if (MK_STOP <= 1) { FINISH(); return; }
    const int hh = w & 1, kq = w >> 1, h = 2 * hp + hh;
    const int r = l & 31, hi = l >> 5;
    const unsigned r8 = hi ? OOB : r * 8, vvo = r <= 20 ? (hi * 24 + r) * 16 : OOB;
    f32x16 o[4];
#pragma unroll
    for (int i = 0; i < 4; ++i) o[i] = f32x16{0, 0, 0, 0, 0, 0, 0, 0, 0, 0, 0, 0, 0, 0, 0, 0};
    {
      int so = KV_OFF + (h * 64 + kq * 16) * 2816, cnt = 8;
      const int sq = Q16_OFF + (h * 64 + T * 4) * 1024, sq8 = Q8_OFF + (h * 64 + T * 4) * 256;
      v4u q16[4], k16a, k16b, v0a, v0b, v1a, v1b;
      v2u q8[4], k8a, k8b;
      asm volatile(ATT_ASM
                   : [o0] "+v"(o[0]), [o1] "+v"(o[1]), [o2] "+v"(o[2]), [o3] "+v"(o[3]), [k16a] "=&v"(k16a), [k16b] "=&v"(k16b), [k8a] "=&v"(k8a),
                     [k8b] "=&v"(k8b), [v0a] "=&v"(v0a), [v0b] "=&v"(v0b), [v1a] "=&v"(v1a), [v1b] "=&v"(v1b), [so] "+s"(so), [cnt] "+s"(cnt),
                     [q160] "=&v"(q16[0]), [q161] "=&v"(q16[1]), [q162] "=&v"(q16[2]), [q163] "=&v"(q16[3]), [q80] "=&v"(q8[0]), [q81] "=&v"(q8[1]),
                     [q82] "=&v"(q8[2]), [q83] "=&v"(q8[3])
                   : [idx] "v"(0xDDDDDDDD), [l16] "v"(l16), [r8] "v"(r8), [vvo] "v"(vvo), [rs] "s"(c.ws), [sq] "s"(sq), [sq8] "s"(sq8)
                   : ATT_CLOB, "scc", "memory");
    }
    STAMP(5);
    poll_flags(c, FK_R, 32 * (l < 32 ? c.g : c.G >> 1) + (l & 31));
    dma_batch<0>(c, 0, 100);
    LAS char* cb = sm + 102400;
#pragma unroll
    for (int i = 0; i < 4; ++i) {
      LAS char* a = cb + ((hh * 4 + kq) * 4 + i) * 1536;
      *(LAS v4u*)(a + l16) = v4u{pk2(o[i][0], o[i][1]), pk2(o[i][2], o[i][3]), pk2(o[i][4], o[i][5]), pk2(o[i][6], o[i][7])};
      *(LAS v2u*)(a + 1024 + l * 8) = hi ? v2u{__float_as_uint(o[i][8]), 0u} : v2u{pk2(o[i][8], o[i][9]), pk2(o[i][10], o[i][11])};
    }
    BARRIER();
    {
      const int tok = 16 * w + t16, qt = tok >> 5, rr = tok & 31;
      float inv[2];
#pragma unroll
      for (int hd = 0; hd < 2; ++hd) {
        float ls = 0;
#pragma unroll
        for (int k = 0; k < 4; ++k) ls += *(LAS float*)(cb + ((hd * 4 + k) * 4 + qt) * 1536 + 1024 + (32 + rr) * 8);
        inv[hd] = __builtin_amdgcn_rcpf(ls);
      }
#pragma unroll
      for (int rnd = 0; rnd < 2; ++rnd) {
        const int ck = rnd ? 4 : R;
        v2u pk[2];
#pragma unroll
        for (int e = 0; e < 2; ++e) {
          const int gi = 2 * ck + e, hd = gi >= 5, pc = gi - 5 * hd;
          const int off = (hd * 16 + qt) * 1536 + (pc < 4 ? ((pc & 1) * 32 + rr) * 16 + (pc >> 1) * 8 : 1024 + rr * 8);
          f32x4 a = {0, 0, 0, 0};
#pragma unroll
          for (int k = 0; k < 4; ++k) a += unpk4(*(LAS v2u*)(cb + off + k * 6144));
          pk[e] = pk4(a * (hd ? inv[1] : inv[0]));
        }
        const int gc = hp * 5 + ck;
        st16(c.ws, (rnd == 0 || R == 0) ? O_OFF + ((8 * T + w) * 20 + (gc >> 2)) * 1024 + ((gc & 3) * 16 + t16) * 16 : OOB, v4u{pk[0].x, pk[0].y, pk[1].x, pk[1].y});
      }
    }
    DRAIN_BARRIER();
    set_flag(c, FK_P2);
    STAMP(6);
  }
  if (MK_STOP <= 2) { FINISH(); return; }
  const int rp = 4 * xc + (j >> 3), kh = w >> 2, mt = 4 * rp + (w & 3);
  const int blk0 = xc + 64 * (j >> 3);
  v2u xres[3];
  {
    const int i = threadIdx.x;
    float s1 = 0.f, s3 = 0.f;
    const unsigned o1 = i < 320 ? CS1_OFF + ((i / 80) * 5 * D + c.g * 80 + i % 80) * 4 : OOB;
    const unsigned o3 = i < 384 ? CS3_OFF + ((i / 96) * 5 * F + c.G * 96 + i % 96) * 4 : OOB;
#pragma unroll
    for (int k = 0; k < 5; ++k) {
      s1 += __uint_as_float(__builtin_amdgcn_raw_buffer_load_b32(c.ws, o1, k * D * 4, 16));
      s3 += __uint_as_float(__builtin_amdgcn_raw_buffer_load_b32(c.ws, o3, k * F * 4, 16));
    }
    const unsigned xo = (16 * mt + t16) * 1280 + c.g * 160 + R * 8;
    xres[0] = __builtin_amdgcn_raw_buffer_load_b64(rx, xo + kh * 64, 0, 0);
    xres[1] = __builtin_amdgcn_raw_buffer_load_b64(rx, xo + kh * 64 + 32, 0, 0);
    xres[2] = __builtin_amdgcn_raw_buffer_load_b64(rx, xo + 128, 0, 0);
    if (i < 320) *(LAS float*)(sm + L_CS1 + i * 4) = s1;
    if (i < 384) *(LAS float*)(sm + L_CS3 + i * 4) = s3;
    asm volatile("" : "+v"(xres[0]), "+v"(xres[1]), "+v"(xres[2]));
  }
  dma_batch<1>(c, 100, 150);
  if (w == 0) poll_flags(c, FK_P2, (t16 >> 1) + 8 * ((t16 & 1) + 2 * (rp >> 1)));
  BARRIER();
  STAMP(7);
  const unsigned vw0 = l16 + kh * 5120;
  v2u hq[3];
  {
    f32x4 a[5];
    unsigned va = O_OFF + (mt * 20 + kh * 10) * 1024 + l16, sa = 0, vw = vw0, dm = (0 + 2 * w) * 1024, ds = stream_src(c, 150 + 2 * w);
    GEMM_SMALL(P3_ASM, a, va, va, sa, vw, dm, ds);
    STAMP(8);
    pair_reduce(sm + 20 * 1024, w, l, a);
    dma_batch<0>(c, 190, 238);
    hq[0] = pk4(a[0] + unpk4(xres[0])); hq[1] = pk4(a[1] + unpk4(xres[1])); hq[2] = pk4(a[4] + unpk4(xres[2]));
    store_tile<true>(c, H_OFF, HST_OFF, mt, hq[0], hq[1], hq[2]);
    DRAIN_BARRIER();
    set_flag(c, FK_P3);
    dma_batch<1>(c, 166, 190);
    dma_batch<1>(c, 238, 250);
    if (w == 0) poll_flags(c, FK_P3, blk0 + 8 * (l & 7));
    BARRIER();
    STAMP(9);
  }
  if (MK_STOP <= 3) { FINISH(); return; }
  f32x4 keep[3];
  {
    f32x4 a[5];
    v2u s0, s1, s2, s3;
    unsigned va = H_OFF + (mt * 20 + kh * 10) * 1024 + l16, sa = 0, vw = vw0, dm = (100 + 2 * w) * 1024, ds = stream_src(c, 250 + 2 * w);
    const unsigned vst = HST_OFF + mt * 2048 + R * 512 + t16 * 8;
    asm volatile(P4_ASM
                 : [c0] "=&v"(a[0]), [c1] "=&v"(a[1]), [c2] "=&v"(a[2]), [c3] "=&v"(a[3]), [c4] "=&v"(a[4]), [vw] "+v"(vw), [sa] "+s"(sa), [dm0] "+s"(dm),
                   [ds0] "+s"(ds), [st0] "=&v"(s0), [st1] "=&v"(s1), [st2] "=&v"(s2), [st3] "=&v"(s3)
                 : [va0] "v"(va), [rs] "s"(c.ws), [l16] "v"(l16), [vst] "v"(vst)
                 : PS_CLOB, "scc", "memory");
    STAMP(10);
    pair_reduce(sm + 120 * 1024, w, l, a);
    STAMP(19);
    dma_batch<0>(c, 290, 338);
    STAMP(20);
    float mean, var;
    row_stats(s0, s1, s2, s3, mean, var);
    const float r1 = __builtin_amdgcn_rsqf(var + EPS);
    const float sc = r1 * __builtin_amdgcn_rsqf(var * r1 * r1 + EPS), sb = -mean * sc;
    const v2u g0 = pk4(gelu4(a[0] * sc + cs_read(sm, L_CS1 + (32 * kh + 4 * R) * 4, 320) * sb));
    const v2u g1 = pk4(gelu4(a[1] * sc + cs_read(sm, L_CS1 + (32 * kh + 16 + 4 * R) * 4, 320) * sb));
    v2u g4 = {0, 0};
    if (!kh) g4 = pk4(gelu4(a[4] * sc + cs_read(sm, L_CS1 + (64 + 4 * R) * 4, 320) * sb));
    store_tile<false>(c, G_OFF, 0, mt, g0, g1, g4);
#pragma unroll
    for (int i = 0; i < 3; ++i) keep[i] = (unpk4(hq[i]) - mean) * r1;
    STAMP(21);
    DRAIN_BARRIER();
    STAMP(22);
    set_flag(c, FK_P4);
    dma_batch<1>(c, 266, 290);
    dma_batch<1>(c, 338, 350);
    STAMP(23);
    if (w == 0) {
      const int rn = min(max(rp + ((l >> 3) & 3) - 1, 0), 31), rq = min(rn, rp + 1);
      poll_flags(c, FK_P4, (rq >> 2) + 64 * (rq & 3) + 8 * (l & 7));
    }
    BARRIER();
    STAMP(11);
  }
  if (MK_STOP <= 4) { FINISH(); return; }
  {
    f32x4 a[5];
    unsigned va[2];
#pragma unroll
    for (int i = 0; i < 2; ++i) {
      const int tk = 16 * mt + t16 + (i ? kh + 1 : -(kh + 1));
      va[i] = (tk >= 0 && tk < S) ? G_OFF + ((tk >> 4) * 20 + kh * 10) * 1024 + (R * 16 + (tk & 15)) * 16 : OOB;
    }
    unsigned sa = 0, vw = vw0, dm = (50 + 2 * w) * 1024, ds = stream_src(c, 350 + 2 * w);
    GEMM_SMALL(P5_ASM, a, va[0], va[1], sa, vw, dm, ds);
    STAMP(12);
    pair_reduce(sm + 70 * 1024, w, l, a);
    dma_batch<0>(c, 390, 438);
    const v2u q0 = pk4(a[0] + keep[0]), q1 = pk4(a[1] + keep[1]), q4 = pk4(a[4] + keep[2]);
    store_tile<true>(c, X1_OFF, X1ST_OFF, mt, q0, q1, q4);
    keep[0] = unpk4(q0) + unpk4(hq[0]); keep[1] = unpk4(q1) + unpk4(hq[1]); keep[2] = unpk4(q4) + unpk4(hq[2]);
    DRAIN_BARRIER();
    set_flag(c, FK_P5);
    dma_batch<1>(c, 366, 390);
    dma_batch<1>(c, 438, 450);
    if (w == 0) poll_flags(c, FK_P5, xc + 128 * (j >> 4) + 8 * t16);
    BARRIER();
    STAMP(13);
  }
  if (MK_STOP <= 5) { FINISH(); return; }
  {
    const int m6 = 8 * (2 * xc + (j >> 4)) + w;
    f32x4 a[6];
    v2u s0, s1, s2, s3;
    unsigned va = X1_OFF + m6 * 20 * 1024 + l16, sa = 0, vw = l16, dm = (0 + 6 * w) * 1024, ds = stream_src(c, 450 + 6 * w);
    const unsigned vst = X1ST_OFF + m6 * 2048 + R * 512 + t16 * 8;
    asm volatile(P6_ASM
                 : [c0] "=&v"(a[0]), [c1] "=&v"(a[1]), [c2] "=&v"(a[2]), [c3] "=&v"(a[3]), [c4] "=&v"(a[4]), [c5] "=&v"(a[5]), [vw] "+v"(vw), [sa] "+s"(sa),
                   [dm0] "+s"(dm), [ds0] "+s"(ds), [st0] "=&v"(s0), [st1] "=&v"(s1), [st2] "=&v"(s2), [st3] "=&v"(s3)
                 : [va0] "v"(va), [rs] "s"(c.ws), [l16] "v"(l16), [vst] "v"(vst)
                 : P6_CLOB, "scc", "memory");
    STAMP(14);
    float mean, var;
    row_stats(s0, s1, s2, s3, mean, var);
    const float sc = __builtin_amdgcn_rsqf(var + EPS), sb = -mean * sc;
#pragma unroll
    for (int i = 0; i < 3; ++i) {
      const v2u u0 = pk4(gelu4(a[2 * i] * sc + cs_read(sm, L_CS3 + (32 * i + 4 * R) * 4, 384) * sb));
      const v2u u1 = pk4(gelu4(a[2 * i + 1] * sc + cs_read(sm, L_CS3 + (32 * i + 16 + 4 * R) * 4, 384) * sb));
      st16(c.ws, U_OFF + (m6 * 48 + 3 * c.G + i) * 1024 + ((2 * (R & 1) + (R >> 1)) * 16 + t16) * 16, swap_pair(u0, u1));
    }
    DRAIN_BARRIER();
    set_flag(c, FK_P6);
    dma_batch<1>(c, 498, 570);
    if (w == 0) poll_flags(c, FK_P6, xc + 128 * (j >> 4) + 8 * t16);
    BARRIER();
    STAMP(15);
  }
  if (MK_STOP <= 6) { FINISH(); return; }
  {
    if (b == 0 && threadIdx.x == 0) __hip_atomic_store((GAS unsigned*)c.wsp, (unsigned)c.tag, __ATOMIC_RELAXED, __HIP_MEMORY_SCOPE_AGENT);
    f32x4 a[5];
    unsigned va = U_OFF + (mt * 48 + kh * 24) * 1024 + l16, sa = 0, vw = vw0;
    unsigned dm0 = ((120 + 5 * w) % RING) * 1024, ds0 = stream_src(c, 570 + 5 * w), dm1 = (10 + 7 * w) * 1024, ds1 = stream_src(c, 610 + 7 * w);
    asm volatile(P7_ASM
                 : [c0] "=&v"(a[0]), [c1] "=&v"(a[1]), [c2] "=&v"(a[2]), [c3] "=&v"(a[3]), [c4] "=&v"(a[4]), [vw] "+v"(vw), [sa] "+s"(sa), [dm0] "+s"(dm0),
                   [ds0] "+s"(ds0), [dm1] "+s"(dm1), [ds1] "+s"(ds1)
                 : [va0] "v"(va), [rs] "s"(c.ws), [l16] "v"(l16)
                 : P7_CLOB, "scc", "memory");
    STAMP(16);
    pair_reduce(sm + 70 * 1024, w, l, a);
    const rsrc_t ro = mk_rsrc(p.out, S * D * 2);
    const unsigned oo = (16 * mt + t16) * 1280 + c.g * 160;
    __builtin_amdgcn_raw_buffer_store_b128(swap_pair(pk4(a[0] + keep[0]), pk4(a[1] + keep[1])), ro, oo + kh * 64 + (R & 1) * 32 + (R >> 1) * 16, 0, 0);
    if (!kh) __builtin_amdgcn_raw_buffer_store_b64(pk4(a[4] + keep[2]), ro, oo + 128 + R * 8, 0, 0);
    STAMP(17);
  }
  FINISH();
}

extern "C" void kernel_launch(void* const* d_in, const int* in_sizes, int n_in, void* d_out, int out_size, void* d_ws, size_t ws_size, hipStream_t stream) {
  Params p;
  p.x = (const u16*)d_in[0];
  p.wqkv = (const u16*)d_in[1];
  p.wo = (const u16*)d_in[2];
  p.w1 = (const u16*)d_in[4];
  p.w2 = (const u16*)d_in[5];
  p.w3 = (const u16*)d_in[6];
  p.w4 = (const u16*)d_in[7];
  p.out = (u16*)d_out;
  p.ws = (unsigned char*)d_ws;
  (void)hipFuncSetAttribute((const void*)mk_fwd, hipFuncAttributeMaxDynamicSharedMemorySize, LDS_BYTES);
  mk_fwd<<<256, 512, LDS_BYTES, stream>>>(p);
}
```

```cpp
#include <hip/hip_runtime.h>

typedef unsigned short u16;
typedef unsigned long long u64;
typedef unsigned v4u __attribute__((ext_vector_type(4)));
typedef unsigned v2u __attribute__((ext_vector_type(2)));
typedef float f32x16 __attribute__((ext_vector_type(16)));
typedef float f32x4 __attribute__((ext_vector_type(4)));
typedef float f32x2 __attribute__((ext_vector_type(2)));
typedef __bf16 bf16x8 __attribute__((ext_vector_type(8)));
typedef __bf16 bf16x2 __attribute__((ext_vector_type(2)));
typedef short s16x4 __attribute__((ext_vector_type(4)));
#define LAS __attribute__((address_space(3)))
#define GAS __attribute__((address_space(1)))
#define CAS __attribute__((address_space(4)))
#define DEV __device__ __forceinline__
typedef __amdgpu_buffer_rsrc_t rsrc_t;

#define MK_STOP 99
#define MK_EXP 0
#define MK_TLB 1
#define MK_OUT_AUX 0
#define MK_L2WARM 0
#define MK_THROTTLE 0
#define MK_STAMP 0

#define P1_ASM_T \
  "s_mov_b32 s90, 0\n" \
  "s_lshl_b32 s91, %[k2], 7\n" \
  "s_mul_i32 s92, %[k2], 245760\n" \
  "s_mov_b32 s93, 0\n" \
  "s_add_u32 s95, %[w2k], 0x10000\n" \
  "v_mov_b32 v64, 0\n" \
  "v_mov_b32 v65, 0\n" \
  "v_mov_b32 v66, 0\n" \
  "v_mov_b32 v67, 0\n" \
  "v_add_u32 v112, s93, %[xa0]\n" \
  "v_add_u32 v113, s93, %[wa]\n" \
  "v_add_u32 v114, s93, %[wb]\n" \
  "v_mfma_f32_16x16x32_bf16 %[c00], v[64:67], v[64:67], 0\n" \
  "v_mfma_f32_16x16x32_bf16 %[c10], v[64:67], v[64:67], 0\n" \
  "v_mfma_f32_16x16x32_bf16 %[c01], v[64:67], v[64:67], 0\n" \
  "v_mfma_f32_16x16x32_bf16 %[c11], v[64:67], v[64:67], 0\n" \
  "v_mfma_f32_16x16x32_bf16 %[c02], v[64:67], v[64:67], 0\n" \
  "v_mfma_f32_16x16x32_bf16 %[c12], v[64:67], v[64:67], 0\n" \
  "v_mfma_f32_16x16x32_bf16 %[c03], v[64:67], v[64:67], 0\n" \
  "v_mfma_f32_16x16x32_bf16 %[c13], v[64:67], v[64:67], 0\n" \
  "s_waitcnt vmcnt(5)\n" \
  "s_barrier\n" \
  "ds_read_b64_tr_b16 v[96:97], v113 offset:0\n" \
  "ds_read_b64_tr_b16 v[98:99], v113 offset:2048\n" \
  "ds_read_b128 v[64:67], v112\n" \
  "ds_read_b64_tr_b16 v[100:101], v114 offset:0\n" \
  "ds_read_b64_tr_b16 v[102:103], v114 offset:2048\n" \
  "ds_read_b128 v[68:71], v112 offset:2048\n" \
  "ds_read_b128 v[72:75], v112 offset:4096\n" \
  "ds_read_b128 v[76:79], v112 offset:6144\n" \
  "LP1_loop%=:\n" \
  "v_add_u32 v112, s93, %[xa1]\n" \
  "v_add_u32 v113, s93, %[wa]\n" \
  "v_add_u32 v114, s93, %[wb]\n" \
  "s_add_u32 s94, s93, 0x8000\n" \
  "s_and_b32 s94, s94, 0x1ffff\n" \
  "s_cmp_gt_u32 s90, 1\n" \
  "s_waitcnt lgkmcnt(0)\n" \
  "v_mfma_f32_16x16x32_bf16 %[c00], v[96:99], v[64:67], %[c00]\n" \
  "ds_read_b64_tr_b16 v[104:105], v113 offset:8192\n" \
  "ds_read_b64_tr_b16 v[106:107], v113 offset:10240\n" \
  "v_mfma_f32_16x16x32_bf16 %[c10], v[100:103], v[64:67], %[c10]\n" \
  "ds_read_b128 v[80:83], v112\n" \
  "ds_read_b64_tr_b16 v[108:109], v114 offset:8192\n" \
  "v_mfma_f32_16x16x32_bf16 %[c01], v[96:99], v[68:71], %[c01]\n" \
  "ds_read_b64_tr_b16 v[110:111], v114 offset:10240\n" \
  "ds_read_b128 v[84:87], v112 offset:2048\n" \
  "v_mfma_f32_16x16x32_bf16 %[c11], v[100:103], v[68:71], %[c11]\n" \
  "ds_read_b128 v[88:91], v112 offset:4096\n" \
  "ds_read_b128 v[92:95], v112 offset:6144\n" \
  "v_mfma_f32_16x16x32_bf16 %[c02], v[96:99], v[72:75], %[c02]\n" \
  "s_cbranch_scc1 LP1_pa%=\n" \
  "s_mov_b32 m0, s95\n" \
  "s_add_u32 s96, s91, 9216\n" \
  "buffer_load_dwordx4 %[xvo], %[rx], s91 offen lds\n" \
  "buffer_load_dwordx4 %[xvo], %[rx], s96 offen offset:1024 lds\n" \
  "LP1_pa%=:\n" \
  "v_mfma_f32_16x16x32_bf16 %[c12], v[100:103], v[72:75], %[c12]\n" \
  "s_cbranch_scc1 LP1_pb%=\n" \
  "s_add_u32 m0, s95, 0x4000\n" \
  "s_add_u32 s91, s91, 128\n" \
  "buffer_load_dwordx4 %[wvo0], %[rw], s92 offen lds\n" \
  "buffer_load_dwordx4 %[wvo1], %[rw], s92 offen offset:1024 lds\n" \
  "s_add_u32 s92, s92, 245760\n" \
  "s_add_u32 s95, s95, 0x8000\n" \
  "s_and_b32 s95, s95, 0x1ffff\n" \
  "s_cmpk_eq_u32 s91, 1280\n" \
  "s_cselect_b32 s91, 0, s91\n" \
  "s_cselect_b32 s92, 0, s92\n" \
  "LP1_pb%=:\n" \
  "v_mfma_f32_16x16x32_bf16 %[c03], v[96:99], v[76:79], %[c03]\n" \
  "v_add_u32 v112, s94, %[xa0]\n" \
  "v_add_u32 v113, s94, %[wa]\n" \
  "v_add_u32 v114, s94, %[wb]\n" \
  "v_mfma_f32_16x16x32_bf16 %[c13], v[100:103], v[76:79], %[c13]\n" \
  "s_cmp_eq_u32 s90, 7\n" \
  "s_cbranch_scc1 LP1_w3%=\n" \
  "s_cmp_gt_u32 s90, 7\n" \
  "s_cbranch_scc1 LP1_w1%=\n" \
  "s_cmp_lg_u32 s90, 0\n" \
  "s_cbranch_scc1 LP1_w2%=\n" \
  "s_waitcnt vmcnt(4)\n" \
  "LP1_w1%=:\n" \
  "s_waitcnt vmcnt(6)\n" \
  "LP1_w2%=:\n" \
  "s_waitcnt vmcnt(8)\n" \
  "LP1_w3%=:\n" \
  "s_cmp_gt_u32 s90, 5\n" \
  "s_waitcnt vmcnt(10) lgkmcnt(0)\n" \
  "s_barrier\n" \
  "v_mfma_f32_16x16x32_bf16 %[c00], v[104:107], v[80:83], %[c00]\n" \
  "ds_read_b64_tr_b16 v[96:97], v113 offset:0\n" \
  "ds_read_b64_tr_b16 v[98:99], v113 offset:2048\n" \
  "v_mfma_f32_16x16x32_bf16 %[c10], v[108:111], v[80:83], %[c10]\n" \
  "ds_read_b128 v[64:67], v112\n" \
  "ds_read_b64_tr_b16 v[100:101], v114 offset:0\n" \
  "v_mfma_f32_16x16x32_bf16 %[c01], v[104:107], v[84:87], %[c01]\n" \
  "ds_read_b64_tr_b16 v[102:103], v114 offset:2048\n" \
  "ds_read_b128 v[68:71], v112 offset:2048\n" \
  "v_mfma_f32_16x16x32_bf16 %[c11], v[108:111], v[84:87], %[c11]\n" \
  "ds_read_b128 v[72:75], v112 offset:4096\n" \
  "ds_read_b128 v[76:79], v112 offset:6144\n" \
  "v_mfma_f32_16x16x32_bf16 %[c02], v[104:107], v[88:91], %[c02]\n" \
  "s_cbranch_scc1 LP1_qa%=\n" \
  "s_mov_b32 m0, s95\n" \
  "s_add_u32 s96, s91, 9216\n" \
  "buffer_load_dwordx4 %[xvo], %[rx], s91 offen lds\n" \
  "buffer_load_dwordx4 %[xvo], %[rx], s96 offen offset:1024 lds\n" \
  "LP1_qa%=:\n" \
  "v_mfma_f32_16x16x32_bf16 %[c12], v[108:111], v[88:91], %[c12]\n" \
  "s_cbranch_scc1 LP1_qb%=\n" \
  "s_add_u32 m0, s95, 0x4000\n" \
  "s_add_u32 s91, s91, 128\n" \
  "buffer_load_dwordx4 %[wvo0], %[rw], s92 offen lds\n" \
  "buffer_load_dwordx4 %[wvo1], %[rw], s92 offen offset:1024 lds\n" \
  "s_add_u32 s92, s92, 245760\n" \
  "s_add_u32 s95, s95, 0x8000\n" \
  "s_and_b32 s95, s95, 0x1ffff\n" \
  "s_cmpk_eq_u32 s91, 1280\n" \
  "s_cselect_b32 s91, 0, s91\n" \
  "s_cselect_b32 s92, 0, s92\n" \
  "s_branch LP1_qc%=\n" \
  "LP1_qb%=:\n" \
  "s_cmp_lg_u32 s90, 6\n" \
  "s_cbranch_scc1 LP1_qc%=\n" \
  "buffer_load_dwordx4 %[st], %[stvo], %[rws], 0 offen sc1\n" \
  "buffer_load_dwordx2 %[cs], %[csvo], %[rws], 0 offen sc1\n" \
  "s_lshl_b32 s96, %[w2k], 1\n" \
  "s_add_u32 m0, s96, 0x20000\n" \
  "s_mov_b32 s96, 0\n" \
  "buffer_load_dwordx4 %[rvo], %[rr], s96 offen offset:0 lds\n" \
  "s_add_u32 s96, s96, %[rst]\n" \
  "buffer_load_dwordx4 %[rvo], %[rr], s96 offen offset:1024 lds\n" \
  "s_add_u32 s96, s96, %[rst]\n" \
  "buffer_load_dwordx4 %[rvo], %[rr], s96 offen offset:2048 lds\n" \
  "s_add_u32 s96, s96, %[rst]\n" \
  "buffer_load_dwordx4 %[rvo], %[rr], s96 offen offset:3072 lds\n" \
  "s_add_u32 s96, s96, %[rst]\n" \
  "LP1_qc%=:\n" \
  "v_mfma_f32_16x16x32_bf16 %[c03], v[104:107], v[92:95], %[c03]\n" \
  "s_mov_b32 s93, s94\n" \
  "s_add_u32 s90, s90, 1\n" \
  "s_cmp_lg_u32 s90, 10\n" \
  "v_mfma_f32_16x16x32_bf16 %[c13], v[108:111], v[92:95], %[c13]\n" \
  "s_cbranch_scc1 LP1_loop%=\n" \
  "s_waitcnt vmcnt(4) lgkmcnt(0)\n" \
  "s_nop 5\n" \
  ""
#define P1_ASM_N \
  "s_mov_b32 s90, 0\n" \
  "s_lshl_b32 s91, %[k2], 7\n" \
  "s_mul_i32 s92, %[k2], 245760\n" \
  "s_mov_b32 s93, 0\n" \
  "s_add_u32 s95, %[w2k], 0x10000\n" \
  "v_mov_b32 v64, 0\n" \
  "v_mov_b32 v65, 0\n" \
  "v_mov_b32 v66, 0\n" \
  "v_mov_b32 v67, 0\n" \
  "v_add_u32 v112, s93, %[xa0]\n" \
  "v_add_u32 v113, s93, %[wa]\n" \
  "v_add_u32 v114, s93, %[wb]\n" \
  "v_mfma_f32_16x16x32_bf16 %[c00], v[64:67], v[64:67], 0\n" \
  "v_mfma_f32_16x16x32_bf16 %[c10], v[64:67], v[64:67], 0\n" \
  "v_mfma_f32_16x16x32_bf16 %[c01], v[64:67], v[64:67], 0\n" \
  "v_mfma_f32_16x16x32_bf16 %[c11], v[64:67], v[64:67], 0\n" \
  "v_mfma_f32_16x16x32_bf16 %[c02], v[64:67], v[64:67], 0\n" \
  "v_mfma_f32_16x16x32_bf16 %[c12], v[64:67], v[64:67], 0\n" \
  "v_mfma_f32_16x16x32_bf16 %[c03], v[64:67], v[64:67], 0\n" \
  "v_mfma_f32_16x16x32_bf16 %[c13], v[64:67], v[64:67], 0\n" \
  "s_waitcnt vmcnt(5)\n" \
  "s_barrier\n" \
  "ds_read_b64_tr_b16 v[96:97], v113 offset:0\n" \
  "ds_read_b64_tr_b16 v[98:99], v113 offset:2048\n" \
  "ds_read_b128 v[64:67], v112\n" \
  "ds_read_b64_tr_b16 v[100:101], v114 offset:0\n" \
  "ds_read_b64_tr_b16 v[102:103], v114 offset:2048\n" \
  "ds_read_b128 v[68:71], v112 offset:2048\n" \
  "ds_read_b128 v[72:75], v112 offset:4096\n" \
  "ds_read_b128 v[76:79], v112 offset:6144\n" \
  "LP1_loop%=:\n" \
  "v_add_u32 v112, s93, %[xa1]\n" \
  "v_add_u32 v113, s93, %[wa]\n" \
  "v_add_u32 v114, s93, %[wb]\n" \
  "s_add_u32 s94, s93, 0x8000\n" \
  "s_and_b32 s94, s94, 0x1ffff\n" \
  "s_cmp_gt_u32 s90, 1\n" \
  "s_waitcnt lgkmcnt(0)\n" \
  "v_mfma_f32_16x16x32_bf16 %[c00], v[64:67], v[96:99], %[c00]\n" \
  "ds_read_b64_tr_b16 v[104:105], v113 offset:8192\n" \
  "ds_read_b64_tr_b16 v[106:107], v113 offset:10240\n" \
  "v_mfma_f32_16x16x32_bf16 %[c10], v[64:67], v[100:103], %[c10]\n" \
  "ds_read_b128 v[80:83], v112\n" \
  "ds_read_b64_tr_b16 v[108:109], v114 offset:8192\n" \
  "v_mfma_f32_16x16x32_bf16 %[c01], v[68:71], v[96:99], %[c01]\n" \
  "ds_read_b64_tr_b16 v[110:111], v114 offset:10240\n" \
  "ds_read_b128 v[84:87], v112 offset:2048\n" \
  "v_mfma_f32_16x16x32_bf16 %[c11], v[68:71], v[100:103], %[c11]\n" \
  "ds_read_b128 v[88:91], v112 offset:4096\n" \
  "ds_read_b128 v[92:95], v112 offset:6144\n" \
  "v_mfma_f32_16x16x32_bf16 %[c02], v[72:75], v[96:99], %[c02]\n" \
  "s_cbranch_scc1 LP1_pa%=\n" \
  "s_mov_b32 m0, s95\n" \
  "s_add_u32 s96, s91, 9216\n" \
  "buffer_load_dwordx4 %[xvo], %[rx], s91 offen lds\n" \
  "buffer_load_dwordx4 %[xvo], %[rx], s96 offen offset:1024 lds\n" \
  "LP1_pa%=:\n" \
  "v_mfma_f32_16x16x32_bf16 %[c12], v[72:75], v[100:103], %[c12]\n" \
  "s_cbranch_scc1 LP1_pb%=\n" \
  "s_add_u32 m0, s95, 0x4000\n" \
  "s_add_u32 s91, s91, 128\n" \
  "buffer_load_dwordx4 %[wvo0], %[rw], s92 offen lds\n" \
  "buffer_load_dwordx4 %[wvo1], %[rw], s92 offen offset:1024 lds\n" \
  "s_add_u32 s92, s92, 245760\n" \
  "s_add_u32 s95, s95, 0x8000\n" \
  "s_and_b32 s95, s95, 0x1ffff\n" \
  "s_cmpk_eq_u32 s91, 1280\n" \
  "s_cselect_b32 s91, 0, s91\n" \
  "s_cselect_b32 s92, 0, s92\n" \
  "LP1_pb%=:\n" \
  "v_mfma_f32_16x16x32_bf16 %[c03], v[76:79], v[96:99], %[c03]\n" \
  "v_add_u32 v112, s94, %[xa0]\n" \
  "v_add_u32 v113, s94, %[wa]\n" \
  "v_add_u32 v114, s94, %[wb]\n" \
  "v_mfma_f32_16x16x32_bf16 %[c13], v[76:79], v[100:103], %[c13]\n" \
  "s_cmp_eq_u32 s90, 7\n" \
  "s_cbranch_scc1 LP1_w3%=\n" \
  "s_cmp_gt_u32 s90, 7\n" \
  "s_cbranch_scc1 LP1_w1%=\n" \
  "s_cmp_lg_u32 s90, 0\n" \
  "s_cbranch_scc1 LP1_w2%=\n" \
  "s_waitcnt vmcnt(4)\n" \
  "LP1_w1%=:\n" \
  "s_waitcnt vmcnt(6)\n" \
  "LP1_w2%=:\n" \
  "s_waitcnt vmcnt(8)\n" \
  "LP1_w3%=:\n" \
  "s_cmp_gt_u32 s90, 5\n" \
  "s_waitcnt vmcnt(10) lgkmcnt(0)\n" \
  "s_barrier\n" \
  "v_mfma_f32_16x16x32_bf16 %[c00], v[80:83], v[104:107], %[c00]\n" \
  "ds_read_b64_tr_b16 v[96:97], v113 offset:0\n" \
  "ds_read_b64_tr_b16 v[98:99], v113 offset:2048\n" \
  "v_mfma_f32_16x16x32_bf16 %[c10], v[80:83], v[108:111], %[c10]\n" \
  "ds_read_b128 v[64:67], v112\n" \
  "ds_read_b64_tr_b16 v[100:101], v114 offset:0\n" \
  "v_mfma_f32_16x16x32_bf16 %[c01], v[84:87], v[104:107], %[c01]\n" \
  "ds_read_b64_tr_b16 v[102:103], v114 offset:2048\n" \
  "ds_read_b128 v[68:71], v112 offset:2048\n" \
  "v_mfma_f32_16x16x32_bf16 %[c11], v[84:87], v[108:111], %[c11]\n" \
  "ds_read_b128 v[72:75], v112 offset:4096\n" \
  "ds_read_b128 v[76:79], v112 offset:6144\n" \
  "v_mfma_f32_16x16x32_bf16 %[c02], v[88:91], v[104:107], %[c02]\n" \
  "s_cbranch_scc1 LP1_qa%=\n" \
  "s_mov_b32 m0, s95\n" \
  "s_add_u32 s96, s91, 9216\n" \
  "buffer_load_dwordx4 %[xvo], %[rx], s91 offen lds\n" \
  "buffer_load_dwordx4 %[xvo], %[rx], s96 offen offset:1024 lds\n" \
  "LP1_qa%=:\n" \
  "v_mfma_f32_16x16x32_bf16 %[c12], v[88:91], v[108:111], %[c12]\n" \
  "s_cbranch_scc1 LP1_qb%=\n" \
  "s_add_u32 m0, s95, 0x4000\n" \
  "s_add_u32 s91, s91, 128\n" \
  "buffer_load_dwordx4 %[wvo0], %[rw], s92 offen lds\n" \
  "buffer_load_dwordx4 %[wvo1], %[rw], s92 offen offset:1024 lds\n" \
  "s_add_u32 s92, s92, 245760\n" \
  "s_add_u32 s95, s95, 0x8000\n" \
  "s_and_b32 s95, s95, 0x1ffff\n" \
  "s_cmpk_eq_u32 s91, 1280\n" \
  "s_cselect_b32 s91, 0, s91\n" \
  "s_cselect_b32 s92, 0, s92\n" \
  "s_branch LP1_qc%=\n" \
  "LP1_qb%=:\n" \
  "s_cmp_lg_u32 s90, 6\n" \
  "s_cbranch_scc1 LP1_qc%=\n" \
  "buffer_load_dwordx4 %[st], %[stvo], %[rws], 0 offen sc1\n" \
  "buffer_load_dwordx2 %[cs], %[csvo], %[rws], 0 offen sc1\n" \
  "s_lshl_b32 s96, %[w2k], 1\n" \
  "s_add_u32 m0, s96, 0x20000\n" \
  "s_mov_b32 s96, 0\n" \
  "buffer_load_dwordx4 %[rvo], %[rr], s96 offen offset:0 lds\n" \
  "s_add_u32 s96, s96, %[rst]\n" \
  "buffer_load_dwordx4 %[rvo], %[rr], s96 offen offset:1024 lds\n" \
  "s_add_u32 s96, s96, %[rst]\n" \
  "buffer_load_dwordx4 %[rvo], %[rr], s96 offen offset:2048 lds\n" \
  "s_add_u32 s96, s96, %[rst]\n" \
  "buffer_load_dwordx4 %[rvo], %[rr], s96 offen offset:3072 lds\n" \
  "s_add_u32 s96, s96, %[rst]\n" \
  "LP1_qc%=:\n" \
  "v_mfma_f32_16x16x32_bf16 %[c03], v[92:95], v[104:107], %[c03]\n" \
  "s_mov_b32 s93, s94\n" \
  "s_add_u32 s90, s90, 1\n" \
  "s_cmp_lg_u32 s90, 10\n" \
  "v_mfma_f32_16x16x32_bf16 %[c13], v[92:95], v[108:111], %[c13]\n" \
  "s_cbranch_scc1 LP1_loop%=\n" \
  "s_waitcnt vmcnt(4) lgkmcnt(0)\n" \
  "s_nop 5\n" \
  ""
#define P1_CLOB "v64", "v65", "v66", "v67", "v68", "v69", "v70", "v71", "v72", "v73", "v74", "v75", "v76", "v77", "v78", "v79", "v80", "v81", "v82", "v83", "v84", "v85", "v86", "v87", "v88", "v89", "v90", "v91", "v92", "v93", "v94", "v95", "v96", "v97", "v98", "v99", "v100", "v101", "v102", "v103", "v104", "v105", "v106", "v107", "v108", "v109", "v110", "v111", "v112", "v113", "v114", "s90", "s91", "s92", "s93", "s94", "s95", "s96"
#define ATT_ASM \
  "buffer_load_dwordx4 %[k16a], %[kvo], %[rs], %[sk] offen sc1\n" \
  "buffer_load_dwordx2 %[k8a], %[r8], %[rs], %[sk] offen sc1\n" \
  "s_add_u32 %[sk], %[sk], 5632\n" \
  "buffer_load_dwordx4 v[120:123], %[kvo], %[rs], %[sq] offen sc1\n" \
  "buffer_load_dwordx2 v[136:137], %[r8], %[rs], %[sq] offen sc1\n" \
  "s_add_u32 %[sq], %[sq], 2560\n" \
  "buffer_load_dwordx4 v[124:127], %[kvo], %[rs], %[sq] offen sc1\n" \
  "buffer_load_dwordx2 v[138:139], %[r8], %[rs], %[sq] offen sc1\n" \
  "s_add_u32 %[sq], %[sq], 2560\n" \
  "buffer_load_dwordx4 v[128:131], %[kvo], %[rs], %[sq] offen sc1\n" \
  "buffer_load_dwordx2 v[140:141], %[r8], %[rs], %[sq] offen sc1\n" \
  "s_add_u32 %[sq], %[sq], 2560\n" \
  "buffer_load_dwordx4 v[132:135], %[kvo], %[rs], %[sq] offen sc1\n" \
  "buffer_load_dwordx2 v[142:143], %[r8], %[rs], %[sq] offen sc1\n" \
  "s_add_u32 %[sq], %[sq], 2560\n" \
  "buffer_load_dwordx4 v[144:147], %[vvo], %[rs], %[sv] offen sc1\n" \
  "buffer_load_dwordx4 v[148:151], %[vvo], %[rs], %[sv] offen offset:768 sc1\n" \
  "s_add_u32 %[sv], %[sv], 5632\n" \
  "buffer_load_dwordx4 %[k16b], %[kvo], %[rs], %[sk] offen sc1\n" \
  "buffer_load_dwordx2 %[k8b], %[r8], %[rs], %[sk] offen sc1\n" \
  "s_add_u32 %[sk], %[sk], 5632\n" \
  "s_waitcnt vmcnt(4)\n" \
  "v_mfma_f32_32x32x16_bf16 v[192:207], %[k16a], v[120:123], 0\n" \
  "v_mfma_f32_32x32x8_bf16 v[192:207], %[k8a], v[136:137], v[192:207]\n" \
  "v_mfma_f32_32x32x16_bf16 v[208:223], %[k16a], v[124:127], 0\n" \
  "v_mfma_f32_32x32x8_bf16 v[208:223], %[k8a], v[138:139], v[208:223]\n" \
  "s_nop 7\n" \
  "s_nop 3\n" \
  "v_exp_f32 v192, v192\n" \
  "v_exp_f32 v193, v193\n" \
  "s_branch LATT_skip%=\n" \
  "LATT_loopp%=:\n" \
  "v_exp_f32 v192, v192\n" \
  "v_exp_f32 v193, v193\n" \
  "v_smfmac_f32_32x32x32_bf16 %[o3], v[156:159], v[248:255], %[idx]\n" \
  "LATT_skip%=:\n" \
  "buffer_load_dwordx4 v[152:155], %[vvo], %[rs], %[sv] offen sc1\n" \
  "buffer_load_dwordx4 v[156:159], %[vvo], %[rs], %[sv] offen offset:768 sc1\n" \
  "s_add_u32 %[sv], %[sv], 5632\n" \
  "v_exp_f32 v194, v194\n" \
  "v_exp_f32 v195, v195\n" \
  "v_exp_f32 v196, v196\n" \
  "v_exp_f32 v197, v197\n" \
  "v_mfma_f32_32x32x16_bf16 v[224:239], %[k16a], v[128:131], 0\n" \
  "v_exp_f32 v198, v198\n" \
  "v_exp_f32 v199, v199\n" \
  "v_exp_f32 v200, v200\n" \
  "v_exp_f32 v201, v201\n" \
  "s_waitcnt vmcnt(4)\n" \
  "v_smfmac_f32_32x32x32_bf16 %[o0], v[144:147], v[192:199], %[idx]\n" \
  "v_exp_f32 v202, v202\n" \
  "v_exp_f32 v203, v203\n" \
  "v_exp_f32 v204, v204\n" \
  "v_exp_f32 v205, v205\n" \
  "v_mfma_f32_32x32x8_bf16 v[224:239], %[k8a], v[140:141], v[224:239]\n" \
  "v_exp_f32 v206, v206\n" \
  "v_exp_f32 v207, v207\n" \
  "v_exp_f32 v208, v208\n" \
  "v_exp_f32 v209, v209\n" \
  "v_smfmac_f32_32x32x32_bf16 %[o0], v[148:151], v[200:207], %[idx]\n" \
  "v_exp_f32 v210, v210\n" \
  "v_exp_f32 v211, v211\n" \
  "v_exp_f32 v212, v212\n" \
  "v_exp_f32 v213, v213\n" \
  "v_mfma_f32_32x32x16_bf16 v[240:255], %[k16a], v[132:135], 0\n" \
  "v_exp_f32 v214, v214\n" \
  "v_exp_f32 v215, v215\n" \
  "v_exp_f32 v216, v216\n" \
  "v_exp_f32 v217, v217\n" \
  "v_smfmac_f32_32x32x32_bf16 %[o1], v[144:147], v[208:215], %[idx]\n" \
  "v_exp_f32 v218, v218\n" \
  "v_exp_f32 v219, v219\n" \
  "v_exp_f32 v220, v220\n" \
  "v_exp_f32 v221, v221\n" \
  "v_mfma_f32_32x32x8_bf16 v[240:255], %[k8a], v[142:143], v[240:255]\n" \
  "v_exp_f32 v222, v222\n" \
  "v_exp_f32 v223, v223\n" \
  "v_exp_f32 v224, v224\n" \
  "v_exp_f32 v225, v225\n" \
  "v_smfmac_f32_32x32x32_bf16 %[o1], v[148:151], v[216:223], %[idx]\n" \
  "v_exp_f32 v226, v226\n" \
  "v_exp_f32 v227, v227\n" \
  "v_exp_f32 v228, v228\n" \
  "v_exp_f32 v229, v229\n" \
  "s_waitcnt vmcnt(2)\n" \
  "v_mfma_f32_32x32x16_bf16 v[192:207], %[k16b], v[120:123], 0\n" \
  "buffer_load_dwordx4 %[k16a], %[kvo], %[rs], %[sk] offen sc1\n" \
  "buffer_load_dwordx2 %[k8a], %[r8], %[rs], %[sk] offen sc1\n" \
  "s_add_u32 %[sk], %[sk], 5632\n" \
  "v_exp_f32 v230, v230\n" \
  "v_exp_f32 v231, v231\n" \
  "buffer_store_dwordx4 %[ff], %[l16], %[rs], %[sx] offen sc1\n" \
  "s_add_u32 %[sx], %[sx], 1024\n" \
  "v_exp_f32 v232, v232\n" \
  "v_exp_f32 v233, v233\n" \
  "v_smfmac_f32_32x32x32_bf16 %[o2], v[144:147], v[224:231], %[idx]\n" \
  "v_exp_f32 v234, v234\n" \
  "v_exp_f32 v235, v235\n" \
  "v_exp_f32 v236, v236\n" \
  "v_exp_f32 v237, v237\n" \
  "v_mfma_f32_32x32x8_bf16 v[192:207], %[k8b], v[136:137], v[192:207]\n" \
  "v_exp_f32 v238, v238\n" \
  "v_exp_f32 v239, v239\n" \
  "v_exp_f32 v240, v240\n" \
  "v_exp_f32 v241, v241\n" \
  "v_smfmac_f32_32x32x32_bf16 %[o2], v[148:151], v[232:239], %[idx]\n" \
  "v_exp_f32 v242, v242\n" \
  "v_exp_f32 v243, v243\n" \
  "v_exp_f32 v244, v244\n" \
  "v_exp_f32 v245, v245\n" \
  "v_mfma_f32_32x32x16_bf16 v[208:223], %[k16b], v[124:127], 0\n" \
  "v_exp_f32 v246, v246\n" \
  "v_exp_f32 v247, v247\n" \
  "v_exp_f32 v248, v248\n" \
  "v_exp_f32 v249, v249\n" \
  "v_smfmac_f32_32x32x32_bf16 %[o3], v[144:147], v[240:247], %[idx]\n" \
  "v_exp_f32 v250, v250\n" \
  "v_exp_f32 v251, v251\n" \
  "v_exp_f32 v252, v252\n" \
  "v_exp_f32 v253, v253\n" \
  "v_mfma_f32_32x32x8_bf16 v[208:223], %[k8b], v[138:139], v[208:223]\n" \
  "v_exp_f32 v254, v254\n" \
  "v_exp_f32 v255, v255\n" \
  "v_exp_f32 v192, v192\n" \
  "v_exp_f32 v193, v193\n" \
  "v_smfmac_f32_32x32x32_bf16 %[o3], v[148:151], v[248:255], %[idx]\n" \
  "buffer_load_dwordx4 v[144:147], %[vvo], %[rs], %[sv] offen sc1\n" \
  "buffer_load_dwordx4 v[148:151], %[vvo], %[rs], %[sv] offen offset:768 sc1\n" \
  "s_add_u32 %[sv], %[sv], 5632\n" \
  "v_exp_f32 v194, v194\n" \
  "v_exp_f32 v195, v195\n" \
  "v_exp_f32 v196, v196\n" \
  "v_exp_f32 v197, v197\n" \
  "v_mfma_f32_32x32x16_bf16 v[224:239], %[k16b], v[128:131], 0\n" \
  "v_exp_f32 v198, v198\n" \
  "v_exp_f32 v199, v199\n" \
  "v_exp_f32 v200, v200\n" \
  "v_exp_f32 v201, v201\n" \
  "s_waitcnt vmcnt(5)\n" \
  "v_smfmac_f32_32x32x32_bf16 %[o0], v[152:155], v[192:199], %[idx]\n" \
  "v_exp_f32 v202, v202\n" \
  "v_exp_f32 v203, v203\n" \
  "v_exp_f32 v204, v204\n" \
  "v_exp_f32 v205, v205\n" \
  "v_mfma_f32_32x32x8_bf16 v[224:239], %[k8b], v[140:141], v[224:239]\n" \
  "v_exp_f32 v206, v206\n" \
  "v_exp_f32 v207, v207\n" \
  "v_exp_f32 v208, v208\n" \
  "v_exp_f32 v209, v209\n" \
  "v_smfmac_f32_32x32x32_bf16 %[o0], v[156:159], v[200:207], %[idx]\n" \
  "v_exp_f32 v210, v210\n" \
  "v_exp_f32 v211, v211\n" \
  "v_exp_f32 v212, v212\n" \
  "v_exp_f32 v213, v213\n" \
  "v_mfma_f32_32x32x16_bf16 v[240:255], %[k16b], v[132:135], 0\n" \
  "v_exp_f32 v214, v214\n" \
  "v_exp_f32 v215, v215\n" \
  "v_exp_f32 v216, v216\n" \
  "v_exp_f32 v217, v217\n" \
  "v_smfmac_f32_32x32x32_bf16 %[o1], v[152:155], v[208:215], %[idx]\n" \
  "v_exp_f32 v218, v218\n" \
  "v_exp_f32 v219, v219\n" \
  "v_exp_f32 v220, v220\n" \
  "v_exp_f32 v221, v221\n" \
  "v_mfma_f32_32x32x8_bf16 v[240:255], %[k8b], v[142:143], v[240:255]\n" \
  "v_exp_f32 v222, v222\n" \
  "v_exp_f32 v223, v223\n" \
  "v_exp_f32 v224, v224\n" \
  "v_exp_f32 v225, v225\n" \
  "v_smfmac_f32_32x32x32_bf16 %[o1], v[156:159], v[216:223], %[idx]\n" \
  "v_exp_f32 v226, v226\n" \
  "v_exp_f32 v227, v227\n" \
  "v_exp_f32 v228, v228\n" \
  "v_exp_f32 v229, v229\n" \
  "s_waitcnt vmcnt(3)\n" \
  "v_mfma_f32_32x32x16_bf16 v[192:207], %[k16a], v[120:123], 0\n" \
  "buffer_load_dwordx4 %[k16b], %[kvo], %[rs], %[sk] offen sc1\n" \
  "buffer_load_dwordx2 %[k8b], %[r8], %[rs], %[sk] offen sc1\n" \
  "s_add_u32 %[sk], %[sk], 5632\n" \
  "v_exp_f32 v230, v230\n" \
  "v_exp_f32 v231, v231\n" \
  "v_exp_f32 v232, v232\n" \
  "v_exp_f32 v233, v233\n" \
  "v_smfmac_f32_32x32x32_bf16 %[o2], v[152:155], v[224:231], %[idx]\n" \
  "v_exp_f32 v234, v234\n" \
  "v_exp_f32 v235, v235\n" \
  "v_exp_f32 v236, v236\n" \
  "v_exp_f32 v237, v237\n" \
  "v_mfma_f32_32x32x8_bf16 v[192:207], %[k8a], v[136:137], v[192:207]\n" \
  "s_add_i32 %[cnt], %[cnt], -1\n" \
  "s_cmp_lg_u32 %[cnt], 6\n" \
  "s_cbranch_scc1 LATT_npp%=\n" \
  "s_setprio 2\n" \
  "LATT_npp%=:\n" \
  "v_exp_f32 v238, v238\n" \
  "v_exp_f32 v239, v239\n" \
  "v_exp_f32 v240, v240\n" \
  "v_exp_f32 v241, v241\n" \
  "v_smfmac_f32_32x32x32_bf16 %[o2], v[156:159], v[232:239], %[idx]\n" \
  "v_exp_f32 v242, v242\n" \
  "v_exp_f32 v243, v243\n" \
  "v_exp_f32 v244, v244\n" \
  "v_exp_f32 v245, v245\n" \
  "v_mfma_f32_32x32x16_bf16 v[208:223], %[k16a], v[124:127], 0\n" \
  "v_exp_f32 v246, v246\n" \
  "v_exp_f32 v247, v247\n" \
  "v_exp_f32 v248, v248\n" \
  "v_exp_f32 v249, v249\n" \
  "v_smfmac_f32_32x32x32_bf16 %[o3], v[152:155], v[240:247], %[idx]\n" \
  "v_exp_f32 v250, v250\n" \
  "v_exp_f32 v251, v251\n" \
  "v_exp_f32 v252, v252\n" \
  "v_exp_f32 v253, v253\n" \
  "v_mfma_f32_32x32x8_bf16 v[208:223], %[k8a], v[138:139], v[208:223]\n" \
  "s_cmp_lg_u32 %[cnt], 4\n" \
  "v_exp_f32 v254, v254\n" \
  "v_exp_f32 v255, v255\n" \
  "s_cbranch_scc1 LATT_loopp%=\n" \
  "s_mov_b32 %[sx], 0x20000\n" \
  "LATT_poll%=:\n" \
  "buffer_load_dwordx2 %[fl], %[fvo], %[rs], 0 offen sc1\n" \
  "s_sub_u32 %[sx], %[sx], 1\n" \
  "s_waitcnt vmcnt(0)\n" \
  "v_cmp_ne_u64 vcc, %[tag], %[fl]\n" \
  "s_cbranch_scc1 LATT_go%=\n" \
  "s_cbranch_vccnz LATT_poll%=\n" \
  "LATT_go%=:\n" \
  "s_setprio 1\n" \
  "LATT_loopd%=:\n" \
  "v_exp_f32 v192, v192\n" \
  "v_exp_f32 v193, v193\n" \
  "v_smfmac_f32_32x32x32_bf16 %[o3], v[156:159], v[248:255], %[idx]\n" \
  "buffer_load_dwordx4 v[152:155], %[vvo], %[rs], %[sv] offen sc1\n" \
  "buffer_load_dwordx4 v[156:159], %[vvo], %[rs], %[sv] offen offset:768 sc1\n" \
  "s_add_u32 %[sv], %[sv], 5632\n" \
  "v_exp_f32 v194, v194\n" \
  "v_exp_f32 v195, v195\n" \
  "v_exp_f32 v196, v196\n" \
  "v_exp_f32 v197, v197\n" \
  "v_mfma_f32_32x32x16_bf16 v[224:239], %[k16a], v[128:131], 0\n" \
  "v_exp_f32 v198, v198\n" \
  "v_exp_f32 v199, v199\n" \
  "v_exp_f32 v200, v200\n" \
  "v_exp_f32 v201, v201\n" \
  "s_waitcnt vmcnt(4)\n" \
  "v_smfmac_f32_32x32x32_bf16 %[o0], v[144:147], v[192:199], %[idx]\n" \
  "v_exp_f32 v202, v202\n" \
  "v_exp_f32 v203, v203\n" \
  "v_exp_f32 v204, v204\n" \
  "v_exp_f32 v205, v205\n" \
  "v_mfma_f32_32x32x8_bf16 v[224:239], %[k8a], v[140:141], v[224:239]\n" \
  "v_exp_f32 v206, v206\n" \
  "v_exp_f32 v207, v207\n" \
  "v_exp_f32 v208, v208\n" \
  "v_exp_f32 v209, v209\n" \
  "v_smfmac_f32_32x32x32_bf16 %[o0], v[148:151], v[200:207], %[idx]\n" \
  "v_exp_f32 v210, v210\n" \
  "v_exp_f32 v211, v211\n" \
  "v_exp_f32 v212, v212\n" \
  "v_exp_f32 v213, v213\n" \
  "v_mfma_f32_32x32x16_bf16 v[240:255], %[k16a], v[132:135], 0\n" \
  "v_exp_f32 v214, v214\n" \
  "v_exp_f32 v215, v215\n" \
  "v_exp_f32 v216, v216\n" \
  "v_exp_f32 v217, v217\n" \
  "v_smfmac_f32_32x32x32_bf16 %[o1], v[144:147], v[208:215], %[idx]\n" \
  "v_exp_f32 v218, v218\n" \
  "v_exp_f32 v219, v219\n" \
  "v_exp_f32 v220, v220\n" \
  "v_exp_f32 v221, v221\n" \
  "v_mfma_f32_32x32x8_bf16 v[240:255], %[k8a], v[142:143], v[240:255]\n" \
  "v_exp_f32 v222, v222\n" \
  "v_exp_f32 v223, v223\n" \
  "v_exp_f32 v224, v224\n" \
  "v_exp_f32 v225, v225\n" \
  "v_smfmac_f32_32x32x32_bf16 %[o1], v[148:151], v[216:223], %[idx]\n" \
  "v_exp_f32 v226, v226\n" \
  "v_exp_f32 v227, v227\n" \
  "v_exp_f32 v228, v228\n" \
  "v_exp_f32 v229, v229\n" \
  "s_waitcnt vmcnt(2)\n" \
  "v_mfma_f32_32x32x16_bf16 v[192:207], %[k16b], v[120:123], 0\n" \
  "buffer_load_dwordx4 %[k16a], %[kvo], %[rs], %[sk] offen sc1\n" \
  "buffer_load_dwordx2 %[k8a], %[r8], %[rs], %[sk] offen sc1\n" \
  "s_add_u32 %[sk], %[sk], 5632\n" \
  "v_exp_f32 v230, v230\n" \
  "v_exp_f32 v231, v231\n" \
  "s_mov_b32 m0, %[sm]\n" \
  "s_add_u32 %[sm], %[sm], 3072\n" \
  "buffer_load_dwordx4 %[l16], %[rs], %[sd] offen offset:0 sc1 lds\n" \
  "buffer_load_dwordx4 %[l16], %[rs], %[sd] offen offset:1024 sc1 lds\n" \
  "buffer_load_dwordx4 %[l16], %[rs], %[sd] offen offset:2048 sc1 lds\n" \
  "s_add_u32 %[sd], %[sd], 3072\n" \
  "v_exp_f32 v232, v232\n" \
  "v_exp_f32 v233, v233\n" \
  "v_smfmac_f32_32x32x32_bf16 %[o2], v[144:147], v[224:231], %[idx]\n" \
  "v_exp_f32 v234, v234\n" \
  "v_exp_f32 v235, v235\n" \
  "v_exp_f32 v236, v236\n" \
  "v_exp_f32 v237, v237\n" \
  "v_mfma_f32_32x32x8_bf16 v[192:207], %[k8b], v[136:137], v[192:207]\n" \
  "v_exp_f32 v238, v238\n" \
  "v_exp_f32 v239, v239\n" \
  "v_exp_f32 v240, v240\n" \
  "v_exp_f32 v241, v241\n" \
  "v_smfmac_f32_32x32x32_bf16 %[o2], v[148:151], v[232:239], %[idx]\n" \
  "v_exp_f32 v242, v242\n" \
  "v_exp_f32 v243, v243\n" \
  "v_exp_f32 v244, v244\n" \
  "v_exp_f32 v245, v245\n" \
  "v_mfma_f32_32x32x16_bf16 v[208:223], %[k16b], v[124:127], 0\n" \
  "v_exp_f32 v246, v246\n" \
  "v_exp_f32 v247, v247\n" \
  "v_exp_f32 v248, v248\n" \
  "v_exp_f32 v249, v249\n" \
  "v_smfmac_f32_32x32x32_bf16 %[o3], v[144:147], v[240:247], %[idx]\n" \
  "v_exp_f32 v250, v250\n" \
  "v_exp_f32 v251, v251\n" \
  "v_exp_f32 v252, v252\n" \
  "v_exp_f32 v253, v253\n" \
  "v_mfma_f32_32x32x8_bf16 v[208:223], %[k8b], v[138:139], v[208:223]\n" \
  "v_exp_f32 v254, v254\n" \
  "v_exp_f32 v255, v255\n" \
  "v_exp_f32 v192, v192\n" \
  "v_exp_f32 v193, v193\n" \
  "v_smfmac_f32_32x32x32_bf16 %[o3], v[148:151], v[248:255], %[idx]\n" \
  "buffer_load_dwordx4 v[144:147], %[vvo], %[rs], %[sv] offen sc1\n" \
  "buffer_load_dwordx4 v[148:151], %[vvo], %[rs], %[sv] offen offset:768 sc1\n" \
  "s_add_u32 %[sv], %[sv], 5632\n" \
  "v_exp_f32 v194, v194\n" \
  "v_exp_f32 v195, v195\n" \
  "v_exp_f32 v196, v196\n" \
  "v_exp_f32 v197, v197\n" \
  "v_mfma_f32_32x32x16_bf16 v[224:239], %[k16b], v[128:131], 0\n" \
  "v_exp_f32 v198, v198\n" \
  "v_exp_f32 v199, v199\n" \
  "v_exp_f32 v200, v200\n" \
  "v_exp_f32 v201, v201\n" \
  "s_waitcnt vmcnt(7)\n" \
  "v_smfmac_f32_32x32x32_bf16 %[o0], v[152:155], v[192:199], %[idx]\n" \
  "v_exp_f32 v202, v202\n" \
  "v_exp_f32 v203, v203\n" \
  "v_exp_f32 v204, v204\n" \
  "v_exp_f32 v205, v205\n" \
  "v_mfma_f32_32x32x8_bf16 v[224:239], %[k8b], v[140:141], v[224:239]\n" \
  "v_exp_f32 v206, v206\n" \
  "v_exp_f32 v207, v207\n" \
  "v_exp_f32 v208, v208\n" \
  "v_exp_f32 v209, v209\n" \
  "v_smfmac_f32_32x32x32_bf16 %[o0], v[156:159], v[200:207], %[idx]\n" \
  "v_exp_f32 v210, v210\n" \
  "v_exp_f32 v211, v211\n" \
  "v_exp_f32 v212, v212\n" \
  "v_exp_f32 v213, v213\n" \
  "v_mfma_f32_32x32x16_bf16 v[240:255], %[k16b], v[132:135], 0\n" \
  "v_exp_f32 v214, v214\n" \
  "v_exp_f32 v215, v215\n" \
  "v_exp_f32 v216, v216\n" \
  "v_exp_f32 v217, v217\n" \
  "v_smfmac_f32_32x32x32_bf16 %[o1], v[152:155], v[208:215], %[idx]\n" \
  "v_exp_f32 v218, v218\n" \
  "v_exp_f32 v219, v219\n" \
  "v_exp_f32 v220, v220\n" \
  "v_exp_f32 v221, v221\n" \
  "v_mfma_f32_32x32x8_bf16 v[240:255], %[k8b], v[142:143], v[240:255]\n" \
  "v_exp_f32 v222, v222\n" \
  "v_exp_f32 v223, v223\n" \
  "v_exp_f32 v224, v224\n" \
  "v_exp_f32 v225, v225\n" \
  "v_smfmac_f32_32x32x32_bf16 %[o1], v[156:159], v[216:223], %[idx]\n" \
  "v_exp_f32 v226, v226\n" \
  "v_exp_f32 v227, v227\n" \
  "v_exp_f32 v228, v228\n" \
  "v_exp_f32 v229, v229\n" \
  "s_waitcnt vmcnt(5)\n" \
  "v_mfma_f32_32x32x16_bf16 v[192:207], %[k16a], v[120:123], 0\n" \
  "buffer_load_dwordx4 %[k16b], %[kvo], %[rs], %[sk] offen sc1\n" \
  "buffer_load_dwordx2 %[k8b], %[r8], %[rs], %[sk] offen sc1\n" \
  "s_add_u32 %[sk], %[sk], 5632\n" \
  "v_exp_f32 v230, v230\n" \
  "v_exp_f32 v231, v231\n" \
  "v_exp_f32 v232, v232\n" \
  "v_exp_f32 v233, v233\n" \
  "v_smfmac_f32_32x32x32_bf16 %[o2], v[152:155], v[224:231], %[idx]\n" \
  "v_exp_f32 v234, v234\n" \
  "v_exp_f32 v235, v235\n" \
  "v_exp_f32 v236, v236\n" \
  "v_exp_f32 v237, v237\n" \
  "v_mfma_f32_32x32x8_bf16 v[192:207], %[k8a], v[136:137], v[192:207]\n" \
  "s_add_i32 %[cnt], %[cnt], -1\n" \
  "s_cmp_lg_u32 %[cnt], 1\n" \
  "s_cbranch_scc1 LATT_npd%=\n" \
  "s_setprio 0\n" \
  "LATT_npd%=:\n" \
  "v_exp_f32 v238, v238\n" \
  "v_exp_f32 v239, v239\n" \
  "v_exp_f32 v240, v240\n" \
  "v_exp_f32 v241, v241\n" \
  "v_smfmac_f32_32x32x32_bf16 %[o2], v[156:159], v[232:239], %[idx]\n" \
  "v_exp_f32 v242, v242\n" \
  "v_exp_f32 v243, v243\n" \
  "v_exp_f32 v244, v244\n" \
  "v_exp_f32 v245, v245\n" \
  "v_mfma_f32_32x32x16_bf16 v[208:223], %[k16a], v[124:127], 0\n" \
  "v_exp_f32 v246, v246\n" \
  "v_exp_f32 v247, v247\n" \
  "v_exp_f32 v248, v248\n" \
  "v_exp_f32 v249, v249\n" \
  "v_smfmac_f32_32x32x32_bf16 %[o3], v[152:155], v[240:247], %[idx]\n" \
  "v_exp_f32 v250, v250\n" \
  "v_exp_f32 v251, v251\n" \
  "v_exp_f32 v252, v252\n" \
  "v_exp_f32 v253, v253\n" \
  "v_mfma_f32_32x32x8_bf16 v[208:223], %[k8a], v[138:139], v[208:223]\n" \
  "s_cmp_lg_u32 %[cnt], 0\n" \
  "v_exp_f32 v254, v254\n" \
  "v_exp_f32 v255, v255\n" \
  "s_cbranch_scc1 LATT_loopd%=\n" \
  "s_nop 1\n" \
  "v_smfmac_f32_32x32x32_bf16 %[o3], v[156:159], v[248:255], %[idx]\n" \
  "s_waitcnt vmcnt(0)\n" \
  "s_nop 7\n" \
  "s_nop 3\n" \
  ""
#define ATT_CLOB "v192", "v193", "v194", "v195", "v196", "v197", "v198", "v199", "v200", "v201", "v202", "v203", "v204", "v205", "v206", "v207", "v208", "v209", "v210", "v211", "v212", "v213", "v214", "v215", "v216", "v217", "v218", "v219", "v220", "v221", "v222", "v223", "v224", "v225", "v226", "v227", "v228", "v229", "v230", "v231", "v232", "v233", "v234", "v235", "v236", "v237", "v238", "v239", "v240", "v241", "v242", "v243", "v244", "v245", "v246", "v247", "v248", "v249", "v250", "v251", "v252", "v253", "v254", "v255", "v120", "v121", "v122", "v123", "v124", "v125", "v126", "v127", "v128", "v129", "v130", "v131", "v132", "v133", "v134", "v135", "v136", "v137", "v138", "v139", "v140", "v141", "v142", "v143", "v144", "v145", "v146", "v147", "v148", "v149", "v150", "v151", "v152", "v153", "v154", "v155", "v156", "v157", "v158", "v159"
#define P3_ASM \
  "buffer_load_dwordx4 v[64:67], %[va0], %[rs], %[sa] offen offset:0 sc1\n" \
  "buffer_load_dwordx4 v[68:71], %[va0], %[rs], %[sa] offen offset:1024 sc1\n" \
  "buffer_load_dwordx4 v[72:75], %[va0], %[rs], %[sa] offen offset:2048 sc1\n" \
  "buffer_load_dwordx4 v[76:79], %[va0], %[rs], %[sa] offen offset:3072 sc1\n" \
  "s_add_u32 %[sa], %[sa], 4096\n" \
  "buffer_load_dwordx4 v[80:83], %[va0], %[rs], %[sa] offen offset:0 sc1\n" \
  "buffer_load_dwordx4 v[84:87], %[va0], %[rs], %[sa] offen offset:1024 sc1\n" \
  "ds_read_b128 v[96:99], %[vw] offset:0\n" \
  "ds_read_b128 v[100:103], %[vw] offset:1024\n" \
  "ds_read_b128 v[104:107], %[vw] offset:2048\n" \
  "ds_read_b128 v[108:111], %[vw] offset:3072\n" \
  "ds_read_b128 v[112:115], %[vw] offset:4096\n" \
  "ds_read_b128 v[116:119], %[vw] offset:10240\n" \
  "ds_read_b128 v[120:123], %[vw] offset:11264\n" \
  "ds_read_b128 v[124:127], %[vw] offset:12288\n" \
  "s_waitcnt vmcnt(5)\n" \
  "ds_read_b128 v[128:131], %[vw] offset:13312\n" \
  "s_waitcnt lgkmcnt(8)\n" \
  "v_mfma_f32_16x16x32_bf16 %[c0], v[96:99], v[64:67], %[c0]\n" \
  "buffer_load_dwordx4 v[88:91], %[va0], %[rs], %[sa] offen offset:2048 sc1\n" \
  "ds_read_b128 v[132:135], %[vw] offset:14336\n" \
  "s_waitcnt lgkmcnt(8)\n" \
  "v_mfma_f32_16x16x32_bf16 %[c1], v[100:103], v[64:67], %[c1]\n" \
  "ds_read_b128 v[96:99], %[vw] offset:20480\n" \
  "s_waitcnt lgkmcnt(8)\n" \
  "v_mfma_f32_16x16x32_bf16 %[c2], v[104:107], v[64:67], 0\n" \
  "ds_read_b128 v[100:103], %[vw] offset:21504\n" \
  "s_waitcnt lgkmcnt(8)\n" \
  "v_mfma_f32_16x16x32_bf16 %[c3], v[108:111], v[64:67], 0\n" \
  "ds_read_b128 v[104:107], %[vw] offset:22528\n" \
  "s_waitcnt lgkmcnt(8)\n" \
  "v_mfma_f32_16x16x32_bf16 %[c4], v[112:115], v[64:67], %[c4]\n" \
  "s_waitcnt vmcnt(5)\n" \
  "ds_read_b128 v[108:111], %[vw] offset:23552\n" \
  "s_waitcnt lgkmcnt(8)\n" \
  "v_mfma_f32_16x16x32_bf16 %[c0], v[116:119], v[68:71], %[c0]\n" \
  "buffer_load_dwordx4 v[92:95], %[va0], %[rs], %[sa] offen offset:3072 sc1\n" \
  "ds_read_b128 v[112:115], %[vw] offset:24576\n" \
  "s_waitcnt lgkmcnt(8)\n" \
  "v_mfma_f32_16x16x32_bf16 %[c1], v[120:123], v[68:71], %[c1]\n" \
  "ds_read_b128 v[116:119], %[vw] offset:30720\n" \
  "s_waitcnt lgkmcnt(8)\n" \
  "v_mfma_f32_16x16x32_bf16 %[c2], v[124:127], v[68:71], %[c2]\n" \
  "ds_read_b128 v[120:123], %[vw] offset:31744\n" \
  "s_waitcnt lgkmcnt(8)\n" \
  "v_mfma_f32_16x16x32_bf16 %[c3], v[128:131], v[68:71], %[c3]\n" \
  "ds_read_b128 v[124:127], %[vw] offset:32768\n" \
  "s_waitcnt lgkmcnt(8)\n" \
  "v_mfma_f32_16x16x32_bf16 %[c4], v[132:135], v[68:71], %[c4]\n" \
  "s_waitcnt vmcnt(5)\n" \
  "ds_read_b128 v[128:131], %[vw] offset:33792\n" \
  "s_waitcnt lgkmcnt(8)\n" \
  "v_mfma_f32_16x16x32_bf16 %[c0], v[96:99], v[72:75], %[c0]\n" \
  "s_add_u32 %[sa], %[sa], 4096\n" \
  "buffer_load_dwordx4 v[64:67], %[va0], %[rs], %[sa] offen offset:0 sc1\n" \
  "ds_read_b128 v[132:135], %[vw] offset:34816\n" \
  "s_waitcnt lgkmcnt(8)\n" \
  "v_mfma_f32_16x16x32_bf16 %[c1], v[100:103], v[72:75], %[c1]\n" \
  "ds_read_b128 v[96:99], %[vw] offset:40960\n" \
  "s_waitcnt lgkmcnt(8)\n" \
  "v_mfma_f32_16x16x32_bf16 %[c2], v[104:107], v[72:75], %[c2]\n" \
  "ds_read_b128 v[100:103], %[vw] offset:41984\n" \
  "s_waitcnt lgkmcnt(8)\n" \
  "v_mfma_f32_16x16x32_bf16 %[c3], v[108:111], v[72:75], %[c3]\n" \
  "ds_read_b128 v[104:107], %[vw] offset:43008\n" \
  "s_waitcnt lgkmcnt(8)\n" \
  "v_mfma_f32_16x16x32_bf16 %[c4], v[112:115], v[72:75], %[c4]\n" \
  "s_waitcnt vmcnt(5)\n" \
  "ds_read_b128 v[108:111], %[vw] offset:44032\n" \
  "s_waitcnt lgkmcnt(8)\n" \
  "v_mfma_f32_16x16x32_bf16 %[c0], v[116:119], v[76:79], %[c0]\n" \
  "buffer_load_dwordx4 v[68:71], %[va0], %[rs], %[sa] offen offset:1024 sc1\n" \
  "ds_read_b128 v[112:115], %[vw] offset:45056\n" \
  "s_waitcnt lgkmcnt(8)\n" \
  "v_mfma_f32_16x16x32_bf16 %[c1], v[120:123], v[76:79], %[c1]\n" \
  "ds_read_b128 v[116:119], %[vw] offset:51200\n" \
  "s_waitcnt lgkmcnt(8)\n" \
  "v_mfma_f32_16x16x32_bf16 %[c2], v[124:127], v[76:79], %[c2]\n" \
  "ds_read_b128 v[120:123], %[vw] offset:52224\n" \
  "s_waitcnt lgkmcnt(8)\n" \
  "v_mfma_f32_16x16x32_bf16 %[c3], v[128:131], v[76:79], %[c3]\n" \
  "ds_read_b128 v[124:127], %[vw] offset:53248\n" \
  "s_waitcnt lgkmcnt(8)\n" \
  "v_mfma_f32_16x16x32_bf16 %[c4], v[132:135], v[76:79], %[c4]\n" \
  "s_waitcnt vmcnt(5)\n" \
  "ds_read_b128 v[128:131], %[vw] offset:54272\n" \
  "s_waitcnt lgkmcnt(8)\n" \
  "v_mfma_f32_16x16x32_bf16 %[c0], v[96:99], v[80:83], %[c0]\n" \
  "ds_read_b128 v[132:135], %[vw] offset:55296\n" \
  "s_waitcnt lgkmcnt(8)\n" \
  "v_mfma_f32_16x16x32_bf16 %[c1], v[100:103], v[80:83], %[c1]\n" \
  "ds_read_b128 v[96:99], %[vw] offset:61440\n" \
  "s_waitcnt lgkmcnt(8)\n" \
  "v_mfma_f32_16x16x32_bf16 %[c2], v[104:107], v[80:83], %[c2]\n" \
  "ds_read_b128 v[100:103], %[vw] offset:62464\n" \
  "s_waitcnt lgkmcnt(8)\n" \
  "v_mfma_f32_16x16x32_bf16 %[c3], v[108:111], v[80:83], %[c3]\n" \
  "ds_read_b128 v[104:107], %[vw] offset:63488\n" \
  "s_waitcnt lgkmcnt(8)\n" \
  "v_mfma_f32_16x16x32_bf16 %[c4], v[112:115], v[80:83], %[c4]\n" \
  "s_waitcnt vmcnt(4)\n" \
  "ds_read_b128 v[108:111], %[vw] offset:64512\n" \
  "s_waitcnt lgkmcnt(8)\n" \
  "v_mfma_f32_16x16x32_bf16 %[c0], v[116:119], v[84:87], %[c0]\n" \
  "v_add_u32 %[vw], 65536, %[vw]\n" \
  "ds_read_b128 v[112:115], %[vw] offset:0\n" \
  "s_waitcnt lgkmcnt(8)\n" \
  "v_mfma_f32_16x16x32_bf16 %[c1], v[120:123], v[84:87], %[c1]\n" \
  "ds_read_b128 v[116:119], %[vw] offset:6144\n" \
  "s_waitcnt lgkmcnt(8)\n" \
  "v_mfma_f32_16x16x32_bf16 %[c2], v[124:127], v[84:87], %[c2]\n" \
  "ds_read_b128 v[120:123], %[vw] offset:7168\n" \
  "s_waitcnt lgkmcnt(8)\n" \
  "v_mfma_f32_16x16x32_bf16 %[c3], v[128:131], v[84:87], %[c3]\n" \
  "ds_read_b128 v[124:127], %[vw] offset:8192\n" \
  "s_waitcnt lgkmcnt(8)\n" \
  "v_mfma_f32_16x16x32_bf16 %[c4], v[132:135], v[84:87], %[c4]\n" \
  "s_waitcnt vmcnt(0)\n" \
  "s_barrier\n" \
  "ds_read_b128 v[128:131], %[vw] offset:9216\n" \
  "s_waitcnt lgkmcnt(8)\n" \
  "v_mfma_f32_16x16x32_bf16 %[c0], v[96:99], v[88:91], %[c0]\n" \
  "ds_read_b128 v[132:135], %[vw] offset:10240\n" \
  "s_waitcnt lgkmcnt(8)\n" \
  "v_mfma_f32_16x16x32_bf16 %[c1], v[100:103], v[88:91], %[c1]\n" \
  "s_cmp_lg_u32 %[kh], 0\n" \
  "s_cbranch_scc1 LG_o32_0_0%=\n" \
  "s_mov_b32 m0, %[dm0]\n" \
  "s_nop 0\n" \
  "buffer_load_dwordx4 %[l16], %[rs], %[ds0] offen offset:0 sc1 lds\n" \
  "LG_o32_0_0%=:\n" \
  "ds_read_b128 v[96:99], %[vw] offset:16384\n" \
  "s_waitcnt lgkmcnt(8)\n" \
  "v_mfma_f32_16x16x32_bf16 %[c2], v[104:107], v[88:91], %[c2]\n" \
  "ds_read_b128 v[100:103], %[vw] offset:17408\n" \
  "s_waitcnt lgkmcnt(8)\n" \
  "v_mfma_f32_16x16x32_bf16 %[c3], v[108:111], v[88:91], %[c3]\n" \
  "ds_read_b128 v[104:107], %[vw] offset:18432\n" \
  "s_waitcnt lgkmcnt(8)\n" \
  "v_mfma_f32_16x16x32_bf16 %[c4], v[112:115], v[88:91], %[c4]\n" \
  "ds_read_b128 v[108:111], %[vw] offset:19456\n" \
  "s_waitcnt lgkmcnt(8)\n" \
  "v_mfma_f32_16x16x32_bf16 %[c0], v[116:119], v[92:95], %[c0]\n" \
  "s_cmp_lg_u32 %[kh], 0\n" \
  "s_cbranch_scc1 LG_o36_0_1%=\n" \
  "s_mov_b32 m0, %[dm0]\n" \
  "s_nop 0\n" \
  "buffer_load_dwordx4 %[l16], %[rs], %[ds0] offen offset:1024 sc1 lds\n" \
  "LG_o36_0_1%=:\n" \
  "ds_read_b128 v[112:115], %[vw] offset:20480\n" \
  "s_waitcnt lgkmcnt(8)\n" \
  "v_mfma_f32_16x16x32_bf16 %[c1], v[120:123], v[92:95], %[c1]\n" \
  "ds_read_b128 v[116:119], %[vw] offset:26624\n" \
  "s_waitcnt lgkmcnt(8)\n" \
  "v_mfma_f32_16x16x32_bf16 %[c2], v[124:127], v[92:95], %[c2]\n" \
  "ds_read_b128 v[120:123], %[vw] offset:27648\n" \
  "s_waitcnt lgkmcnt(8)\n" \
  "v_mfma_f32_16x16x32_bf16 %[c3], v[128:131], v[92:95], %[c3]\n" \
  "ds_read_b128 v[124:127], %[vw] offset:28672\n" \
  "s_waitcnt lgkmcnt(8)\n" \
  "v_mfma_f32_16x16x32_bf16 %[c4], v[132:135], v[92:95], %[c4]\n" \
  "s_cmp_lg_u32 %[kh], 0\n" \
  "s_cbranch_scc1 LG_o40_0_2%=\n" \
  "s_mov_b32 m0, %[dm0]\n" \
  "s_nop 0\n" \
  "buffer_load_dwordx4 %[l16], %[rs], %[ds0] offen offset:2048 sc1 lds\n" \
  "LG_o40_0_2%=:\n" \
  "ds_read_b128 v[128:131], %[vw] offset:29696\n" \
  "s_waitcnt lgkmcnt(8)\n" \
  "v_mfma_f32_16x16x32_bf16 %[c0], v[96:99], v[64:67], %[c0]\n" \
  "ds_read_b128 v[132:135], %[vw] offset:30720\n" \
  "s_waitcnt lgkmcnt(8)\n" \
  "v_mfma_f32_16x16x32_bf16 %[c1], v[100:103], v[64:67], %[c1]\n" \
  "s_waitcnt lgkmcnt(7)\n" \
  "v_mfma_f32_16x16x32_bf16 %[c2], v[104:107], v[64:67], %[c2]\n" \
  "s_waitcnt lgkmcnt(6)\n" \
  "v_mfma_f32_16x16x32_bf16 %[c3], v[108:111], v[64:67], %[c3]\n" \
  "s_cmp_lg_u32 %[kh], 0\n" \
  "s_cbranch_scc1 LG_o44_0_3%=\n" \
  "s_mov_b32 m0, %[dm0]\n" \
  "s_nop 0\n" \
  "buffer_load_dwordx4 %[l16], %[rs], %[ds0] offen offset:3072 sc1 lds\n" \
  "LG_o44_0_3%=:\n" \
  "s_waitcnt lgkmcnt(5)\n" \
  "v_mfma_f32_16x16x32_bf16 %[c4], v[112:115], v[64:67], %[c4]\n" \
  "s_waitcnt lgkmcnt(4)\n" \
  "v_mfma_f32_16x16x32_bf16 %[c0], v[116:119], v[68:71], %[c0]\n" \
  "s_waitcnt lgkmcnt(3)\n" \
  "v_mfma_f32_16x16x32_bf16 %[c1], v[120:123], v[68:71], %[c1]\n" \
  "s_waitcnt lgkmcnt(2)\n" \
  "v_mfma_f32_16x16x32_bf16 %[c2], v[124:127], v[68:71], %[c2]\n" \
  "s_cmp_lg_u32 %[kh], 0\n" \
  "s_cbranch_scc1 LG_o48_0_4%=\n" \
  "s_add_u32 %[dm0], %[dm0], 4096\n" \
  "s_add_u32 %[ds0], %[ds0], 4096\n" \
  "s_mov_b32 m0, %[dm0]\n" \
  "s_nop 0\n" \
  "buffer_load_dwordx4 %[l16], %[rs], %[ds0] offen offset:0 sc1 lds\n" \
  "LG_o48_0_4%=:\n" \
  "s_waitcnt lgkmcnt(1)\n" \
  "v_mfma_f32_16x16x32_bf16 %[c3], v[128:131], v[68:71], %[c3]\n" \
  "s_waitcnt lgkmcnt(0)\n" \
  "v_mfma_f32_16x16x32_bf16 %[c4], v[132:135], v[68:71], %[c4]\n" \
  "s_cmp_lg_u32 %[kh], 0\n" \
  "s_cbranch_scc1 LG_e0_5%=\n" \
  "s_mov_b32 m0, %[dm0]\n" \
  "s_nop 0\n" \
  "buffer_load_dwordx4 %[l16], %[rs], %[ds0] offen offset:1024 sc1 lds\n" \
  "buffer_load_dwordx4 %[l16], %[rs], %[ds0] offen offset:2048 sc1 lds\n" \
  "buffer_load_dwordx4 %[l16], %[rs], %[ds0] offen offset:3072 sc1 lds\n" \
  "s_add_u32 %[dm0], %[dm0], 4096\n" \
  "s_add_u32 %[ds0], %[ds0], 4096\n" \
  "s_mov_b32 m0, %[dm0]\n" \
  "s_nop 0\n" \
  "buffer_load_dwordx4 %[l16], %[rs], %[ds0] offen offset:0 sc1 lds\n" \
  "buffer_load_dwordx4 %[l16], %[rs], %[ds0] offen offset:1024 sc1 lds\n" \
  "LG_e0_5%=:\n" \
  "s_nop 7\n" \
  ""
#define P4_ASM \
  "s_mov_b32 s88, %[sa]\n" \
  "s_mov_b32 s89, 0x4000\n" \
  "buffer_load_dwordx4 v[64:67], %[va0], %[rs], %[sa] offen offset:0 sc1\n" \
  "ds_read_b128 v[156:159], %[vc] offset:0\n" \
  "ds_read_b128 v[160:163], %[vc] offset:64\n" \
  "ds_read_b128 v[164:167], %[vc4] offset:0\n" \
  "buffer_load_dwordx4 v[68:71], %[va0], %[rs], %[sa] offen offset:1024 sc1\n" \
  "buffer_load_dwordx4 v[144:147], %[vst], %[rs], 0 offen offset:0 sc1\n" \
  "buffer_load_dwordx4 v[148:151], %[vst], %[rs], 0 offen offset:256 sc1\n" \
  "buffer_load_dwordx4 v[72:75], %[va0], %[rs], %[sa] offen offset:2048 sc1\n" \
  "buffer_load_dwordx4 v[76:79], %[va0], %[rs], %[sa] offen offset:3072 sc1\n" \
  "s_add_u32 %[sa], %[sa], 4096\n" \
  "buffer_load_dwordx4 v[80:83], %[va0], %[rs], %[sa] offen offset:0 sc1\n" \
  "buffer_load_dwordx4 v[84:87], %[va0], %[rs], %[sa] offen offset:1024 sc1\n" \
  "v_add_u32 %[vw], 98304, %[vw]\n" \
  "ds_read_b128 v[96:99], %[vw] offset:4096\n" \
  "ds_read_b128 v[100:103], %[vw] offset:5120\n" \
  "ds_read_b128 v[104:107], %[vw] offset:6144\n" \
  "ds_read_b128 v[108:111], %[vw] offset:7168\n" \
  "ds_read_b128 v[112:115], %[vw] offset:8192\n" \
  "ds_read_b128 v[116:119], %[vw] offset:14336\n" \
  "ds_read_b128 v[120:123], %[vw] offset:15360\n" \
  "ds_read_b128 v[124:127], %[vw] offset:16384\n" \
  "s_waitcnt vmcnt(7)\n" \
  "LG_ca0%=:\n" \
  "v_max3_u32 v180, v64, v65, v66\n" \
  "v_max3_u32 v180, v180, v67, v67\n" \
  "v_cmp_eq_u32 vcc, -1, v180\n" \
  "s_cbranch_vccnz LG_ra0%=\n" \
  "LG_ba0%=:\n" \
  "ds_read_b128 v[128:131], %[vw] offset:17408\n" \
  "s_waitcnt lgkmcnt(8)\n" \
  "v_mfma_f32_16x16x32_bf16 %[c0], v[96:99], v[64:67], 0\n" \
  "buffer_load_dwordx4 v[88:91], %[va0], %[rs], %[sa] offen offset:2048 sc1\n" \
  "ds_read_b128 v[132:135], %[vw] offset:18432\n" \
  "s_waitcnt lgkmcnt(8)\n" \
  "v_mfma_f32_16x16x32_bf16 %[c1], v[100:103], v[64:67], 0\n" \
  "ds_read_b128 v[96:99], %[vw] offset:24576\n" \
  "s_waitcnt lgkmcnt(8)\n" \
  "v_mfma_f32_16x16x32_bf16 %[c2], v[104:107], v[64:67], 0\n" \
  "ds_read_b128 v[100:103], %[vw] offset:25600\n" \
  "s_waitcnt lgkmcnt(8)\n" \
  "v_mfma_f32_16x16x32_bf16 %[c3], v[108:111], v[64:67], 0\n" \
  "ds_read_b128 v[104:107], %[vw] offset:26624\n" \
  "s_waitcnt lgkmcnt(8)\n" \
  "v_mfma_f32_16x16x32_bf16 %[c4], v[112:115], v[64:67], 0\n" \
  "s_waitcnt vmcnt(7)\n" \
  "LG_ca1%=:\n" \
  "v_max3_u32 v180, v68, v69, v70\n" \
  "v_max3_u32 v180, v180, v71, v71\n" \
  "v_cmp_eq_u32 vcc, -1, v180\n" \
  "s_cbranch_vccnz LG_ra1%=\n" \
  "LG_ba1%=:\n" \
  "ds_read_b128 v[108:111], %[vw] offset:27648\n" \
  "s_waitcnt lgkmcnt(8)\n" \
  "v_mfma_f32_16x16x32_bf16 %[c0], v[116:119], v[68:71], %[c0]\n" \
  "buffer_load_dwordx4 v[92:95], %[va0], %[rs], %[sa] offen offset:3072 sc1\n" \
  "ds_read_b128 v[112:115], %[vw] offset:28672\n" \
  "s_waitcnt lgkmcnt(8)\n" \
  "v_mfma_f32_16x16x32_bf16 %[c1], v[120:123], v[68:71], %[c1]\n" \
  "ds_read_b128 v[116:119], %[vw] offset:34816\n" \
  "s_waitcnt lgkmcnt(8)\n" \
  "v_mfma_f32_16x16x32_bf16 %[c2], v[124:127], v[68:71], %[c2]\n" \
  "ds_read_b128 v[120:123], %[vw] offset:35840\n" \
  "s_waitcnt lgkmcnt(8)\n" \
  "v_mfma_f32_16x16x32_bf16 %[c3], v[128:131], v[68:71], %[c3]\n" \
  "ds_read_b128 v[124:127], %[vw] offset:36864\n" \
  "s_waitcnt lgkmcnt(8)\n" \
  "v_mfma_f32_16x16x32_bf16 %[c4], v[132:135], v[68:71], %[c4]\n" \
  "s_waitcnt vmcnt(5)\n" \
  "LG_ca2%=:\n" \
  "v_max3_u32 v180, v72, v73, v74\n" \
  "v_max3_u32 v180, v180, v75, v75\n" \
  "v_cmp_eq_u32 vcc, -1, v180\n" \
  "s_cbranch_vccnz LG_ra2%=\n" \
  "LG_ba2%=:\n" \
  "ds_read_b128 v[128:131], %[vw] offset:37888\n" \
  "s_waitcnt lgkmcnt(8)\n" \
  "v_mfma_f32_16x16x32_bf16 %[c0], v[96:99], v[72:75], %[c0]\n" \
  "s_waitcnt vmcnt(6)\n" \
  "LG_cs%=:\n" \
  "v_max3_u32 v180, v144, v145, v146\n" \
  "v_max3_u32 v180, v180, v147, v148\n" \
  "v_max3_u32 v180, v180, v149, v150\n" \
  "v_max3_u32 v180, v180, v151, v151\n" \
  "v_cmp_eq_u32 vcc, -1, v180\n" \
  "s_cbranch_vccnz LG_rs%=\n" \
  "LG_bs%=:\n" \
  "v_add_f32 v144, v144, v146\n" \
  "s_add_u32 %[sa], %[sa], 4096\n" \
  "buffer_load_dwordx4 v[64:67], %[va0], %[rs], %[sa] offen offset:0 sc1\n" \
  "ds_read_b128 v[132:135], %[vw] offset:38912\n" \
  "s_waitcnt lgkmcnt(8)\n" \
  "v_mfma_f32_16x16x32_bf16 %[c1], v[100:103], v[72:75], %[c1]\n" \
  "v_add_f32 v148, v148, v150\n" \
  "ds_read_b128 v[96:99], %[vw] offset:45056\n" \
  "s_waitcnt lgkmcnt(8)\n" \
  "v_mfma_f32_16x16x32_bf16 %[c2], v[104:107], v[72:75], %[c2]\n" \
  "v_add_f32 v145, v145, v147\n" \
  "ds_read_b128 v[100:103], %[vw] offset:46080\n" \
  "s_waitcnt lgkmcnt(8)\n" \
  "v_mfma_f32_16x16x32_bf16 %[c3], v[108:111], v[72:75], %[c3]\n" \
  "v_add_f32 v149, v149, v151\n" \
  "ds_read_b128 v[104:107], %[vw] offset:47104\n" \
  "s_waitcnt lgkmcnt(8)\n" \
  "v_mfma_f32_16x16x32_bf16 %[c4], v[112:115], v[72:75], %[c4]\n" \
  "v_add_f32 v144, v144, v148\n" \
  "s_waitcnt vmcnt(5)\n" \
  "LG_ca3%=:\n" \
  "v_max3_u32 v180, v76, v77, v78\n" \
  "v_max3_u32 v180, v180, v79, v79\n" \
  "v_cmp_eq_u32 vcc, -1, v180\n" \
  "s_cbranch_vccnz LG_ra3%=\n" \
  "LG_ba3%=:\n" \
  "ds_read_b128 v[108:111], %[vw] offset:48128\n" \
  "s_waitcnt lgkmcnt(8)\n" \
  "v_mfma_f32_16x16x32_bf16 %[c0], v[116:119], v[76:79], %[c0]\n" \
  "v_add_f32 v145, v145, v149\n" \
  "buffer_load_dwordx4 v[68:71], %[va0], %[rs], %[sa] offen offset:1024 sc1\n" \
  "ds_read_b128 v[112:115], %[vw] offset:49152\n" \
  "s_waitcnt lgkmcnt(8)\n" \
  "v_mfma_f32_16x16x32_bf16 %[c1], v[120:123], v[76:79], %[c1]\n" \
  "v_mov_b32 v146, v144\n" \
  "v_add_u32 %[vw], 4294868992, %[vw]\n" \
  "ds_read_b128 v[116:119], %[vw] offset:0\n" \
  "s_waitcnt lgkmcnt(8)\n" \
  "v_mfma_f32_16x16x32_bf16 %[c2], v[124:127], v[76:79], %[c2]\n" \
  "v_mov_b32 v147, v145\n" \
  "ds_read_b128 v[120:123], %[vw] offset:1024\n" \
  "s_waitcnt lgkmcnt(8)\n" \
  "v_mfma_f32_16x16x32_bf16 %[c3], v[128:131], v[76:79], %[c3]\n" \
  "v_permlane16_swap_b32 v144, v146\n" \
  "ds_read_b128 v[124:127], %[vw] offset:2048\n" \
  "s_waitcnt lgkmcnt(8)\n" \
  "v_mfma_f32_16x16x32_bf16 %[c4], v[132:135], v[76:79], %[c4]\n" \
  "v_permlane16_swap_b32 v145, v147\n" \
  "s_waitcnt vmcnt(5)\n" \
  "LG_ca4%=:\n" \
  "v_max3_u32 v180, v80, v81, v82\n" \
  "v_max3_u32 v180, v180, v83, v83\n" \
  "v_cmp_eq_u32 vcc, -1, v180\n" \
  "s_cbranch_vccnz LG_ra4%=\n" \
  "LG_ba4%=:\n" \
  "ds_read_b128 v[128:131], %[vw] offset:3072\n" \
  "s_waitcnt lgkmcnt(8)\n" \
  "v_mfma_f32_16x16x32_bf16 %[c0], v[96:99], v[80:83], %[c0]\n" \
  "v_add_f32 v144, v144, v146\n" \
  "ds_read_b128 v[132:135], %[vw] offset:4096\n" \
  "s_waitcnt lgkmcnt(8)\n" \
  "v_mfma_f32_16x16x32_bf16 %[c1], v[100:103], v[80:83], %[c1]\n" \
  "v_add_f32 v145, v145, v147\n" \
  "ds_read_b128 v[96:99], %[vw] offset:10240\n" \
  "s_waitcnt lgkmcnt(8)\n" \
  "v_mfma_f32_16x16x32_bf16 %[c2], v[104:107], v[80:83], %[c2]\n" \
  "v_mov_b32 v146, v144\n" \
  "ds_read_b128 v[100:103], %[vw] offset:11264\n" \
  "s_waitcnt lgkmcnt(8)\n" \
  "v_mfma_f32_16x16x32_bf16 %[c3], v[108:111], v[80:83], %[c3]\n" \
  "v_mov_b32 v147, v145\n" \
  "ds_read_b128 v[104:107], %[vw] offset:12288\n" \
  "s_waitcnt lgkmcnt(8)\n" \
  "v_mfma_f32_16x16x32_bf16 %[c4], v[112:115], v[80:83], %[c4]\n" \
  "v_permlane32_swap_b32 v144, v146\n" \
  "s_waitcnt vmcnt(4)\n" \
  "LG_ca5%=:\n" \
  "v_max3_u32 v180, v84, v85, v86\n" \
  "v_max3_u32 v180, v180, v87, v87\n" \
  "v_cmp_eq_u32 vcc, -1, v180\n" \
  "s_cbranch_vccnz LG_ra5%=\n" \
  "LG_ba5%=:\n" \
  "ds_read_b128 v[108:111], %[vw] offset:13312\n" \
  "s_waitcnt lgkmcnt(8)\n" \
  "v_mfma_f32_16x16x32_bf16 %[c0], v[116:119], v[84:87], %[c0]\n" \
  "v_permlane32_swap_b32 v145, v147\n" \
  "ds_read_b128 v[112:115], %[vw] offset:14336\n" \
  "s_waitcnt lgkmcnt(8)\n" \
  "v_mfma_f32_16x16x32_bf16 %[c1], v[120:123], v[84:87], %[c1]\n" \
  "v_add_f32 v144, v144, v146\n" \
  "ds_read_b128 v[116:119], %[vw] offset:20480\n" \
  "s_waitcnt lgkmcnt(8)\n" \
  "v_mfma_f32_16x16x32_bf16 %[c2], v[124:127], v[84:87], %[c2]\n" \
  "v_add_f32 v145, v145, v147\n" \
  "ds_read_b128 v[120:123], %[vw] offset:21504\n" \
  "s_waitcnt lgkmcnt(8)\n" \
  "v_mfma_f32_16x16x32_bf16 %[c3], v[128:131], v[84:87], %[c3]\n" \
  "v_mul_f32 v152, 0x3acccccd, v144\n" \
  "ds_read_b128 v[124:127], %[vw] offset:22528\n" \
  "s_waitcnt lgkmcnt(8)\n" \
  "v_mfma_f32_16x16x32_bf16 %[c4], v[132:135], v[84:87], %[c4]\n" \
  "v_mul_f32 v145, 0x3acccccd, v145\n" \
  "s_waitcnt vmcnt(0)\n" \
  "s_barrier\n" \
  "LG_ca6%=:\n" \
  "v_max3_u32 v180, v88, v89, v90\n" \
  "v_max3_u32 v180, v180, v91, v91\n" \
  "v_cmp_eq_u32 vcc, -1, v180\n" \
  "s_cbranch_vccnz LG_ra6%=\n" \
  "LG_ba6%=:\n" \
  "ds_read_b128 v[128:131], %[vw] offset:23552\n" \
  "s_waitcnt lgkmcnt(8)\n" \
  "v_mfma_f32_16x16x32_bf16 %[c0], v[96:99], v[88:91], %[c0]\n" \
  "v_fma_f32 v145, -v152, v152, v145\n" \
  "ds_read_b128 v[132:135], %[vw] offset:24576\n" \
  "s_waitcnt lgkmcnt(8)\n" \
  "v_mfma_f32_16x16x32_bf16 %[c1], v[100:103], v[88:91], %[c1]\n" \
  "v_max_f32 v145, 0, v145\n" \
  "s_cmp_lg_u32 %[kh], 0\n" \
  "s_cbranch_scc1 LG_o32_0_0%=\n" \
  "s_mov_b32 m0, %[dm0]\n" \
  "s_nop 0\n" \
  "buffer_load_dwordx4 %[l16], %[rs], %[ds0] offen offset:0 sc1 lds\n" \
  "LG_o32_0_0%=:\n" \
  "ds_read_b128 v[96:99], %[vw] offset:30720\n" \
  "s_waitcnt lgkmcnt(8)\n" \
  "v_mfma_f32_16x16x32_bf16 %[c2], v[104:107], v[88:91], %[c2]\n" \
  "v_add_f32 v146, 0x3727c5ac, v145\n" \
  "ds_read_b128 v[100:103], %[vw] offset:31744\n" \
  "s_waitcnt lgkmcnt(8)\n" \
  "v_mfma_f32_16x16x32_bf16 %[c3], v[108:111], v[88:91], %[c3]\n" \
  "v_rsq_f32 v153, v146\n" \
  "ds_read_b128 v[104:107], %[vw] offset:32768\n" \
  "s_waitcnt lgkmcnt(8)\n" \
  "v_mfma_f32_16x16x32_bf16 %[c4], v[112:115], v[88:91], %[c4]\n" \
  "v_mul_f32 v145, v145, v153\n" \
  "LG_ca7%=:\n" \
  "v_max3_u32 v180, v92, v93, v94\n" \
  "v_max3_u32 v180, v180, v95, v95\n" \
  "v_cmp_eq_u32 vcc, -1, v180\n" \
  "s_cbranch_vccnz LG_ra7%=\n" \
  "LG_ba7%=:\n" \
  "ds_read_b128 v[108:111], %[vw] offset:33792\n" \
  "s_waitcnt lgkmcnt(8)\n" \
  "v_mfma_f32_16x16x32_bf16 %[c0], v[116:119], v[92:95], %[c0]\n" \
  "v_fmaak_f32 v145, v145, v153, 0x3727c5ac\n" \
  "s_cmp_lg_u32 %[kh], 0\n" \
  "s_cbranch_scc1 LG_o36_0_1%=\n" \
  "s_mov_b32 m0, %[dm0]\n" \
  "s_nop 0\n" \
  "buffer_load_dwordx4 %[l16], %[rs], %[ds0] offen offset:1024 sc1 lds\n" \
  "LG_o36_0_1%=:\n" \
  "ds_read_b128 v[112:115], %[vw] offset:34816\n" \
  "s_waitcnt lgkmcnt(8)\n" \
  "v_mfma_f32_16x16x32_bf16 %[c1], v[120:123], v[92:95], %[c1]\n" \
  "v_rsq_f32 v145, v145\n" \
  "ds_read_b128 v[116:119], %[vw] offset:40960\n" \
  "s_waitcnt lgkmcnt(8)\n" \
  "v_mfma_f32_16x16x32_bf16 %[c2], v[124:127], v[92:95], %[c2]\n" \
  "ds_read_b128 v[120:123], %[vw] offset:41984\n" \
  "s_waitcnt lgkmcnt(8)\n" \
  "v_mfma_f32_16x16x32_bf16 %[c3], v[128:131], v[92:95], %[c3]\n" \
  "v_mul_f32 v154, v153, v145\n" \
  "ds_read_b128 v[124:127], %[vw] offset:43008\n" \
  "s_waitcnt lgkmcnt(8)\n" \
  "v_mfma_f32_16x16x32_bf16 %[c4], v[132:135], v[92:95], %[c4]\n" \
  "v_mul_f32 v155, -v152, v154\n" \
  "s_cmp_lg_u32 %[kh], 0\n" \
  "s_cbranch_scc1 LG_o40_0_2%=\n" \
  "s_mov_b32 m0, %[dm0]\n" \
  "s_nop 0\n" \
  "buffer_load_dwordx4 %[l16], %[rs], %[ds0] offen offset:2048 sc1 lds\n" \
  "LG_o40_0_2%=:\n" \
  "LG_ca8%=:\n" \
  "v_max3_u32 v180, v64, v65, v66\n" \
  "v_max3_u32 v180, v180, v67, v67\n" \
  "v_cmp_eq_u32 vcc, -1, v180\n" \
  "s_cbranch_vccnz LG_ra8%=\n" \
  "LG_ba8%=:\n" \
  "ds_read_b128 v[128:131], %[vw] offset:44032\n" \
  "s_waitcnt lgkmcnt(8)\n" \
  "v_mfma_f32_16x16x32_bf16 %[c0], v[96:99], v[64:67], %[c0]\n" \
  "v_mul_f32 v156, v155, v156\n" \
  "v_mul_f32 v157, v155, v157\n" \
  "ds_read_b128 v[132:135], %[vw] offset:45056\n" \
  "s_waitcnt lgkmcnt(8)\n" \
  "v_mfma_f32_16x16x32_bf16 %[c1], v[100:103], v[64:67], %[c1]\n" \
  "v_mul_f32 v158, v155, v158\n" \
  "v_mul_f32 v159, v155, v159\n" \
  "s_waitcnt lgkmcnt(7)\n" \
  "v_mfma_f32_16x16x32_bf16 %[c2], v[104:107], v[64:67], %[c2]\n" \
  "v_mul_f32 v160, v155, v160\n" \
  "s_waitcnt lgkmcnt(6)\n" \
  "v_mfma_f32_16x16x32_bf16 %[c3], v[108:111], v[64:67], %[c3]\n" \
  "v_mul_f32 v161, v155, v161\n" \
  "s_cmp_lg_u32 %[kh], 0\n" \
  "s_cbranch_scc1 LG_o44_0_3%=\n" \
  "s_mov_b32 m0, %[dm0]\n" \
  "s_nop 0\n" \
  "buffer_load_dwordx4 %[l16], %[rs], %[ds0] offen offset:3072 sc1 lds\n" \
  "LG_o44_0_3%=:\n" \
  "s_waitcnt lgkmcnt(5)\n" \
  "v_mfma_f32_16x16x32_bf16 %[c4], v[112:115], v[64:67], %[c4]\n" \
  "v_mul_f32 v162, v155, v162\n" \
  "LG_ca9%=:\n" \
  "v_max3_u32 v180, v68, v69, v70\n" \
  "v_max3_u32 v180, v180, v71, v71\n" \
  "v_cmp_eq_u32 vcc, -1, v180\n" \
  "s_cbranch_vccnz LG_ra9%=\n" \
  "LG_ba9%=:\n" \
  "s_waitcnt lgkmcnt(4)\n" \
  "v_mfma_f32_16x16x32_bf16 %[c0], v[116:119], v[68:71], %[c0]\n" \
  "v_mul_f32 v163, v155, v163\n" \
  "s_waitcnt lgkmcnt(3)\n" \
  "v_mfma_f32_16x16x32_bf16 %[c1], v[120:123], v[68:71], %[c1]\n" \
  "v_mul_f32 v164, v155, v164\n" \
  "s_waitcnt lgkmcnt(2)\n" \
  "v_mfma_f32_16x16x32_bf16 %[c2], v[124:127], v[68:71], %[c2]\n" \
  "v_mul_f32 v165, v155, v165\n" \
  "s_cmp_lg_u32 %[kh], 0\n" \
  "s_cbranch_scc1 LG_o48_0_4%=\n" \
  "s_add_u32 %[dm0], %[dm0], 4096\n" \
  "s_add_u32 %[ds0], %[ds0], 4096\n" \
  "s_mov_b32 m0, %[dm0]\n" \
  "s_nop 0\n" \
  "buffer_load_dwordx4 %[l16], %[rs], %[ds0] offen offset:0 sc1 lds\n" \
  "LG_o48_0_4%=:\n" \
  "s_waitcnt lgkmcnt(1)\n" \
  "v_mfma_f32_16x16x32_bf16 %[c3], v[128:131], v[68:71], %[c3]\n" \
  "v_mul_f32 v166, v155, v166\n" \
  "s_waitcnt lgkmcnt(0)\n" \
  "v_mfma_f32_16x16x32_bf16 %[c4], v[132:135], v[68:71], %[c4]\n" \
  "v_mul_f32 v167, v155, v167\n" \
  "s_cmp_lg_u32 %[kh], 0\n" \
  "s_cbranch_scc1 LG_e0_5%=\n" \
  "s_mov_b32 m0, %[dm0]\n" \
  "s_nop 0\n" \
  "buffer_load_dwordx4 %[l16], %[rs], %[ds0] offen offset:1024 sc1 lds\n" \
  "buffer_load_dwordx4 %[l16], %[rs], %[ds0] offen offset:2048 sc1 lds\n" \
  "buffer_load_dwordx4 %[l16], %[rs], %[ds0] offen offset:3072 sc1 lds\n" \
  "s_add_u32 %[dm0], %[dm0], 4096\n" \
  "s_add_u32 %[ds0], %[ds0], 4096\n" \
  "s_mov_b32 m0, %[dm0]\n" \
  "s_nop 0\n" \
  "buffer_load_dwordx4 %[l16], %[rs], %[ds0] offen offset:0 sc1 lds\n" \
  "buffer_load_dwordx4 %[l16], %[rs], %[ds0] offen offset:1024 sc1 lds\n" \
  "LG_e0_5%=:\n" \
  "s_branch LG_end%=\n" \
  "LG_ra0%=:\n" \
  "s_sub_u32 s89, s89, 1\n" \
  "s_cbranch_scc1 LG_xa0%=\n" \
  "s_add_u32 s87, s88, 0\n" \
  "buffer_load_dwordx4 v[64:67], %[va0], %[rs], s87 offen sc1\n" \
  "s_waitcnt vmcnt(0)\n" \
  "s_branch LG_ca0%=\n" \
  "LG_xa0%=:\n" \
  "s_mov_b32 s89, 0\n" \
  "s_branch LG_ba0%=\n" \
  "LG_ra1%=:\n" \
  "s_sub_u32 s89, s89, 1\n" \
  "s_cbranch_scc1 LG_xa1%=\n" \
  "s_add_u32 s87, s88, 1024\n" \
  "buffer_load_dwordx4 v[68:71], %[va0], %[rs], s87 offen sc1\n" \
  "s_waitcnt vmcnt(0)\n" \
  "s_branch LG_ca1%=\n" \
  "LG_xa1%=:\n" \
  "s_mov_b32 s89, 0\n" \
  "s_branch LG_ba1%=\n" \
  "LG_ra2%=:\n" \
  "s_sub_u32 s89, s89, 1\n" \
  "s_cbranch_scc1 LG_xa2%=\n" \
  "s_add_u32 s87, s88, 2048\n" \
  "buffer_load_dwordx4 v[72:75], %[va0], %[rs], s87 offen sc1\n" \
  "s_waitcnt vmcnt(0)\n" \
  "s_branch LG_ca2%=\n" \
  "LG_xa2%=:\n" \
  "s_mov_b32 s89, 0\n" \
  "s_branch LG_ba2%=\n" \
  "LG_rs%=:\n" \
  "s_sub_u32 s89, s89, 1\n" \
  "s_cbranch_scc1 LG_xs%=\n" \
  "buffer_load_dwordx4 v[144:147], %[vst], %[rs], 0 offen offset:0 sc1\n" \
  "buffer_load_dwordx4 v[148:151], %[vst], %[rs], 0 offen offset:256 sc1\n" \
  "s_waitcnt vmcnt(0)\n" \
  "s_branch LG_cs%=\n" \
  "LG_xs%=:\n" \
  "s_mov_b32 s89, 0\n" \
  "s_branch LG_bs%=\n" \
  "LG_ra3%=:\n" \
  "s_sub_u32 s89, s89, 1\n" \
  "s_cbranch_scc1 LG_xa3%=\n" \
  "s_add_u32 s87, s88, 3072\n" \
  "buffer_load_dwordx4 v[76:79], %[va0], %[rs], s87 offen sc1\n" \
  "s_waitcnt vmcnt(0)\n" \
  "s_branch LG_ca3%=\n" \
  "LG_xa3%=:\n" \
  "s_mov_b32 s89, 0\n" \
  "s_branch LG_ba3%=\n" \
  "LG_ra4%=:\n" \
  "s_sub_u32 s89, s89, 1\n" \
  "s_cbranch_scc1 LG_xa4%=\n" \
  "s_add_u32 s87, s88, 4096\n" \
  "buffer_load_dwordx4 v[80:83], %[va0], %[rs], s87 offen sc1\n" \
  "s_waitcnt vmcnt(0)\n" \
  "s_branch LG_ca4%=\n" \
  "LG_xa4%=:\n" \
  "s_mov_b32 s89, 0\n" \
  "s_branch LG_ba4%=\n" \
  "LG_ra5%=:\n" \
  "s_sub_u32 s89, s89, 1\n" \
  "s_cbranch_scc1 LG_xa5%=\n" \
  "s_add_u32 s87, s88, 5120\n" \
  "buffer_load_dwordx4 v[84:87], %[va0], %[rs], s87 offen sc1\n" \
  "s_waitcnt vmcnt(0)\n" \
  "s_branch LG_ca5%=\n" \
  "LG_xa5%=:\n" \
  "s_mov_b32 s89, 0\n" \
  "s_branch LG_ba5%=\n" \
  "LG_ra6%=:\n" \
  "s_sub_u32 s89, s89, 1\n" \
  "s_cbranch_scc1 LG_xa6%=\n" \
  "s_add_u32 s87, s88, 6144\n" \
  "buffer_load_dwordx4 v[88:91], %[va0], %[rs], s87 offen sc1\n" \
  "s_waitcnt vmcnt(0)\n" \
  "s_branch LG_ca6%=\n" \
  "LG_xa6%=:\n" \
  "s_mov_b32 s89, 0\n" \
  "s_branch LG_ba6%=\n" \
  "LG_ra7%=:\n" \
  "s_sub_u32 s89, s89, 1\n" \
  "s_cbranch_scc1 LG_xa7%=\n" \
  "s_add_u32 s87, s88, 7168\n" \
  "buffer_load_dwordx4 v[92:95], %[va0], %[rs], s87 offen sc1\n" \
  "s_waitcnt vmcnt(0)\n" \
  "s_branch LG_ca7%=\n" \
  "LG_xa7%=:\n" \
  "s_mov_b32 s89, 0\n" \
  "s_branch LG_ba7%=\n" \
  "LG_ra8%=:\n" \
  "s_sub_u32 s89, s89, 1\n" \
  "s_cbranch_scc1 LG_xa8%=\n" \
  "s_add_u32 s87, s88, 8192\n" \
  "buffer_load_dwordx4 v[64:67], %[va0], %[rs], s87 offen sc1\n" \
  "s_waitcnt vmcnt(0)\n" \
  "s_branch LG_ca8%=\n" \
  "LG_xa8%=:\n" \
  "s_mov_b32 s89, 0\n" \
  "s_branch LG_ba8%=\n" \
  "LG_ra9%=:\n" \
  "s_sub_u32 s89, s89, 1\n" \
  "s_cbranch_scc1 LG_xa9%=\n" \
  "s_add_u32 s87, s88, 9216\n" \
  "buffer_load_dwordx4 v[68:71], %[va0], %[rs], s87 offen sc1\n" \
  "s_waitcnt vmcnt(0)\n" \
  "s_branch LG_ca9%=\n" \
  "LG_xa9%=:\n" \
  "s_mov_b32 s89, 0\n" \
  "s_branch LG_ba9%=\n" \
  "LG_end%=:\n" \
  "s_nop 7\n" \
  ""
#define P5_ASM \
  "s_mov_b32 s88, %[sa]\n" \
  "s_mov_b32 s89, 0x4000\n" \
  "buffer_load_dwordx4 v[64:67], %[va0], %[rs], %[sa] offen offset:0 sc1\n" \
  "buffer_load_dwordx4 v[68:71], %[va0], %[rs], %[sa] offen offset:1024 sc1\n" \
  "buffer_load_dwordx4 v[72:75], %[va0], %[rs], %[sa] offen offset:2048 sc1\n" \
  "buffer_load_dwordx4 v[76:79], %[va0], %[rs], %[sa] offen offset:3072 sc1\n" \
  "s_add_u32 %[sa], %[sa], 4096\n" \
  "buffer_load_dwordx4 v[80:83], %[va0], %[rs], %[sa] offen offset:0 sc1\n" \
  "buffer_load_dwordx4 v[84:87], %[va1], %[rs], %[sa] offen offset:1024 sc1\n" \
  "ds_read_b128 v[96:99], %[vw] offset:51200\n" \
  "ds_read_b128 v[100:103], %[vw] offset:52224\n" \
  "ds_read_b128 v[104:107], %[vw] offset:53248\n" \
  "ds_read_b128 v[108:111], %[vw] offset:54272\n" \
  "ds_read_b128 v[112:115], %[vw] offset:55296\n" \
  "ds_read_b128 v[116:119], %[vw] offset:61440\n" \
  "ds_read_b128 v[120:123], %[vw] offset:62464\n" \
  "ds_read_b128 v[124:127], %[vw] offset:63488\n" \
  "s_waitcnt vmcnt(5)\n" \
  "LG_ca0%=:\n" \
  "v_max3_u32 v180, v64, v65, v66\n" \
  "v_max3_u32 v180, v180, v67, v67\n" \
  "v_cmp_eq_u32 vcc, -1, v180\n" \
  "s_cbranch_vccnz LG_ra0%=\n" \
  "LG_ba0%=:\n" \
  "ds_read_b128 v[128:131], %[vw] offset:64512\n" \
  "s_waitcnt lgkmcnt(8)\n" \
  "v_mfma_f32_16x16x32_bf16 %[c0], v[96:99], v[64:67], %[c0]\n" \
  "buffer_load_dwordx4 v[88:91], %[va1], %[rs], %[sa] offen offset:2048 sc1\n" \
  "v_add_u32 %[vw], 65536, %[vw]\n" \
  "ds_read_b128 v[132:135], %[vw] offset:0\n" \
  "s_waitcnt lgkmcnt(8)\n" \
  "v_mfma_f32_16x16x32_bf16 %[c1], v[100:103], v[64:67], %[c1]\n" \
  "ds_read_b128 v[96:99], %[vw] offset:6144\n" \
  "s_waitcnt lgkmcnt(8)\n" \
  "v_mfma_f32_16x16x32_bf16 %[c2], v[104:107], v[64:67], 0\n" \
  "ds_read_b128 v[100:103], %[vw] offset:7168\n" \
  "s_waitcnt lgkmcnt(8)\n" \
  "v_mfma_f32_16x16x32_bf16 %[c3], v[108:111], v[64:67], 0\n" \
  "ds_read_b128 v[104:107], %[vw] offset:8192\n" \
  "s_waitcnt lgkmcnt(8)\n" \
  "v_mfma_f32_16x16x32_bf16 %[c4], v[112:115], v[64:67], %[c4]\n" \
  "s_waitcnt vmcnt(5)\n" \
  "LG_ca1%=:\n" \
  "v_max3_u32 v180, v68, v69, v70\n" \
  "v_max3_u32 v180, v180, v71, v71\n" \
  "v_cmp_eq_u32 vcc, -1, v180\n" \
  "s_cbranch_vccnz LG_ra1%=\n" \
  "LG_ba1%=:\n" \
  "ds_read_b128 v[108:111], %[vw] offset:9216\n" \
  "s_waitcnt lgkmcnt(8)\n" \
  "v_mfma_f32_16x16x32_bf16 %[c0], v[116:119], v[68:71], %[c0]\n" \
  "buffer_load_dwordx4 v[92:95], %[va1], %[rs], %[sa] offen offset:3072 sc1\n" \
  "ds_read_b128 v[112:115], %[vw] offset:10240\n" \
  "s_waitcnt lgkmcnt(8)\n" \
  "v_mfma_f32_16x16x32_bf16 %[c1], v[120:123], v[68:71], %[c1]\n" \
  "ds_read_b128 v[116:119], %[vw] offset:16384\n" \
  "s_waitcnt lgkmcnt(8)\n" \
  "v_mfma_f32_16x16x32_bf16 %[c2], v[124:127], v[68:71], %[c2]\n" \
  "ds_read_b128 v[120:123], %[vw] offset:17408\n" \
  "s_waitcnt lgkmcnt(8)\n" \
  "v_mfma_f32_16x16x32_bf16 %[c3], v[128:131], v[68:71], %[c3]\n" \
  "ds_read_b128 v[124:127], %[vw] offset:18432\n" \
  "s_waitcnt lgkmcnt(8)\n" \
  "v_mfma_f32_16x16x32_bf16 %[c4], v[132:135], v[68:71], %[c4]\n" \
  "s_waitcnt vmcnt(5)\n" \
  "LG_ca2%=:\n" \
  "v_max3_u32 v180, v72, v73, v74\n" \
  "v_max3_u32 v180, v180, v75, v75\n" \
  "v_cmp_eq_u32 vcc, -1, v180\n" \
  "s_cbranch_vccnz LG_ra2%=\n" \
  "LG_ba2%=:\n" \
  "ds_read_b128 v[128:131], %[vw] offset:19456\n" \
  "s_waitcnt lgkmcnt(8)\n" \
  "v_mfma_f32_16x16x32_bf16 %[c0], v[96:99], v[72:75], %[c0]\n" \
  "s_add_u32 %[sa], %[sa], 4096\n" \
  "buffer_load_dwordx4 v[64:67], %[va1], %[rs], %[sa] offen offset:0 sc1\n" \
  "ds_read_b128 v[132:135], %[vw] offset:20480\n" \
  "s_waitcnt lgkmcnt(8)\n" \
  "v_mfma_f32_16x16x32_bf16 %[c1], v[100:103], v[72:75], %[c1]\n" \
  "ds_read_b128 v[96:99], %[vw] offset:26624\n" \
  "s_waitcnt lgkmcnt(8)\n" \
  "v_mfma_f32_16x16x32_bf16 %[c2], v[104:107], v[72:75], %[c2]\n" \
  "ds_read_b128 v[100:103], %[vw] offset:27648\n" \
  "s_waitcnt lgkmcnt(8)\n" \
  "v_mfma_f32_16x16x32_bf16 %[c3], v[108:111], v[72:75], %[c3]\n" \
  "ds_read_b128 v[104:107], %[vw] offset:28672\n" \
  "s_waitcnt lgkmcnt(8)\n" \
  "v_mfma_f32_16x16x32_bf16 %[c4], v[112:115], v[72:75], %[c4]\n" \
  "s_waitcnt vmcnt(5)\n" \
  "LG_ca3%=:\n" \
  "v_max3_u32 v180, v76, v77, v78\n" \
  "v_max3_u32 v180, v180, v79, v79\n" \
  "v_cmp_eq_u32 vcc, -1, v180\n" \
  "s_cbranch_vccnz LG_ra3%=\n" \
  "LG_ba3%=:\n" \
  "ds_read_b128 v[108:111], %[vw] offset:29696\n" \
  "s_waitcnt lgkmcnt(8)\n" \
  "v_mfma_f32_16x16x32_bf16 %[c0], v[116:119], v[76:79], %[c0]\n" \
  "buffer_load_dwordx4 v[68:71], %[va1], %[rs], %[sa] offen offset:1024 sc1\n" \
  "ds_read_b128 v[112:115], %[vw] offset:30720\n" \
  "s_waitcnt lgkmcnt(8)\n" \
  "v_mfma_f32_16x16x32_bf16 %[c1], v[120:123], v[76:79], %[c1]\n" \
  "ds_read_b128 v[116:119], %[vw] offset:36864\n" \
  "s_waitcnt lgkmcnt(8)\n" \
  "v_mfma_f32_16x16x32_bf16 %[c2], v[124:127], v[76:79], %[c2]\n" \
  "ds_read_b128 v[120:123], %[vw] offset:37888\n" \
  "s_waitcnt lgkmcnt(8)\n" \
  "v_mfma_f32_16x16x32_bf16 %[c3], v[128:131], v[76:79], %[c3]\n" \
  "ds_read_b128 v[124:127], %[vw] offset:38912\n" \
  "s_waitcnt lgkmcnt(8)\n" \
  "v_mfma_f32_16x16x32_bf16 %[c4], v[132:135], v[76:79], %[c4]\n" \
  "s_waitcnt vmcnt(5)\n" \
  "LG_ca4%=:\n" \
  "v_max3_u32 v180, v80, v81, v82\n" \
  "v_max3_u32 v180, v180, v83, v83\n" \
  "v_cmp_eq_u32 vcc, -1, v180\n" \
  "s_cbranch_vccnz LG_ra4%=\n" \
  "LG_ba4%=:\n" \
  "ds_read_b128 v[128:131], %[vw] offset:39936\n" \
  "s_waitcnt lgkmcnt(8)\n" \
  "v_mfma_f32_16x16x32_bf16 %[c0], v[96:99], v[80:83], %[c0]\n" \
  "ds_read_b128 v[132:135], %[vw] offset:40960\n" \
  "s_waitcnt lgkmcnt(8)\n" \
  "v_mfma_f32_16x16x32_bf16 %[c1], v[100:103], v[80:83], %[c1]\n" \
  "ds_read_b128 v[96:99], %[vw] offset:47104\n" \
  "s_waitcnt lgkmcnt(8)\n" \
  "v_mfma_f32_16x16x32_bf16 %[c2], v[104:107], v[80:83], %[c2]\n" \
  "ds_read_b128 v[100:103], %[vw] offset:48128\n" \
  "s_waitcnt lgkmcnt(8)\n" \
  "v_mfma_f32_16x16x32_bf16 %[c3], v[108:111], v[80:83], %[c3]\n" \
  "ds_read_b128 v[104:107], %[vw] offset:49152\n" \
  "s_waitcnt lgkmcnt(8)\n" \
  "v_mfma_f32_16x16x32_bf16 %[c4], v[112:115], v[80:83], %[c4]\n" \
  "s_waitcnt vmcnt(4)\n" \
  "LG_ca5%=:\n" \
  "v_max3_u32 v180, v84, v85, v86\n" \
  "v_max3_u32 v180, v180, v87, v87\n" \
  "v_cmp_eq_u32 vcc, -1, v180\n" \
  "s_cbranch_vccnz LG_ra5%=\n" \
  "LG_ba5%=:\n" \
  "ds_read_b128 v[108:111], %[vw] offset:50176\n" \
  "s_waitcnt lgkmcnt(8)\n" \
  "v_mfma_f32_16x16x32_bf16 %[c0], v[116:119], v[84:87], %[c0]\n" \
  "ds_read_b128 v[112:115], %[vw] offset:51200\n" \
  "s_waitcnt lgkmcnt(8)\n" \
  "v_mfma_f32_16x16x32_bf16 %[c1], v[120:123], v[84:87], %[c1]\n" \
  "ds_read_b128 v[116:119], %[vw] offset:57344\n" \
  "s_waitcnt lgkmcnt(8)\n" \
  "v_mfma_f32_16x16x32_bf16 %[c2], v[124:127], v[84:87], %[c2]\n" \
  "ds_read_b128 v[120:123], %[vw] offset:58368\n" \
  "s_waitcnt lgkmcnt(8)\n" \
  "v_mfma_f32_16x16x32_bf16 %[c3], v[128:131], v[84:87], %[c3]\n" \
  "ds_read_b128 v[124:127], %[vw] offset:59392\n" \
  "s_waitcnt lgkmcnt(8)\n" \
  "v_mfma_f32_16x16x32_bf16 %[c4], v[132:135], v[84:87], %[c4]\n" \
  "s_waitcnt vmcnt(0)\n" \
  "s_barrier\n" \
  "LG_ca6%=:\n" \
  "v_max3_u32 v180, v88, v89, v90\n" \
  "v_max3_u32 v180, v180, v91, v91\n" \
  "v_cmp_eq_u32 vcc, -1, v180\n" \
  "s_cbranch_vccnz LG_ra6%=\n" \
  "LG_ba6%=:\n" \
  "ds_read_b128 v[128:131], %[vw] offset:60416\n" \
  "s_waitcnt lgkmcnt(8)\n" \
  "v_mfma_f32_16x16x32_bf16 %[c0], v[96:99], v[88:91], %[c0]\n" \
  "ds_read_b128 v[132:135], %[vw] offset:61440\n" \
  "s_waitcnt lgkmcnt(8)\n" \
  "v_mfma_f32_16x16x32_bf16 %[c1], v[100:103], v[88:91], %[c1]\n" \
  "s_cmp_lg_u32 %[kh], 0\n" \
  "s_cbranch_scc1 LG_o32_0_0%=\n" \
  "s_mov_b32 m0, %[dm0]\n" \
  "s_nop 0\n" \
  "buffer_load_dwordx4 %[l16], %[rs], %[ds0] offen offset:0 sc1 lds\n" \
  "LG_o32_0_0%=:\n" \
  "v_add_u32 %[vw], 65536, %[vw]\n" \
  "ds_read_b128 v[96:99], %[vw] offset:2048\n" \
  "s_waitcnt lgkmcnt(8)\n" \
  "v_mfma_f32_16x16x32_bf16 %[c2], v[104:107], v[88:91], %[c2]\n" \
  "ds_read_b128 v[100:103], %[vw] offset:3072\n" \
  "s_waitcnt lgkmcnt(8)\n" \
  "v_mfma_f32_16x16x32_bf16 %[c3], v[108:111], v[88:91], %[c3]\n" \
  "ds_read_b128 v[104:107], %[vw] offset:4096\n" \
  "s_waitcnt lgkmcnt(8)\n" \
  "v_mfma_f32_16x16x32_bf16 %[c4], v[112:115], v[88:91], %[c4]\n" \
  "LG_ca7%=:\n" \
  "v_max3_u32 v180, v92, v93, v94\n" \
  "v_max3_u32 v180, v180, v95, v95\n" \
  "v_cmp_eq_u32 vcc, -1, v180\n" \
  "s_cbranch_vccnz LG_ra7%=\n" \
  "LG_ba7%=:\n" \
  "ds_read_b128 v[108:111], %[vw] offset:5120\n" \
  "s_waitcnt lgkmcnt(8)\n" \
  "v_mfma_f32_16x16x32_bf16 %[c0], v[116:119], v[92:95], %[c0]\n" \
  "s_cmp_lg_u32 %[kh], 0\n" \
  "s_cbranch_scc1 LG_o36_0_1%=\n" \
  "s_mov_b32 m0, %[dm0]\n" \
  "s_nop 0\n" \
  "buffer_load_dwordx4 %[l16], %[rs], %[ds0] offen offset:1024 sc1 lds\n" \
  "LG_o36_0_1%=:\n" \
  "ds_read_b128 v[112:115], %[vw] offset:6144\n" \
  "s_waitcnt lgkmcnt(8)\n" \
  "v_mfma_f32_16x16x32_bf16 %[c1], v[120:123], v[92:95], %[c1]\n" \
  "ds_read_b128 v[116:119], %[vw] offset:12288\n" \
  "s_waitcnt lgkmcnt(8)\n" \
  "v_mfma_f32_16x16x32_bf16 %[c2], v[124:127], v[92:95], %[c2]\n" \
  "ds_read_b128 v[120:123], %[vw] offset:13312\n" \
  "s_waitcnt lgkmcnt(8)\n" \
  "v_mfma_f32_16x16x32_bf16 %[c3], v[128:131], v[92:95], %[c3]\n" \
  "ds_read_b128 v[124:127], %[vw] offset:14336\n" \
  "s_waitcnt lgkmcnt(8)\n" \
  "v_mfma_f32_16x16x32_bf16 %[c4], v[132:135], v[92:95], %[c4]\n" \
  "s_cmp_lg_u32 %[kh], 0\n" \
  "s_cbranch_scc1 LG_o40_0_2%=\n" \
  "s_mov_b32 m0, %[dm0]\n" \
  "s_nop 0\n" \
  "buffer_load_dwordx4 %[l16], %[rs], %[ds0] offen offset:2048 sc1 lds\n" \
  "LG_o40_0_2%=:\n" \
  "LG_ca8%=:\n" \
  "v_max3_u32 v180, v64, v65, v66\n" \
  "v_max3_u32 v180, v180, v67, v67\n" \
  "v_cmp_eq_u32 vcc, -1, v180\n" \
  "s_cbranch_vccnz LG_ra8%=\n" \
  "LG_ba8%=:\n" \
  "ds_read_b128 v[128:131], %[vw] offset:15360\n" \
  "s_waitcnt lgkmcnt(8)\n" \
  "v_mfma_f32_16x16x32_bf16 %[c0], v[96:99], v[64:67], %[c0]\n" \
  "ds_read_b128 v[132:135], %[vw] offset:16384\n" \
  "s_waitcnt lgkmcnt(8)\n" \
  "v_mfma_f32_16x16x32_bf16 %[c1], v[100:103], v[64:67], %[c1]\n" \
  "s_waitcnt lgkmcnt(7)\n" \
  "v_mfma_f32_16x16x32_bf16 %[c2], v[104:107], v[64:67], %[c2]\n" \
  "s_waitcnt lgkmcnt(6)\n" \
  "v_mfma_f32_16x16x32_bf16 %[c3], v[108:111], v[64:67], %[c3]\n" \
  "s_cmp_lg_u32 %[kh], 0\n" \
  "s_cbranch_scc1 LG_o44_0_3%=\n" \
  "s_mov_b32 m0, %[dm0]\n" \
  "s_nop 0\n" \
  "buffer_load_dwordx4 %[l16], %[rs], %[ds0] offen offset:3072 sc1 lds\n" \
  "LG_o44_0_3%=:\n" \
  "s_waitcnt lgkmcnt(5)\n" \
  "v_mfma_f32_16x16x32_bf16 %[c4], v[112:115], v[64:67], %[c4]\n" \
  "LG_ca9%=:\n" \
  "v_max3_u32 v180, v68, v69, v70\n" \
  "v_max3_u32 v180, v180, v71, v71\n" \
  "v_cmp_eq_u32 vcc, -1, v180\n" \
  "s_cbranch_vccnz LG_ra9%=\n" \
  "LG_ba9%=:\n" \
  "s_waitcnt lgkmcnt(4)\n" \
  "v_mfma_f32_16x16x32_bf16 %[c0], v[116:119], v[68:71], %[c0]\n" \
  "s_waitcnt lgkmcnt(3)\n" \
  "v_mfma_f32_16x16x32_bf16 %[c1], v[120:123], v[68:71], %[c1]\n" \
  "s_waitcnt lgkmcnt(2)\n" \
  "v_mfma_f32_16x16x32_bf16 %[c2], v[124:127], v[68:71], %[c2]\n" \
  "s_cmp_lg_u32 %[kh], 0\n" \
  "s_cbranch_scc1 LG_o48_0_4%=\n" \
  "s_add_u32 %[dm0], %[dm0], 4096\n" \
  "s_add_u32 %[ds0], %[ds0], 4096\n" \
  "s_mov_b32 m0, %[dm0]\n" \
  "s_nop 0\n" \
  "buffer_load_dwordx4 %[l16], %[rs], %[ds0] offen offset:0 sc1 lds\n" \
  "LG_o48_0_4%=:\n" \
  "s_waitcnt lgkmcnt(1)\n" \
  "v_mfma_f32_16x16x32_bf16 %[c3], v[128:131], v[68:71], %[c3]\n" \
  "s_waitcnt lgkmcnt(0)\n" \
  "v_mfma_f32_16x16x32_bf16 %[c4], v[132:135], v[68:71], %[c4]\n" \
  "s_cmp_lg_u32 %[kh], 0\n" \
  "s_cbranch_scc1 LG_e0_5%=\n" \
  "s_mov_b32 m0, %[dm0]\n" \
  "s_nop 0\n" \
  "buffer_load_dwordx4 %[l16], %[rs], %[ds0] offen offset:1024 sc1 lds\n" \
  "buffer_load_dwordx4 %[l16], %[rs], %[ds0] offen offset:2048 sc1 lds\n" \
  "buffer_load_dwordx4 %[l16], %[rs], %[ds0] offen offset:3072 sc1 lds\n" \
  "s_add_u32 %[dm0], %[dm0], 4096\n" \
  "s_add_u32 %[ds0], %[ds0], 4096\n" \
  "s_mov_b32 m0, %[dm0]\n" \
  "s_nop 0\n" \
  "buffer_load_dwordx4 %[l16], %[rs], %[ds0] offen offset:0 sc1 lds\n" \
  "buffer_load_dwordx4 %[l16], %[rs], %[ds0] offen offset:1024 sc1 lds\n" \
  "LG_e0_5%=:\n" \
  "s_branch LG_end%=\n" \
  "LG_ra0%=:\n" \
  "s_sub_u32 s89, s89, 1\n" \
  "s_cbranch_scc1 LG_xa0%=\n" \
  "s_add_u32 s87, s88, 0\n" \
  "buffer_load_dwordx4 v[64:67], %[va0], %[rs], s87 offen sc1\n" \
  "s_waitcnt vmcnt(0)\n" \
  "s_branch LG_ca0%=\n" \
  "LG_xa0%=:\n" \
  "s_mov_b32 s89, 0\n" \
  "s_branch LG_ba0%=\n" \
  "LG_ra1%=:\n" \
  "s_sub_u32 s89, s89, 1\n" \
  "s_cbranch_scc1 LG_xa1%=\n" \
  "s_add_u32 s87, s88, 1024\n" \
  "buffer_load_dwordx4 v[68:71], %[va0], %[rs], s87 offen sc1\n" \
  "s_waitcnt vmcnt(0)\n" \
  "s_branch LG_ca1%=\n" \
  "LG_xa1%=:\n" \
  "s_mov_b32 s89, 0\n" \
  "s_branch LG_ba1%=\n" \
  "LG_ra2%=:\n" \
  "s_sub_u32 s89, s89, 1\n" \
  "s_cbranch_scc1 LG_xa2%=\n" \
  "s_add_u32 s87, s88, 2048\n" \
  "buffer_load_dwordx4 v[72:75], %[va0], %[rs], s87 offen sc1\n" \
  "s_waitcnt vmcnt(0)\n" \
  "s_branch LG_ca2%=\n" \
  "LG_xa2%=:\n" \
  "s_mov_b32 s89, 0\n" \
  "s_branch LG_ba2%=\n" \
  "LG_ra3%=:\n" \
  "s_sub_u32 s89, s89, 1\n" \
  "s_cbranch_scc1 LG_xa3%=\n" \
  "s_add_u32 s87, s88, 3072\n" \
  "buffer_load_dwordx4 v[76:79], %[va0], %[rs], s87 offen sc1\n" \
  "s_waitcnt vmcnt(0)\n" \
  "s_branch LG_ca3%=\n" \
  "LG_xa3%=:\n" \
  "s_mov_b32 s89, 0\n" \
  "s_branch LG_ba3%=\n" \
  "LG_ra4%=:\n" \
  "s_sub_u32 s89, s89, 1\n" \
  "s_cbranch_scc1 LG_xa4%=\n" \
  "s_add_u32 s87, s88, 4096\n" \
  "buffer_load_dwordx4 v[80:83], %[va0], %[rs], s87 offen sc1\n" \
  "s_waitcnt vmcnt(0)\n" \
  "s_branch LG_ca4%=\n" \
  "LG_xa4%=:\n" \
  "s_mov_b32 s89, 0\n" \
  "s_branch LG_ba4%=\n" \
  "LG_ra5%=:\n" \
  "s_sub_u32 s89, s89, 1\n" \
  "s_cbranch_scc1 LG_xa5%=\n" \
  "s_add_u32 s87, s88, 5120\n" \
  "buffer_load_dwordx4 v[84:87], %[va1], %[rs], s87 offen sc1\n" \
  "s_waitcnt vmcnt(0)\n" \
  "s_branch LG_ca5%=\n" \
  "LG_xa5%=:\n" \
  "s_mov_b32 s89, 0\n" \
  "s_branch LG_ba5%=\n" \
  "LG_ra6%=:\n" \
  "s_sub_u32 s89, s89, 1\n" \
  "s_cbranch_scc1 LG_xa6%=\n" \
  "s_add_u32 s87, s88, 6144\n" \
  "buffer_load_dwordx4 v[88:91], %[va1], %[rs], s87 offen sc1\n" \
  "s_waitcnt vmcnt(0)\n" \
  "s_branch LG_ca6%=\n" \
  "LG_xa6%=:\n" \
  "s_mov_b32 s89, 0\n" \
  "s_branch LG_ba6%=\n" \
  "LG_ra7%=:\n" \
  "s_sub_u32 s89, s89, 1\n" \
  "s_cbranch_scc1 LG_xa7%=\n" \
  "s_add_u32 s87, s88, 7168\n" \
  "buffer_load_dwordx4 v[92:95], %[va1], %[rs], s87 offen sc1\n" \
  "s_waitcnt vmcnt(0)\n" \
  "s_branch LG_ca7%=\n" \
  "LG_xa7%=:\n" \
  "s_mov_b32 s89, 0\n" \
  "s_branch LG_ba7%=\n" \
  "LG_ra8%=:\n" \
  "s_sub_u32 s89, s89, 1\n" \
  "s_cbranch_scc1 LG_xa8%=\n" \
  "s_add_u32 s87, s88, 8192\n" \
  "buffer_load_dwordx4 v[64:67], %[va1], %[rs], s87 offen sc1\n" \
  "s_waitcnt vmcnt(0)\n" \
  "s_branch LG_ca8%=\n" \
  "LG_xa8%=:\n" \
  "s_mov_b32 s89, 0\n" \
  "s_branch LG_ba8%=\n" \
  "LG_ra9%=:\n" \
  "s_sub_u32 s89, s89, 1\n" \
  "s_cbranch_scc1 LG_xa9%=\n" \
  "s_add_u32 s87, s88, 9216\n" \
  "buffer_load_dwordx4 v[68:71], %[va1], %[rs], s87 offen sc1\n" \
  "s_waitcnt vmcnt(0)\n" \
  "s_branch LG_ca9%=\n" \
  "LG_xa9%=:\n" \
  "s_mov_b32 s89, 0\n" \
  "s_branch LG_ba9%=\n" \
  "LG_end%=:\n" \
  "s_nop 7\n" \
  ""
#define PS_CLOB "v64", "v65", "v66", "v67", "v68", "v69", "v70", "v71", "v72", "v73", "v74", "v75", "v76", "v77", "v78", "v79", "v80", "v81", "v82", "v83", "v84", "v85", "v86", "v87", "v88", "v89", "v90", "v91", "v92", "v93", "v94", "v95", "v96", "v97", "v98", "v99", "v100", "v101", "v102", "v103", "v104", "v105", "v106", "v107", "v108", "v109", "v110", "v111", "v112", "v113", "v114", "v115", "v116", "v117", "v118", "v119", "v120", "v121", "v122", "v123", "v124", "v125", "v126", "v127", "v128", "v129", "v130", "v131", "v132", "v133", "v134", "v135", "v180", "s87", "s88", "s89", "vcc"
#define P6_ASM \
  "s_mov_b32 s88, %[sa]\n" \
  "s_mov_b32 s89, 0x4000\n" \
  "buffer_load_dwordx4 v[64:67], %[va0], %[rs], %[sa] offen offset:0 sc1\n" \
  "ds_read_b128 v[156:159], %[vc] offset:0\n" \
  "ds_read_b128 v[160:163], %[vc] offset:64\n" \
  "ds_read_b128 v[164:167], %[vc] offset:128\n" \
  "ds_read_b128 v[168:171], %[vc] offset:192\n" \
  "ds_read_b128 v[172:175], %[vc] offset:256\n" \
  "ds_read_b128 v[176:179], %[vc] offset:320\n" \
  "buffer_load_dwordx4 v[68:71], %[va0], %[rs], %[sa] offen offset:1024 sc1\n" \
  "buffer_load_dwordx4 v[144:147], %[vst], %[rs], 0 offen offset:0 sc1\n" \
  "buffer_load_dwordx4 v[148:151], %[vst], %[rs], 0 offen offset:256 sc1\n" \
  "buffer_load_dwordx4 v[72:75], %[va0], %[rs], %[sa] offen offset:2048 sc1\n" \
  "buffer_load_dwordx4 v[76:79], %[va0], %[rs], %[sa] offen offset:3072 sc1\n" \
  "s_add_u32 %[sa], %[sa], 4096\n" \
  "buffer_load_dwordx4 v[80:83], %[va0], %[rs], %[sa] offen offset:0 sc1\n" \
  "buffer_load_dwordx4 v[84:87], %[va0], %[rs], %[sa] offen offset:1024 sc1\n" \
  "ds_read_b128 v[96:99], %[vw] offset:0\n" \
  "ds_read_b128 v[100:103], %[vw] offset:1024\n" \
  "ds_read_b128 v[104:107], %[vw] offset:2048\n" \
  "ds_read_b128 v[108:111], %[vw] offset:3072\n" \
  "ds_read_b128 v[112:115], %[vw] offset:4096\n" \
  "ds_read_b128 v[116:119], %[vw] offset:5120\n" \
  "ds_read_b128 v[120:123], %[vw] offset:6144\n" \
  "ds_read_b128 v[124:127], %[vw] offset:7168\n" \
  "s_waitcnt vmcnt(7)\n" \
  "LG_ca0%=:\n" \
  "v_max3_u32 v180, v64, v65, v66\n" \
  "v_max3_u32 v180, v180, v67, v67\n" \
  "v_cmp_eq_u32 vcc, -1, v180\n" \
  "s_cbranch_vccnz LG_ra0%=\n" \
  "LG_ba0%=:\n" \
  "ds_read_b128 v[128:131], %[vw] offset:8192\n" \
  "s_waitcnt lgkmcnt(8)\n" \
  "v_mfma_f32_16x16x32_bf16 %[c0], v[96:99], v[64:67], 0\n" \
  "buffer_load_dwordx4 v[88:91], %[va0], %[rs], %[sa] offen offset:2048 sc1\n" \
  "ds_read_b128 v[132:135], %[vw] offset:9216\n" \
  "s_waitcnt lgkmcnt(8)\n" \
  "v_mfma_f32_16x16x32_bf16 %[c1], v[100:103], v[64:67], 0\n" \
  "ds_read_b128 v[96:99], %[vw] offset:10240\n" \
  "s_waitcnt lgkmcnt(8)\n" \
  "v_mfma_f32_16x16x32_bf16 %[c2], v[104:107], v[64:67], 0\n" \
  "ds_read_b128 v[100:103], %[vw] offset:11264\n" \
  "s_waitcnt lgkmcnt(8)\n" \
  "v_mfma_f32_16x16x32_bf16 %[c3], v[108:111], v[64:67], 0\n" \
  "ds_read_b128 v[104:107], %[vw] offset:12288\n" \
  "s_waitcnt lgkmcnt(8)\n" \
  "v_mfma_f32_16x16x32_bf16 %[c4], v[112:115], v[64:67], 0\n" \
  "ds_read_b128 v[108:111], %[vw] offset:13312\n" \
  "s_waitcnt lgkmcnt(8)\n" \
  "v_mfma_f32_16x16x32_bf16 %[c5], v[116:119], v[64:67], 0\n" \
  "s_waitcnt vmcnt(7)\n" \
  "LG_ca1%=:\n" \
  "v_max3_u32 v180, v68, v69, v70\n" \
  "v_max3_u32 v180, v180, v71, v71\n" \
  "v_cmp_eq_u32 vcc, -1, v180\n" \
  "s_cbranch_vccnz LG_ra1%=\n" \
  "LG_ba1%=:\n" \
  "ds_read_b128 v[112:115], %[vw] offset:14336\n" \
  "s_waitcnt lgkmcnt(8)\n" \
  "v_mfma_f32_16x16x32_bf16 %[c0], v[120:123], v[68:71], %[c0]\n" \
  "buffer_load_dwordx4 v[92:95], %[va0], %[rs], %[sa] offen offset:3072 sc1\n" \
  "ds_read_b128 v[116:119], %[vw] offset:15360\n" \
  "s_waitcnt lgkmcnt(8)\n" \
  "v_mfma_f32_16x16x32_bf16 %[c1], v[124:127], v[68:71], %[c1]\n" \
  "ds_read_b128 v[120:123], %[vw] offset:16384\n" \
  "s_waitcnt lgkmcnt(8)\n" \
  "v_mfma_f32_16x16x32_bf16 %[c2], v[128:131], v[68:71], %[c2]\n" \
  "ds_read_b128 v[124:127], %[vw] offset:17408\n" \
  "s_waitcnt lgkmcnt(8)\n" \
  "v_mfma_f32_16x16x32_bf16 %[c3], v[132:135], v[68:71], %[c3]\n" \
  "ds_read_b128 v[128:131], %[vw] offset:18432\n" \
  "s_waitcnt lgkmcnt(8)\n" \
  "v_mfma_f32_16x16x32_bf16 %[c4], v[96:99], v[68:71], %[c4]\n" \
  "ds_read_b128 v[132:135], %[vw] offset:19456\n" \
  "s_waitcnt lgkmcnt(8)\n" \
  "v_mfma_f32_16x16x32_bf16 %[c5], v[100:103], v[68:71], %[c5]\n" \
  "s_waitcnt vmcnt(5)\n" \
  "LG_ca2%=:\n" \
  "v_max3_u32 v180, v72, v73, v74\n" \
  "v_max3_u32 v180, v180, v75, v75\n" \
  "v_cmp_eq_u32 vcc, -1, v180\n" \
  "s_cbranch_vccnz LG_ra2%=\n" \
  "LG_ba2%=:\n" \
  "ds_read_b128 v[96:99], %[vw] offset:20480\n" \
  "s_waitcnt lgkmcnt(8)\n" \
  "v_mfma_f32_16x16x32_bf16 %[c0], v[104:107], v[72:75], %[c0]\n" \
  "s_waitcnt vmcnt(6)\n" \
  "LG_cs%=:\n" \
  "v_max3_u32 v180, v144, v145, v146\n" \
  "v_max3_u32 v180, v180, v147, v148\n" \
  "v_max3_u32 v180, v180, v149, v150\n" \
  "v_max3_u32 v180, v180, v151, v151\n" \
  "v_cmp_eq_u32 vcc, -1, v180\n" \
  "s_cbranch_vccnz LG_rs%=\n" \
  "LG_bs%=:\n" \
  "v_add_f32 v144, v144, v146\n" \
  "s_add_u32 %[sa], %[sa], 4096\n" \
  "buffer_load_dwordx4 v[64:67], %[va0], %[rs], %[sa] offen offset:0 sc1\n" \
  "ds_read_b128 v[100:103], %[vw] offset:21504\n" \
  "s_waitcnt lgkmcnt(8)\n" \
  "v_mfma_f32_16x16x32_bf16 %[c1], v[108:111], v[72:75], %[c1]\n" \
  "v_add_f32 v148, v148, v150\n" \
  "ds_read_b128 v[104:107], %[vw] offset:22528\n" \
  "s_waitcnt lgkmcnt(8)\n" \
  "v_mfma_f32_16x16x32_bf16 %[c2], v[112:115], v[72:75], %[c2]\n" \
  "v_add_f32 v145, v145, v147\n" \
  "ds_read_b128 v[108:111], %[vw] offset:23552\n" \
  "s_waitcnt lgkmcnt(8)\n" \
  "v_mfma_f32_16x16x32_bf16 %[c3], v[116:119], v[72:75], %[c3]\n" \
  "v_add_f32 v149, v149, v151\n" \
  "ds_read_b128 v[112:115], %[vw] offset:24576\n" \
  "s_waitcnt lgkmcnt(8)\n" \
  "v_mfma_f32_16x16x32_bf16 %[c4], v[120:123], v[72:75], %[c4]\n" \
  "v_add_f32 v144, v144, v148\n" \
  "ds_read_b128 v[116:119], %[vw] offset:25600\n" \
  "s_waitcnt lgkmcnt(8)\n" \
  "v_mfma_f32_16x16x32_bf16 %[c5], v[124:127], v[72:75], %[c5]\n" \
  "v_add_f32 v145, v145, v149\n" \
  "s_waitcnt vmcnt(5)\n" \
  "LG_ca3%=:\n" \
  "v_max3_u32 v180, v76, v77, v78\n" \
  "v_max3_u32 v180, v180, v79, v79\n" \
  "v_cmp_eq_u32 vcc, -1, v180\n" \
  "s_cbranch_vccnz LG_ra3%=\n" \
  "LG_ba3%=:\n" \
  "ds_read_b128 v[120:123], %[vw] offset:26624\n" \
  "s_waitcnt lgkmcnt(8)\n" \
  "v_mfma_f32_16x16x32_bf16 %[c0], v[128:131], v[76:79], %[c0]\n" \
  "v_mov_b32 v146, v144\n" \
  "buffer_load_dwordx4 v[68:71], %[va0], %[rs], %[sa] offen offset:1024 sc1\n" \
  "ds_read_b128 v[124:127], %[vw] offset:27648\n" \
  "s_waitcnt lgkmcnt(8)\n" \
  "v_mfma_f32_16x16x32_bf16 %[c1], v[132:135], v[76:79], %[c1]\n" \
  "v_mov_b32 v147, v145\n" \
  "ds_read_b128 v[128:131], %[vw] offset:28672\n" \
  "s_waitcnt lgkmcnt(8)\n" \
  "v_mfma_f32_16x16x32_bf16 %[c2], v[96:99], v[76:79], %[c2]\n" \
  "v_permlane16_swap_b32 v144, v146\n" \
  "ds_read_b128 v[132:135], %[vw] offset:29696\n" \
  "s_waitcnt lgkmcnt(8)\n" \
  "v_mfma_f32_16x16x32_bf16 %[c3], v[100:103], v[76:79], %[c3]\n" \
  "v_permlane16_swap_b32 v145, v147\n" \
  "ds_read_b128 v[96:99], %[vw] offset:30720\n" \
  "s_waitcnt lgkmcnt(8)\n" \
  "v_mfma_f32_16x16x32_bf16 %[c4], v[104:107], v[76:79], %[c4]\n" \
  "v_add_f32 v144, v144, v146\n" \
  "ds_read_b128 v[100:103], %[vw] offset:31744\n" \
  "s_waitcnt lgkmcnt(8)\n" \
  "v_mfma_f32_16x16x32_bf16 %[c5], v[108:111], v[76:79], %[c5]\n" \
  "v_add_f32 v145, v145, v147\n" \
  "s_waitcnt vmcnt(5)\n" \
  "LG_ca4%=:\n" \
  "v_max3_u32 v180, v80, v81, v82\n" \
  "v_max3_u32 v180, v180, v83, v83\n" \
  "v_cmp_eq_u32 vcc, -1, v180\n" \
  "s_cbranch_vccnz LG_ra4%=\n" \
  "LG_ba4%=:\n" \
  "ds_read_b128 v[104:107], %[vw] offset:32768\n" \
  "s_waitcnt lgkmcnt(8)\n" \
  "v_mfma_f32_16x16x32_bf16 %[c0], v[112:115], v[80:83], %[c0]\n" \
  "v_mov_b32 v146, v144\n" \
  "buffer_load_dwordx4 v[72:75], %[va0], %[rs], %[sa] offen offset:2048 sc1\n" \
  "ds_read_b128 v[108:111], %[vw] offset:33792\n" \
  "s_waitcnt lgkmcnt(8)\n" \
  "v_mfma_f32_16x16x32_bf16 %[c1], v[116:119], v[80:83], %[c1]\n" \
  "v_mov_b32 v147, v145\n" \
  "ds_read_b128 v[112:115], %[vw] offset:34816\n" \
  "s_waitcnt lgkmcnt(8)\n" \
  "v_mfma_f32_16x16x32_bf16 %[c2], v[120:123], v[80:83], %[c2]\n" \
  "v_permlane32_swap_b32 v144, v146\n" \
  "ds_read_b128 v[116:119], %[vw] offset:35840\n" \
  "s_waitcnt lgkmcnt(8)\n" \
  "v_mfma_f32_16x16x32_bf16 %[c3], v[124:127], v[80:83], %[c3]\n" \
  "v_permlane32_swap_b32 v145, v147\n" \
  "ds_read_b128 v[120:123], %[vw] offset:36864\n" \
  "s_waitcnt lgkmcnt(8)\n" \
  "v_mfma_f32_16x16x32_bf16 %[c4], v[128:131], v[80:83], %[c4]\n" \
  "v_add_f32 v144, v144, v146\n" \
  "ds_read_b128 v[124:127], %[vw] offset:37888\n" \
  "s_waitcnt lgkmcnt(8)\n" \
  "v_mfma_f32_16x16x32_bf16 %[c5], v[132:135], v[80:83], %[c5]\n" \
  "v_add_f32 v145, v145, v147\n" \
  "s_waitcnt vmcnt(5)\n" \
  "LG_ca5%=:\n" \
  "v_max3_u32 v180, v84, v85, v86\n" \
  "v_max3_u32 v180, v180, v87, v87\n" \
  "v_cmp_eq_u32 vcc, -1, v180\n" \
  "s_cbranch_vccnz LG_ra5%=\n" \
  "LG_ba5%=:\n" \
  "ds_read_b128 v[128:131], %[vw] offset:38912\n" \
  "s_waitcnt lgkmcnt(8)\n" \
  "v_mfma_f32_16x16x32_bf16 %[c0], v[96:99], v[84:87], %[c0]\n" \
  "v_mul_f32 v152, 0x3acccccd, v144\n" \
  "buffer_load_dwordx4 v[76:79], %[va0], %[rs], %[sa] offen offset:3072 sc1\n" \
  "ds_read_b128 v[132:135], %[vw] offset:39936\n" \
  "s_waitcnt lgkmcnt(8)\n" \
  "v_mfma_f32_16x16x32_bf16 %[c1], v[100:103], v[84:87], %[c1]\n" \
  "v_mul_f32 v145, 0x3acccccd, v145\n" \
  "ds_read_b128 v[96:99], %[vw] offset:40960\n" \
  "s_waitcnt lgkmcnt(8)\n" \
  "v_mfma_f32_16x16x32_bf16 %[c2], v[104:107], v[84:87], %[c2]\n" \
  "v_fma_f32 v145, -v152, v152, v145\n" \
  "ds_read_b128 v[100:103], %[vw] offset:41984\n" \
  "s_waitcnt lgkmcnt(8)\n" \
  "v_mfma_f32_16x16x32_bf16 %[c3], v[108:111], v[84:87], %[c3]\n" \
  "v_max_f32 v145, 0, v145\n" \
  "ds_read_b128 v[104:107], %[vw] offset:43008\n" \
  "s_waitcnt lgkmcnt(8)\n" \
  "v_mfma_f32_16x16x32_bf16 %[c4], v[112:115], v[84:87], %[c4]\n" \
  "v_add_f32 v146, 0x3727c5ac, v145\n" \
  "ds_read_b128 v[108:111], %[vw] offset:44032\n" \
  "s_waitcnt lgkmcnt(8)\n" \
  "v_mfma_f32_16x16x32_bf16 %[c5], v[116:119], v[84:87], %[c5]\n" \
  "v_rsq_f32 v153, v146\n" \
  "s_waitcnt vmcnt(5)\n" \
  "LG_ca6%=:\n" \
  "v_max3_u32 v180, v88, v89, v90\n" \
  "v_max3_u32 v180, v180, v91, v91\n" \
  "v_cmp_eq_u32 vcc, -1, v180\n" \
  "s_cbranch_vccnz LG_ra6%=\n" \
  "LG_ba6%=:\n" \
  "ds_read_b128 v[112:115], %[vw] offset:45056\n" \
  "s_waitcnt lgkmcnt(8)\n" \
  "v_mfma_f32_16x16x32_bf16 %[c0], v[120:123], v[88:91], %[c0]\n" \
  "v_mov_b32 v154, v153\n" \
  "s_add_u32 %[sa], %[sa], 4096\n" \
  "buffer_load_dwordx4 v[80:83], %[va0], %[rs], %[sa] offen offset:0 sc1\n" \
  "ds_read_b128 v[116:119], %[vw] offset:46080\n" \
  "s_waitcnt lgkmcnt(8)\n" \
  "v_mfma_f32_16x16x32_bf16 %[c1], v[124:127], v[88:91], %[c1]\n" \
  "v_mul_f32 v155, -v152, v154\n" \
  "ds_read_b128 v[120:123], %[vw] offset:47104\n" \
  "s_waitcnt lgkmcnt(8)\n" \
  "v_mfma_f32_16x16x32_bf16 %[c2], v[128:131], v[88:91], %[c2]\n" \
  "v_mul_f32 v156, v155, v156\n" \
  "ds_read_b128 v[124:127], %[vw] offset:48128\n" \
  "s_waitcnt lgkmcnt(8)\n" \
  "v_mfma_f32_16x16x32_bf16 %[c3], v[132:135], v[88:91], %[c3]\n" \
  "v_mul_f32 v157, v155, v157\n" \
  "ds_read_b128 v[128:131], %[vw] offset:49152\n" \
  "s_waitcnt lgkmcnt(8)\n" \
  "v_mfma_f32_16x16x32_bf16 %[c4], v[96:99], v[88:91], %[c4]\n" \
  "v_mul_f32 v158, v155, v158\n" \
  "ds_read_b128 v[132:135], %[vw] offset:50176\n" \
  "s_waitcnt lgkmcnt(8)\n" \
  "v_mfma_f32_16x16x32_bf16 %[c5], v[100:103], v[88:91], %[c5]\n" \
  "v_mul_f32 v159, v155, v159\n" \
  "s_waitcnt vmcnt(5)\n" \
  "LG_ca7%=:\n" \
  "v_max3_u32 v180, v92, v93, v94\n" \
  "v_max3_u32 v180, v180, v95, v95\n" \
  "v_cmp_eq_u32 vcc, -1, v180\n" \
  "s_cbranch_vccnz LG_ra7%=\n" \
  "LG_ba7%=:\n" \
  "ds_read_b128 v[96:99], %[vw] offset:51200\n" \
  "s_waitcnt lgkmcnt(8)\n" \
  "v_mfma_f32_16x16x32_bf16 %[c0], v[104:107], v[92:95], %[c0]\n" \
  "v_mul_f32 v160, v155, v160\n" \
  "buffer_load_dwordx4 v[84:87], %[va0], %[rs], %[sa] offen offset:1024 sc1\n" \
  "ds_read_b128 v[100:103], %[vw] offset:52224\n" \
  "s_waitcnt lgkmcnt(8)\n" \
  "v_mfma_f32_16x16x32_bf16 %[c1], v[108:111], v[92:95], %[c1]\n" \
  "v_mul_f32 v161, v155, v161\n" \
  "ds_read_b128 v[104:107], %[vw] offset:53248\n" \
  "s_waitcnt lgkmcnt(8)\n" \
  "v_mfma_f32_16x16x32_bf16 %[c2], v[112:115], v[92:95], %[c2]\n" \
  "v_mul_f32 v162, v155, v162\n" \
  "ds_read_b128 v[108:111], %[vw] offset:54272\n" \
  "s_waitcnt lgkmcnt(8)\n" \
  "v_mfma_f32_16x16x32_bf16 %[c3], v[116:119], v[92:95], %[c3]\n" \
  "v_mul_f32 v163, v155, v163\n" \
  "ds_read_b128 v[112:115], %[vw] offset:55296\n" \
  "s_waitcnt lgkmcnt(8)\n" \
  "v_mfma_f32_16x16x32_bf16 %[c4], v[120:123], v[92:95], %[c4]\n" \
  "v_mul_f32 v164, v155, v164\n" \
  "ds_read_b128 v[116:119], %[vw] offset:56320\n" \
  "s_waitcnt lgkmcnt(8)\n" \
  "v_mfma_f32_16x16x32_bf16 %[c5], v[124:127], v[92:95], %[c5]\n" \
  "v_mul_f32 v165, v155, v165\n" \
  "s_barrier\n" \
  "s_waitcnt vmcnt(5)\n" \
  "LG_ca8%=:\n" \
  "v_max3_u32 v180, v64, v65, v66\n" \
  "v_max3_u32 v180, v180, v67, v67\n" \
  "v_cmp_eq_u32 vcc, -1, v180\n" \
  "s_cbranch_vccnz LG_ra8%=\n" \
  "LG_ba8%=:\n" \
  "ds_read_b128 v[120:123], %[vw] offset:57344\n" \
  "s_waitcnt lgkmcnt(8)\n" \
  "v_mfma_f32_16x16x32_bf16 %[c0], v[128:131], v[64:67], %[c0]\n" \
  "v_mul_f32 v166, v155, v166\n" \
  "buffer_load_dwordx4 v[88:91], %[va0], %[rs], %[sa] offen offset:2048 sc1\n" \
  "ds_read_b128 v[124:127], %[vw] offset:58368\n" \
  "s_waitcnt lgkmcnt(8)\n" \
  "v_mfma_f32_16x16x32_bf16 %[c1], v[132:135], v[64:67], %[c1]\n" \
  "v_mul_f32 v167, v155, v167\n" \
  "ds_read_b128 v[128:131], %[vw] offset:59392\n" \
  "s_waitcnt lgkmcnt(8)\n" \
  "v_mfma_f32_16x16x32_bf16 %[c2], v[96:99], v[64:67], %[c2]\n" \
  "v_mul_f32 v168, v155, v168\n" \
  "ds_read_b128 v[132:135], %[vw] offset:60416\n" \
  "s_waitcnt lgkmcnt(8)\n" \
  "v_mfma_f32_16x16x32_bf16 %[c3], v[100:103], v[64:67], %[c3]\n" \
  "v_mul_f32 v169, v155, v169\n" \
  "s_mov_b32 m0, %[dm0]\n" \
  "s_nop 0\n" \
  "buffer_load_dwordx4 %[l16], %[rs], %[ds0] offen offset:0 sc1 lds\n" \
  "ds_read_b128 v[96:99], %[vw] offset:61440\n" \
  "s_waitcnt lgkmcnt(8)\n" \
  "v_mfma_f32_16x16x32_bf16 %[c4], v[104:107], v[64:67], %[c4]\n" \
  "v_mul_f32 v170, v155, v170\n" \
  "ds_read_b128 v[100:103], %[vw] offset:62464\n" \
  "s_waitcnt lgkmcnt(8)\n" \
  "v_mfma_f32_16x16x32_bf16 %[c5], v[108:111], v[64:67], %[c5]\n" \
  "v_mul_f32 v171, v155, v171\n" \
  "s_waitcnt vmcnt(6)\n" \
  "LG_ca9%=:\n" \
  "v_max3_u32 v180, v68, v69, v70\n" \
  "v_max3_u32 v180, v180, v71, v71\n" \
  "v_cmp_eq_u32 vcc, -1, v180\n" \
  "s_cbranch_vccnz LG_ra9%=\n" \
  "LG_ba9%=:\n" \
  "ds_read_b128 v[104:107], %[vw] offset:63488\n" \
  "s_waitcnt lgkmcnt(8)\n" \
  "v_mfma_f32_16x16x32_bf16 %[c0], v[112:115], v[68:71], %[c0]\n" \
  "v_mul_f32 v172, v155, v172\n" \
  "buffer_load_dwordx4 v[92:95], %[va0], %[rs], %[sa] offen offset:3072 sc1\n" \
  "ds_read_b128 v[108:111], %[vw] offset:64512\n" \
  "s_waitcnt lgkmcnt(8)\n" \
  "v_mfma_f32_16x16x32_bf16 %[c1], v[116:119], v[68:71], %[c1]\n" \
  "v_mul_f32 v173, v155, v173\n" \
  "v_add_u32 %[vw], 65536, %[vw]\n" \
  "ds_read_b128 v[112:115], %[vw] offset:0\n" \
  "s_waitcnt lgkmcnt(8)\n" \
  "v_mfma_f32_16x16x32_bf16 %[c2], v[120:123], v[68:71], %[c2]\n" \
  "v_mul_f32 v174, v155, v174\n" \
  "ds_read_b128 v[116:119], %[vw] offset:1024\n" \
  "s_waitcnt lgkmcnt(8)\n" \
  "v_mfma_f32_16x16x32_bf16 %[c3], v[124:127], v[68:71], %[c3]\n" \
  "v_mul_f32 v175, v155, v175\n" \
  "ds_read_b128 v[120:123], %[vw] offset:2048\n" \
  "s_waitcnt lgkmcnt(8)\n" \
  "v_mfma_f32_16x16x32_bf16 %[c4], v[128:131], v[68:71], %[c4]\n" \
  "v_mul_f32 v176, v155, v176\n" \
  "ds_read_b128 v[124:127], %[vw] offset:3072\n" \
  "s_waitcnt lgkmcnt(8)\n" \
  "v_mfma_f32_16x16x32_bf16 %[c5], v[132:135], v[68:71], %[c5]\n" \
  "v_mul_f32 v177, v155, v177\n" \
  "s_waitcnt vmcnt(6)\n" \
  "LG_ca10%=:\n" \
  "v_max3_u32 v180, v72, v73, v74\n" \
  "v_max3_u32 v180, v180, v75, v75\n" \
  "v_cmp_eq_u32 vcc, -1, v180\n" \
  "s_cbranch_vccnz LG_ra10%=\n" \
  "LG_ba10%=:\n" \
  "ds_read_b128 v[128:131], %[vw] offset:4096\n" \
  "s_waitcnt lgkmcnt(8)\n" \
  "v_mfma_f32_16x16x32_bf16 %[c0], v[96:99], v[72:75], %[c0]\n" \
  "v_mul_f32 v178, v155, v178\n" \
  "s_add_u32 %[sa], %[sa], 4096\n" \
  "buffer_load_dwordx4 v[64:67], %[va0], %[rs], %[sa] offen offset:0 sc1\n" \
  "ds_read_b128 v[132:135], %[vw] offset:5120\n" \
  "s_waitcnt lgkmcnt(8)\n" \
  "v_mfma_f32_16x16x32_bf16 %[c1], v[100:103], v[72:75], %[c1]\n" \
  "v_mul_f32 v179, v155, v179\n" \
  "ds_read_b128 v[96:99], %[vw] offset:6144\n" \
  "s_waitcnt lgkmcnt(8)\n" \
  "v_mfma_f32_16x16x32_bf16 %[c2], v[104:107], v[72:75], %[c2]\n" \
  "ds_read_b128 v[100:103], %[vw] offset:7168\n" \
  "s_waitcnt lgkmcnt(8)\n" \
  "v_mfma_f32_16x16x32_bf16 %[c3], v[108:111], v[72:75], %[c3]\n" \
  "buffer_load_dwordx4 %[l16], %[rs], %[ds0] offen offset:1024 sc1 lds\n" \
  "ds_read_b128 v[104:107], %[vw] offset:8192\n" \
  "s_waitcnt lgkmcnt(8)\n" \
  "v_mfma_f32_16x16x32_bf16 %[c4], v[112:115], v[72:75], %[c4]\n" \
  "ds_read_b128 v[108:111], %[vw] offset:9216\n" \
  "s_waitcnt lgkmcnt(8)\n" \
  "v_mfma_f32_16x16x32_bf16 %[c5], v[116:119], v[72:75], %[c5]\n" \
  "s_waitcnt vmcnt(7)\n" \
  "LG_ca11%=:\n" \
  "v_max3_u32 v180, v76, v77, v78\n" \
  "v_max3_u32 v180, v180, v79, v79\n" \
  "v_cmp_eq_u32 vcc, -1, v180\n" \
  "s_cbranch_vccnz LG_ra11%=\n" \
  "LG_ba11%=:\n" \
  "ds_read_b128 v[112:115], %[vw] offset:10240\n" \
  "s_waitcnt lgkmcnt(8)\n" \
  "v_mfma_f32_16x16x32_bf16 %[c0], v[120:123], v[76:79], %[c0]\n" \
  "buffer_load_dwordx4 v[68:71], %[va0], %[rs], %[sa] offen offset:1024 sc1\n" \
  "ds_read_b128 v[116:119], %[vw] offset:11264\n" \
  "s_waitcnt lgkmcnt(8)\n" \
  "v_mfma_f32_16x16x32_bf16 %[c1], v[124:127], v[76:79], %[c1]\n" \
  "ds_read_b128 v[120:123], %[vw] offset:12288\n" \
  "s_waitcnt lgkmcnt(8)\n" \
  "v_mfma_f32_16x16x32_bf16 %[c2], v[128:131], v[76:79], %[c2]\n" \
  "ds_read_b128 v[124:127], %[vw] offset:13312\n" \
  "s_waitcnt lgkmcnt(8)\n" \
  "v_mfma_f32_16x16x32_bf16 %[c3], v[132:135], v[76:79], %[c3]\n" \
  "ds_read_b128 v[128:131], %[vw] offset:14336\n" \
  "s_waitcnt lgkmcnt(8)\n" \
  "v_mfma_f32_16x16x32_bf16 %[c4], v[96:99], v[76:79], %[c4]\n" \
  "ds_read_b128 v[132:135], %[vw] offset:15360\n" \
  "s_waitcnt lgkmcnt(8)\n" \
  "v_mfma_f32_16x16x32_bf16 %[c5], v[100:103], v[76:79], %[c5]\n" \
  "s_waitcnt vmcnt(7)\n" \
  "LG_ca12%=:\n" \
  "v_max3_u32 v180, v80, v81, v82\n" \
  "v_max3_u32 v180, v180, v83, v83\n" \
  "v_cmp_eq_u32 vcc, -1, v180\n" \
  "s_cbranch_vccnz LG_ra12%=\n" \
  "LG_ba12%=:\n" \
  "ds_read_b128 v[96:99], %[vw] offset:16384\n" \
  "s_waitcnt lgkmcnt(8)\n" \
  "v_mfma_f32_16x16x32_bf16 %[c0], v[104:107], v[80:83], %[c0]\n" \
  "buffer_load_dwordx4 v[72:75], %[va0], %[rs], %[sa] offen offset:2048 sc1\n" \
  "ds_read_b128 v[100:103], %[vw] offset:17408\n" \
  "s_waitcnt lgkmcnt(8)\n" \
  "v_mfma_f32_16x16x32_bf16 %[c1], v[108:111], v[80:83], %[c1]\n" \
  "ds_read_b128 v[104:107], %[vw] offset:18432\n" \
  "s_waitcnt lgkmcnt(8)\n" \
  "v_mfma_f32_16x16x32_bf16 %[c2], v[112:115], v[80:83], %[c2]\n" \
  "ds_read_b128 v[108:111], %[vw] offset:19456\n" \
  "s_waitcnt lgkmcnt(8)\n" \
  "v_mfma_f32_16x16x32_bf16 %[c3], v[116:119], v[80:83], %[c3]\n" \
  "buffer_load_dwordx4 %[l16], %[rs], %[ds0] offen offset:2048 sc1 lds\n" \
  "ds_read_b128 v[112:115], %[vw] offset:20480\n" \
  "s_waitcnt lgkmcnt(8)\n" \
  "v_mfma_f32_16x16x32_bf16 %[c4], v[120:123], v[80:83], %[c4]\n" \
  "ds_read_b128 v[116:119], %[vw] offset:21504\n" \
  "s_waitcnt lgkmcnt(8)\n" \
  "v_mfma_f32_16x16x32_bf16 %[c5], v[124:127], v[80:83], %[c5]\n" \
  "s_waitcnt vmcnt(8)\n" \
  "LG_ca13%=:\n" \
  "v_max3_u32 v180, v84, v85, v86\n" \
  "v_max3_u32 v180, v180, v87, v87\n" \
  "v_cmp_eq_u32 vcc, -1, v180\n" \
  "s_cbranch_vccnz LG_ra13%=\n" \
  "LG_ba13%=:\n" \
  "ds_read_b128 v[120:123], %[vw] offset:22528\n" \
  "s_waitcnt lgkmcnt(8)\n" \
  "v_mfma_f32_16x16x32_bf16 %[c0], v[128:131], v[84:87], %[c0]\n" \
  "buffer_load_dwordx4 v[76:79], %[va0], %[rs], %[sa] offen offset:3072 sc1\n" \
  "ds_read_b128 v[124:127], %[vw] offset:23552\n" \
  "s_waitcnt lgkmcnt(8)\n" \
  "v_mfma_f32_16x16x32_bf16 %[c1], v[132:135], v[84:87], %[c1]\n" \
  "ds_read_b128 v[128:131], %[vw] offset:24576\n" \
  "s_waitcnt lgkmcnt(8)\n" \
  "v_mfma_f32_16x16x32_bf16 %[c2], v[96:99], v[84:87], %[c2]\n" \
  "ds_read_b128 v[132:135], %[vw] offset:25600\n" \
  "s_waitcnt lgkmcnt(8)\n" \
  "v_mfma_f32_16x16x32_bf16 %[c3], v[100:103], v[84:87], %[c3]\n" \
  "s_barrier\n" \
  "ds_read_b128 v[96:99], %[vw] offset:26624\n" \
  "s_waitcnt lgkmcnt(8)\n" \
  "v_mfma_f32_16x16x32_bf16 %[c4], v[104:107], v[84:87], %[c4]\n" \
  "ds_read_b128 v[100:103], %[vw] offset:27648\n" \
  "s_waitcnt lgkmcnt(8)\n" \
  "v_mfma_f32_16x16x32_bf16 %[c5], v[108:111], v[84:87], %[c5]\n" \
  "s_waitcnt vmcnt(8)\n" \
  "LG_ca14%=:\n" \
  "v_max3_u32 v180, v88, v89, v90\n" \
  "v_max3_u32 v180, v180, v91, v91\n" \
  "v_cmp_eq_u32 vcc, -1, v180\n" \
  "s_cbranch_vccnz LG_ra14%=\n" \
  "LG_ba14%=:\n" \
  "ds_read_b128 v[104:107], %[vw] offset:28672\n" \
  "s_waitcnt lgkmcnt(8)\n" \
  "v_mfma_f32_16x16x32_bf16 %[c0], v[112:115], v[88:91], %[c0]\n" \
  "ds_read_b128 v[108:111], %[vw] offset:29696\n" \
  "s_waitcnt lgkmcnt(8)\n" \
  "v_mfma_f32_16x16x32_bf16 %[c1], v[116:119], v[88:91], %[c1]\n" \
  "ds_read_b128 v[112:115], %[vw] offset:30720\n" \
  "s_waitcnt lgkmcnt(8)\n" \
  "v_mfma_f32_16x16x32_bf16 %[c2], v[120:123], v[88:91], %[c2]\n" \
  "ds_read_b128 v[116:119], %[vw] offset:31744\n" \
  "s_waitcnt lgkmcnt(8)\n" \
  "v_mfma_f32_16x16x32_bf16 %[c3], v[124:127], v[88:91], %[c3]\n" \
  "buffer_load_dwordx4 %[l16], %[rs], %[ds0] offen offset:3072 sc1 lds\n" \
  "ds_read_b128 v[120:123], %[vw] offset:32768\n" \
  "s_waitcnt lgkmcnt(8)\n" \
  "v_mfma_f32_16x16x32_bf16 %[c4], v[128:131], v[88:91], %[c4]\n" \
  "ds_read_b128 v[124:127], %[vw] offset:33792\n" \
  "s_waitcnt lgkmcnt(8)\n" \
  "v_mfma_f32_16x16x32_bf16 %[c5], v[132:135], v[88:91], %[c5]\n" \
  "s_waitcnt vmcnt(7)\n" \
  "LG_ca15%=:\n" \
  "v_max3_u32 v180, v92, v93, v94\n" \
  "v_max3_u32 v180, v180, v95, v95\n" \
  "v_cmp_eq_u32 vcc, -1, v180\n" \
  "s_cbranch_vccnz LG_ra15%=\n" \
  "LG_ba15%=:\n" \
  "ds_read_b128 v[128:131], %[vw] offset:34816\n" \
  "s_waitcnt lgkmcnt(8)\n" \
  "v_mfma_f32_16x16x32_bf16 %[c0], v[96:99], v[92:95], %[c0]\n" \
  "ds_read_b128 v[132:135], %[vw] offset:35840\n" \
  "s_waitcnt lgkmcnt(8)\n" \
  "v_mfma_f32_16x16x32_bf16 %[c1], v[100:103], v[92:95], %[c1]\n" \
  "ds_read_b128 v[96:99], %[vw] offset:36864\n" \
  "s_waitcnt lgkmcnt(8)\n" \
  "v_mfma_f32_16x16x32_bf16 %[c2], v[104:107], v[92:95], %[c2]\n" \
  "ds_read_b128 v[100:103], %[vw] offset:37888\n" \
  "s_waitcnt lgkmcnt(8)\n" \
  "v_mfma_f32_16x16x32_bf16 %[c3], v[108:111], v[92:95], %[c3]\n" \
  "ds_read_b128 v[104:107], %[vw] offset:38912\n" \
  "s_waitcnt lgkmcnt(8)\n" \
  "v_mfma_f32_16x16x32_bf16 %[c4], v[112:115], v[92:95], %[c4]\n" \
  "ds_read_b128 v[108:111], %[vw] offset:39936\n" \
  "s_waitcnt lgkmcnt(8)\n" \
  "v_mfma_f32_16x16x32_bf16 %[c5], v[116:119], v[92:95], %[c5]\n" \
  "s_waitcnt vmcnt(6)\n" \
  "LG_ca16%=:\n" \
  "v_max3_u32 v180, v64, v65, v66\n" \
  "v_max3_u32 v180, v180, v67, v67\n" \
  "v_cmp_eq_u32 vcc, -1, v180\n" \
  "s_cbranch_vccnz LG_ra16%=\n" \
  "LG_ba16%=:\n" \
  "ds_read_b128 v[112:115], %[vw] offset:40960\n" \
  "s_waitcnt lgkmcnt(8)\n" \
  "v_mfma_f32_16x16x32_bf16 %[c0], v[120:123], v[64:67], %[c0]\n" \
  "ds_read_b128 v[116:119], %[vw] offset:41984\n" \
  "s_waitcnt lgkmcnt(8)\n" \
  "v_mfma_f32_16x16x32_bf16 %[c1], v[124:127], v[64:67], %[c1]\n" \
  "ds_read_b128 v[120:123], %[vw] offset:43008\n" \
  "s_waitcnt lgkmcnt(8)\n" \
  "v_mfma_f32_16x16x32_bf16 %[c2], v[128:131], v[64:67], %[c2]\n" \
  "ds_read_b128 v[124:127], %[vw] offset:44032\n" \
  "s_waitcnt lgkmcnt(8)\n" \
  "v_mfma_f32_16x16x32_bf16 %[c3], v[132:135], v[64:67], %[c3]\n" \
  "s_add_u32 %[dm0], %[dm0], 4096\n" \
  "s_add_u32 %[ds0], %[ds0], 4096\n" \
  "s_mov_b32 m0, %[dm0]\n" \
  "s_nop 0\n" \
  "buffer_load_dwordx4 %[l16], %[rs], %[ds0] offen offset:0 sc1 lds\n" \
  "ds_read_b128 v[128:131], %[vw] offset:45056\n" \
  "s_waitcnt lgkmcnt(8)\n" \
  "v_mfma_f32_16x16x32_bf16 %[c4], v[96:99], v[64:67], %[c4]\n" \
  "ds_read_b128 v[132:135], %[vw] offset:46080\n" \
  "s_waitcnt lgkmcnt(8)\n" \
  "v_mfma_f32_16x16x32_bf16 %[c5], v[100:103], v[64:67], %[c5]\n" \
  "s_waitcnt vmcnt(5)\n" \
  "LG_ca17%=:\n" \
  "v_max3_u32 v180, v68, v69, v70\n" \
  "v_max3_u32 v180, v180, v71, v71\n" \
  "v_cmp_eq_u32 vcc, -1, v180\n" \
  "s_cbranch_vccnz LG_ra17%=\n" \
  "LG_ba17%=:\n" \
  "ds_read_b128 v[96:99], %[vw] offset:47104\n" \
  "s_waitcnt lgkmcnt(8)\n" \
  "v_mfma_f32_16x16x32_bf16 %[c0], v[104:107], v[68:71], %[c0]\n" \
  "ds_read_b128 v[100:103], %[vw] offset:48128\n" \
  "s_waitcnt lgkmcnt(8)\n" \
  "v_mfma_f32_16x16x32_bf16 %[c1], v[108:111], v[68:71], %[c1]\n" \
  "ds_read_b128 v[104:107], %[vw] offset:49152\n" \
  "s_waitcnt lgkmcnt(8)\n" \
  "v_mfma_f32_16x16x32_bf16 %[c2], v[112:115], v[68:71], %[c2]\n" \
  "ds_read_b128 v[108:111], %[vw] offset:50176\n" \
  "s_waitcnt lgkmcnt(8)\n" \
  "v_mfma_f32_16x16x32_bf16 %[c3], v[116:119], v[68:71], %[c3]\n" \
  "ds_read_b128 v[112:115], %[vw] offset:51200\n" \
  "s_waitcnt lgkmcnt(8)\n" \
  "v_mfma_f32_16x16x32_bf16 %[c4], v[120:123], v[68:71], %[c4]\n" \
  "ds_read_b128 v[116:119], %[vw] offset:52224\n" \
  "s_waitcnt lgkmcnt(8)\n" \
  "v_mfma_f32_16x16x32_bf16 %[c5], v[124:127], v[68:71], %[c5]\n" \
  "s_waitcnt vmcnt(4)\n" \
  "LG_ca18%=:\n" \
  "v_max3_u32 v180, v72, v73, v74\n" \
  "v_max3_u32 v180, v180, v75, v75\n" \
  "v_cmp_eq_u32 vcc, -1, v180\n" \
  "s_cbranch_vccnz LG_ra18%=\n" \
  "LG_ba18%=:\n" \
  "ds_read_b128 v[120:123], %[vw] offset:53248\n" \
  "s_waitcnt lgkmcnt(8)\n" \
  "v_mfma_f32_16x16x32_bf16 %[c0], v[128:131], v[72:75], %[c0]\n" \
  "ds_read_b128 v[124:127], %[vw] offset:54272\n" \
  "s_waitcnt lgkmcnt(8)\n" \
  "v_mfma_f32_16x16x32_bf16 %[c1], v[132:135], v[72:75], %[c1]\n" \
  "ds_read_b128 v[128:131], %[vw] offset:55296\n" \
  "s_waitcnt lgkmcnt(8)\n" \
  "v_mfma_f32_16x16x32_bf16 %[c2], v[96:99], v[72:75], %[c2]\n" \
  "ds_read_b128 v[132:135], %[vw] offset:56320\n" \
  "s_waitcnt lgkmcnt(8)\n" \
  "v_mfma_f32_16x16x32_bf16 %[c3], v[100:103], v[72:75], %[c3]\n" \
  "s_waitcnt lgkmcnt(7)\n" \
  "v_mfma_f32_16x16x32_bf16 %[c4], v[104:107], v[72:75], %[c4]\n" \
  "s_waitcnt lgkmcnt(6)\n" \
  "v_mfma_f32_16x16x32_bf16 %[c5], v[108:111], v[72:75], %[c5]\n" \
  "s_waitcnt vmcnt(2)\n" \
  "LG_ca19%=:\n" \
  "v_max3_u32 v180, v76, v77, v78\n" \
  "v_max3_u32 v180, v180, v79, v79\n" \
  "v_cmp_eq_u32 vcc, -1, v180\n" \
  "s_cbranch_vccnz LG_ra19%=\n" \
  "LG_ba19%=:\n" \
  "s_waitcnt lgkmcnt(5)\n" \
  "v_mfma_f32_16x16x32_bf16 %[c0], v[112:115], v[76:79], %[c0]\n" \
  "s_waitcnt lgkmcnt(4)\n" \
  "v_mfma_f32_16x16x32_bf16 %[c1], v[116:119], v[76:79], %[c1]\n" \
  "s_waitcnt lgkmcnt(3)\n" \
  "v_mfma_f32_16x16x32_bf16 %[c2], v[120:123], v[76:79], %[c2]\n" \
  "s_waitcnt lgkmcnt(2)\n" \
  "v_mfma_f32_16x16x32_bf16 %[c3], v[124:127], v[76:79], %[c3]\n" \
  "s_waitcnt lgkmcnt(1)\n" \
  "v_mfma_f32_16x16x32_bf16 %[c4], v[128:131], v[76:79], %[c4]\n" \
  "s_waitcnt lgkmcnt(0)\n" \
  "v_mfma_f32_16x16x32_bf16 %[c5], v[132:135], v[76:79], %[c5]\n" \
  "s_branch LG_end%=\n" \
  "LG_ra0%=:\n" \
  "s_sub_u32 s89, s89, 1\n" \
  "s_cbranch_scc1 LG_xa0%=\n" \
  "s_add_u32 s87, s88, 0\n" \
  "buffer_load_dwordx4 v[64:67], %[va0], %[rs], s87 offen sc1\n" \
  "s_waitcnt vmcnt(0)\n" \
  "s_branch LG_ca0%=\n" \
  "LG_xa0%=:\n" \
  "s_mov_b32 s89, 0\n" \
  "s_branch LG_ba0%=\n" \
  "LG_ra1%=:\n" \
  "s_sub_u32 s89, s89, 1\n" \
  "s_cbranch_scc1 LG_xa1%=\n" \
  "s_add_u32 s87, s88, 1024\n" \
  "buffer_load_dwordx4 v[68:71], %[va0], %[rs], s87 offen sc1\n" \
  "s_waitcnt vmcnt(0)\n" \
  "s_branch LG_ca1%=\n" \
  "LG_xa1%=:\n" \
  "s_mov_b32 s89, 0\n" \
  "s_branch LG_ba1%=\n" \
  "LG_ra2%=:\n" \
  "s_sub_u32 s89, s89, 1\n" \
  "s_cbranch_scc1 LG_xa2%=\n" \
  "s_add_u32 s87, s88, 2048\n" \
  "buffer_load_dwordx4 v[72:75], %[va0], %[rs], s87 offen sc1\n" \
  "s_waitcnt vmcnt(0)\n" \
  "s_branch LG_ca2%=\n" \
  "LG_xa2%=:\n" \
  "s_mov_b32 s89, 0\n" \
  "s_branch LG_ba2%=\n" \
  "LG_rs%=:\n" \
  "s_sub_u32 s89, s89, 1\n" \
  "s_cbranch_scc1 LG_xs%=\n" \
  "buffer_load_dwordx4 v[144:147], %[vst], %[rs], 0 offen offset:0 sc1\n" \
  "buffer_load_dwordx4 v[148:151], %[vst], %[rs], 0 offen offset:256 sc1\n" \
  "s_waitcnt vmcnt(0)\n" \
  "s_branch LG_cs%=\n" \
  "LG_xs%=:\n" \
  "s_mov_b32 s89, 0\n" \
  "s_branch LG_bs%=\n" \
  "LG_ra3%=:\n" \
  "s_sub_u32 s89, s89, 1\n" \
  "s_cbranch_scc1 LG_xa3%=\n" \
  "s_add_u32 s87, s88, 3072\n" \
  "buffer_load_dwordx4 v[76:79], %[va0], %[rs], s87 offen sc1\n" \
  "s_waitcnt vmcnt(0)\n" \
  "s_branch LG_ca3%=\n" \
  "LG_xa3%=:\n" \
  "s_mov_b32 s89, 0\n" \
  "s_branch LG_ba3%=\n" \
  "LG_ra4%=:\n" \
  "s_sub_u32 s89, s89, 1\n" \
  "s_cbranch_scc1 LG_xa4%=\n" \
  "s_add_u32 s87, s88, 4096\n" \
  "buffer_load_dwordx4 v[80:83], %[va0], %[rs], s87 offen sc1\n" \
  "s_waitcnt vmcnt(0)\n" \
  "s_branch LG_ca4%=\n" \
  "LG_xa4%=:\n" \
  "s_mov_b32 s89, 0\n" \
  "s_branch LG_ba4%=\n" \
  "LG_ra5%=:\n" \
  "s_sub_u32 s89, s89, 1\n" \
  "s_cbranch_scc1 LG_xa5%=\n" \
  "s_add_u32 s87, s88, 5120\n" \
  "buffer_load_dwordx4 v[84:87], %[va0], %[rs], s87 offen sc1\n" \
  "s_waitcnt vmcnt(0)\n" \
  "s_branch LG_ca5%=\n" \
  "LG_xa5%=:\n" \
  "s_mov_b32 s89, 0\n" \
  "s_branch LG_ba5%=\n" \
  "LG_ra6%=:\n" \
  "s_sub_u32 s89, s89, 1\n" \
  "s_cbranch_scc1 LG_xa6%=\n" \
  "s_add_u32 s87, s88, 6144\n" \
  "buffer_load_dwordx4 v[88:91], %[va0], %[rs], s87 offen sc1\n" \
  "s_waitcnt vmcnt(0)\n" \
  "s_branch LG_ca6%=\n" \
  "LG_xa6%=:\n" \
  "s_mov_b32 s89, 0\n" \
  "s_branch LG_ba6%=\n" \
  "LG_ra7%=:\n" \
  "s_sub_u32 s89, s89, 1\n" \
  "s_cbranch_scc1 LG_xa7%=\n" \
  "s_add_u32 s87, s88, 7168\n" \
  "buffer_load_dwordx4 v[92:95], %[va0], %[rs], s87 offen sc1\n" \
  "s_waitcnt vmcnt(0)\n" \
  "s_branch LG_ca7%=\n" \
  "LG_xa7%=:\n" \
  "s_mov_b32 s89, 0\n" \
  "s_branch LG_ba7%=\n" \
  "LG_ra8%=:\n" \
  "s_sub_u32 s89, s89, 1\n" \
  "s_cbranch_scc1 LG_xa8%=\n" \
  "s_add_u32 s87, s88, 8192\n" \
  "buffer_load_dwordx4 v[64:67], %[va0], %[rs], s87 offen sc1\n" \
  "s_waitcnt vmcnt(0)\n" \
  "s_branch LG_ca8%=\n" \
  "LG_xa8%=:\n" \
  "s_mov_b32 s89, 0\n" \
  "s_branch LG_ba8%=\n" \
  "LG_ra9%=:\n" \
  "s_sub_u32 s89, s89, 1\n" \
  "s_cbranch_scc1 LG_xa9%=\n" \
  "s_add_u32 s87, s88, 9216\n" \
  "buffer_load_dwordx4 v[68:71], %[va0], %[rs], s87 offen sc1\n" \
  "s_waitcnt vmcnt(0)\n" \
  "s_branch LG_ca9%=\n" \
  "LG_xa9%=:\n" \
  "s_mov_b32 s89, 0\n" \
  "s_branch LG_ba9%=\n" \
  "LG_ra10%=:\n" \
  "s_sub_u32 s89, s89, 1\n" \
  "s_cbranch_scc1 LG_xa10%=\n" \
  "s_add_u32 s87, s88, 10240\n" \
  "buffer_load_dwordx4 v[72:75], %[va0], %[rs], s87 offen sc1\n" \
  "s_waitcnt vmcnt(0)\n" \
  "s_branch LG_ca10%=\n" \
  "LG_xa10%=:\n" \
  "s_mov_b32 s89, 0\n" \
  "s_branch LG_ba10%=\n" \
  "LG_ra11%=:\n" \
  "s_sub_u32 s89, s89, 1\n" \
  "s_cbranch_scc1 LG_xa11%=\n" \
  "s_add_u32 s87, s88, 11264\n" \
  "buffer_load_dwordx4 v[76:79], %[va0], %[rs], s87 offen sc1\n" \
  "s_waitcnt vmcnt(0)\n" \
  "s_branch LG_ca11%=\n" \
  "LG_xa11%=:\n" \
  "s_mov_b32 s89, 0\n" \
  "s_branch LG_ba11%=\n" \
  "LG_ra12%=:\n" \
  "s_sub_u32 s89, s89, 1\n" \
  "s_cbranch_scc1 LG_xa12%=\n" \
  "s_add_u32 s87, s88, 12288\n" \
  "buffer_load_dwordx4 v[80:83], %[va0], %[rs], s87 offen sc1\n" \
  "s_waitcnt vmcnt(0)\n" \
  "s_branch LG_ca12%=\n" \
  "LG_xa12%=:\n" \
  "s_mov_b32 s89, 0\n" \
  "s_branch LG_ba12%=\n" \
  "LG_ra13%=:\n" \
  "s_sub_u32 s89, s89, 1\n" \
  "s_cbranch_scc1 LG_xa13%=\n" \
  "s_add_u32 s87, s88, 13312\n" \
  "buffer_load_dwordx4 v[84:87], %[va0], %[rs], s87 offen sc1\n" \
  "s_waitcnt vmcnt(0)\n" \
  "s_branch LG_ca13%=\n" \
  "LG_xa13%=:\n" \
  "s_mov_b32 s89, 0\n" \
  "s_branch LG_ba13%=\n" \
  "LG_ra14%=:\n" \
  "s_sub_u32 s89, s89, 1\n" \
  "s_cbranch_scc1 LG_xa14%=\n" \
  "s_add_u32 s87, s88, 14336\n" \
  "buffer_load_dwordx4 v[88:91], %[va0], %[rs], s87 offen sc1\n" \
  "s_waitcnt vmcnt(0)\n" \
  "s_branch LG_ca14%=\n" \
  "LG_xa14%=:\n" \
  "s_mov_b32 s89, 0\n" \
  "s_branch LG_ba14%=\n" \
  "LG_ra15%=:\n" \
  "s_sub_u32 s89, s89, 1\n" \
  "s_cbranch_scc1 LG_xa15%=\n" \
  "s_add_u32 s87, s88, 15360\n" \
  "buffer_load_dwordx4 v[92:95], %[va0], %[rs], s87 offen sc1\n" \
  "s_waitcnt vmcnt(0)\n" \
  "s_branch LG_ca15%=\n" \
  "LG_xa15%=:\n" \
  "s_mov_b32 s89, 0\n" \
  "s_branch LG_ba15%=\n" \
  "LG_ra16%=:\n" \
  "s_sub_u32 s89, s89, 1\n" \
  "s_cbranch_scc1 LG_xa16%=\n" \
  "s_add_u32 s87, s88, 16384\n" \
  "buffer_load_dwordx4 v[64:67], %[va0], %[rs], s87 offen sc1\n" \
  "s_waitcnt vmcnt(0)\n" \
  "s_branch LG_ca16%=\n" \
  "LG_xa16%=:\n" \
  "s_mov_b32 s89, 0\n" \
  "s_branch LG_ba16%=\n" \
  "LG_ra17%=:\n" \
  "s_sub_u32 s89, s89, 1\n" \
  "s_cbranch_scc1 LG_xa17%=\n" \
  "s_add_u32 s87, s88, 17408\n" \
  "buffer_load_dwordx4 v[68:71], %[va0], %[rs], s87 offen sc1\n" \
  "s_waitcnt vmcnt(0)\n" \
  "s_branch LG_ca17%=\n" \
  "LG_xa17%=:\n" \
  "s_mov_b32 s89, 0\n" \
  "s_branch LG_ba17%=\n" \
  "LG_ra18%=:\n" \
  "s_sub_u32 s89, s89, 1\n" \
  "s_cbranch_scc1 LG_xa18%=\n" \
  "s_add_u32 s87, s88, 18432\n" \
  "buffer_load_dwordx4 v[72:75], %[va0], %[rs], s87 offen sc1\n" \
  "s_waitcnt vmcnt(0)\n" \
  "s_branch LG_ca18%=\n" \
  "LG_xa18%=:\n" \
  "s_mov_b32 s89, 0\n" \
  "s_branch LG_ba18%=\n" \
  "LG_ra19%=:\n" \
  "s_sub_u32 s89, s89, 1\n" \
  "s_cbranch_scc1 LG_xa19%=\n" \
  "s_add_u32 s87, s88, 19456\n" \
  "buffer_load_dwordx4 v[76:79], %[va0], %[rs], s87 offen sc1\n" \
  "s_waitcnt vmcnt(0)\n" \
  "s_branch LG_ca19%=\n" \
  "LG_xa19%=:\n" \
  "s_mov_b32 s89, 0\n" \
  "s_branch LG_ba19%=\n" \
  "LG_end%=:\n" \
  "s_nop 7\n" \
  ""
#define P7_ASM \
  "buffer_load_dwordx4 v[64:67], %[va0], %[rs], %[sa] offen offset:0 sc1\n" \
  "buffer_load_dwordx4 v[68:71], %[va0], %[rs], %[sa] offen offset:1024 sc1\n" \
  "buffer_load_dwordx4 v[72:75], %[va0], %[rs], %[sa] offen offset:2048 sc1\n" \
  "buffer_load_dwordx4 v[76:79], %[va0], %[rs], %[sa] offen offset:3072 sc1\n" \
  "s_add_u32 %[sa], %[sa], 4096\n" \
  "buffer_load_dwordx4 v[80:83], %[va0], %[rs], %[sa] offen offset:0 sc1\n" \
  "buffer_load_dwordx4 v[84:87], %[va0], %[rs], %[sa] offen offset:1024 sc1\n" \
  "v_add_u32 %[vw], 98304, %[vw]\n" \
  "ds_read_b128 v[96:99], %[vw] offset:24576\n" \
  "ds_read_b128 v[100:103], %[vw] offset:25600\n" \
  "ds_read_b128 v[104:107], %[vw] offset:26624\n" \
  "ds_read_b128 v[108:111], %[vw] offset:27648\n" \
  "ds_read_b128 v[112:115], %[vw] offset:28672\n" \
  "ds_read_b128 v[116:119], %[vw] offset:34816\n" \
  "ds_read_b128 v[120:123], %[vw] offset:35840\n" \
  "ds_read_b128 v[124:127], %[vw] offset:36864\n" \
  "s_waitcnt vmcnt(5)\n" \
  "ds_read_b128 v[128:131], %[vw] offset:37888\n" \
  "s_waitcnt lgkmcnt(8)\n" \
  "v_mfma_f32_16x16x32_bf16 %[c0], v[96:99], v[64:67], %[c0]\n" \
  "buffer_load_dwordx4 v[88:91], %[va0], %[rs], %[sa] offen offset:2048 sc1\n" \
  "ds_read_b128 v[132:135], %[vw] offset:38912\n" \
  "s_waitcnt lgkmcnt(8)\n" \
  "v_mfma_f32_16x16x32_bf16 %[c1], v[100:103], v[64:67], %[c1]\n" \
  "ds_read_b128 v[96:99], %[vw] offset:45056\n" \
  "s_waitcnt lgkmcnt(8)\n" \
  "v_mfma_f32_16x16x32_bf16 %[c2], v[104:107], v[64:67], 0\n" \
  "ds_read_b128 v[100:103], %[vw] offset:46080\n" \
  "s_waitcnt lgkmcnt(8)\n" \
  "v_mfma_f32_16x16x32_bf16 %[c3], v[108:111], v[64:67], 0\n" \
  "ds_read_b128 v[104:107], %[vw] offset:47104\n" \
  "s_waitcnt lgkmcnt(8)\n" \
  "v_mfma_f32_16x16x32_bf16 %[c4], v[112:115], v[64:67], %[c4]\n" \
  "s_waitcnt vmcnt(5)\n" \
  "ds_read_b128 v[108:111], %[vw] offset:48128\n" \
  "s_waitcnt lgkmcnt(8)\n" \
  "v_mfma_f32_16x16x32_bf16 %[c0], v[116:119], v[68:71], %[c0]\n" \
  "buffer_load_dwordx4 v[92:95], %[va0], %[rs], %[sa] offen offset:3072 sc1\n" \
  "ds_read_b128 v[112:115], %[vw] offset:49152\n" \
  "s_waitcnt lgkmcnt(8)\n" \
  "v_mfma_f32_16x16x32_bf16 %[c1], v[120:123], v[68:71], %[c1]\n" \
  "v_add_u32 %[vw], 4294868992, %[vw]\n" \
  "ds_read_b128 v[116:119], %[vw] offset:0\n" \
  "s_waitcnt lgkmcnt(8)\n" \
  "v_mfma_f32_16x16x32_bf16 %[c2], v[124:127], v[68:71], %[c2]\n" \
  "ds_read_b128 v[120:123], %[vw] offset:1024\n" \
  "s_waitcnt lgkmcnt(8)\n" \
  "v_mfma_f32_16x16x32_bf16 %[c3], v[128:131], v[68:71], %[c3]\n" \
  "ds_read_b128 v[124:127], %[vw] offset:2048\n" \
  "s_waitcnt lgkmcnt(8)\n" \
  "v_mfma_f32_16x16x32_bf16 %[c4], v[132:135], v[68:71], %[c4]\n" \
  "s_waitcnt vmcnt(5)\n" \
  "ds_read_b128 v[128:131], %[vw] offset:3072\n" \
  "s_waitcnt lgkmcnt(8)\n" \
  "v_mfma_f32_16x16x32_bf16 %[c0], v[96:99], v[72:75], %[c0]\n" \
  "s_add_u32 %[sa], %[sa], 4096\n" \
  "buffer_load_dwordx4 v[64:67], %[va0], %[rs], %[sa] offen offset:0 sc1\n" \
  "ds_read_b128 v[132:135], %[vw] offset:4096\n" \
  "s_waitcnt lgkmcnt(8)\n" \
  "v_mfma_f32_16x16x32_bf16 %[c1], v[100:103], v[72:75], %[c1]\n" \
  "ds_read_b128 v[96:99], %[vw] offset:10240\n" \
  "s_waitcnt lgkmcnt(8)\n" \
  "v_mfma_f32_16x16x32_bf16 %[c2], v[104:107], v[72:75], %[c2]\n" \
  "ds_read_b128 v[100:103], %[vw] offset:11264\n" \
  "s_waitcnt lgkmcnt(8)\n" \
  "v_mfma_f32_16x16x32_bf16 %[c3], v[108:111], v[72:75], %[c3]\n" \
  "ds_read_b128 v[104:107], %[vw] offset:12288\n" \
  "s_waitcnt lgkmcnt(8)\n" \
  "v_mfma_f32_16x16x32_bf16 %[c4], v[112:115], v[72:75], %[c4]\n" \
  "s_waitcnt vmcnt(5)\n" \
  "ds_read_b128 v[108:111], %[vw] offset:13312\n" \
  "s_waitcnt lgkmcnt(8)\n" \
  "v_mfma_f32_16x16x32_bf16 %[c0], v[116:119], v[76:79], %[c0]\n" \
  "buffer_load_dwordx4 v[68:71], %[va0], %[rs], %[sa] offen offset:1024 sc1\n" \
  "ds_read_b128 v[112:115], %[vw] offset:14336\n" \
  "s_waitcnt lgkmcnt(8)\n" \
  "v_mfma_f32_16x16x32_bf16 %[c1], v[120:123], v[76:79], %[c1]\n" \
  "ds_read_b128 v[116:119], %[vw] offset:20480\n" \
  "s_waitcnt lgkmcnt(8)\n" \
  "v_mfma_f32_16x16x32_bf16 %[c2], v[124:127], v[76:79], %[c2]\n" \
  "ds_read_b128 v[120:123], %[vw] offset:21504\n" \
  "s_waitcnt lgkmcnt(8)\n" \
  "v_mfma_f32_16x16x32_bf16 %[c3], v[128:131], v[76:79], %[c3]\n" \
  "ds_read_b128 v[124:127], %[vw] offset:22528\n" \
  "s_waitcnt lgkmcnt(8)\n" \
  "v_mfma_f32_16x16x32_bf16 %[c4], v[132:135], v[76:79], %[c4]\n" \
  "s_barrier\n" \
  "s_waitcnt vmcnt(5)\n" \
  "ds_read_b128 v[128:131], %[vw] offset:23552\n" \
  "s_waitcnt lgkmcnt(8)\n" \
  "v_mfma_f32_16x16x32_bf16 %[c0], v[96:99], v[80:83], %[c0]\n" \
  "s_mov_b32 m0, %[dm0]\n" \
  "s_nop 0\n" \
  "buffer_load_dwordx4 %[l16], %[rs], %[ds0] offen offset:0 sc1 lds\n" \
  "buffer_load_dwordx4 v[72:75], %[va0], %[rs], %[sa] offen offset:2048 sc1\n" \
  "ds_read_b128 v[132:135], %[vw] offset:24576\n" \
  "s_waitcnt lgkmcnt(8)\n" \
  "v_mfma_f32_16x16x32_bf16 %[c1], v[100:103], v[80:83], %[c1]\n" \
  "ds_read_b128 v[96:99], %[vw] offset:30720\n" \
  "s_waitcnt lgkmcnt(8)\n" \
  "v_mfma_f32_16x16x32_bf16 %[c2], v[104:107], v[80:83], %[c2]\n" \
  "ds_read_b128 v[100:103], %[vw] offset:31744\n" \
  "s_waitcnt lgkmcnt(8)\n" \
  "v_mfma_f32_16x16x32_bf16 %[c3], v[108:111], v[80:83], %[c3]\n" \
  "ds_read_b128 v[104:107], %[vw] offset:32768\n" \
  "s_waitcnt lgkmcnt(8)\n" \
  "v_mfma_f32_16x16x32_bf16 %[c4], v[112:115], v[80:83], %[c4]\n" \
  "s_waitcnt vmcnt(6)\n" \
  "ds_read_b128 v[108:111], %[vw] offset:33792\n" \
  "s_waitcnt lgkmcnt(8)\n" \
  "v_mfma_f32_16x16x32_bf16 %[c0], v[116:119], v[84:87], %[c0]\n" \
  "buffer_load_dwordx4 %[l16], %[rs], %[ds0] offen offset:1024 sc1 lds\n" \
  "buffer_load_dwordx4 v[76:79], %[va0], %[rs], %[sa] offen offset:3072 sc1\n" \
  "ds_read_b128 v[112:115], %[vw] offset:34816\n" \
  "s_waitcnt lgkmcnt(8)\n" \
  "v_mfma_f32_16x16x32_bf16 %[c1], v[120:123], v[84:87], %[c1]\n" \
  "ds_read_b128 v[116:119], %[vw] offset:40960\n" \
  "s_waitcnt lgkmcnt(8)\n" \
  "v_mfma_f32_16x16x32_bf16 %[c2], v[124:127], v[84:87], %[c2]\n" \
  "ds_read_b128 v[120:123], %[vw] offset:41984\n" \
  "s_waitcnt lgkmcnt(8)\n" \
  "v_mfma_f32_16x16x32_bf16 %[c3], v[128:131], v[84:87], %[c3]\n" \
  "ds_read_b128 v[124:127], %[vw] offset:43008\n" \
  "s_waitcnt lgkmcnt(8)\n" \
  "v_mfma_f32_16x16x32_bf16 %[c4], v[132:135], v[84:87], %[c4]\n" \
  "s_waitcnt vmcnt(7)\n" \
  "ds_read_b128 v[128:131], %[vw] offset:44032\n" \
  "s_waitcnt lgkmcnt(8)\n" \
  "v_mfma_f32_16x16x32_bf16 %[c0], v[96:99], v[88:91], %[c0]\n" \
  "buffer_load_dwordx4 %[l16], %[rs], %[ds0] offen offset:2048 sc1 lds\n" \
  "s_add_u32 %[sa], %[sa], 4096\n" \
  "buffer_load_dwordx4 v[80:83], %[va0], %[rs], %[sa] offen offset:0 sc1\n" \
  "ds_read_b128 v[132:135], %[vw] offset:45056\n" \
  "s_waitcnt lgkmcnt(8)\n" \
  "v_mfma_f32_16x16x32_bf16 %[c1], v[100:103], v[88:91], %[c1]\n" \
  "ds_read_b128 v[96:99], %[vw] offset:51200\n" \
  "s_waitcnt lgkmcnt(8)\n" \
  "v_mfma_f32_16x16x32_bf16 %[c2], v[104:107], v[88:91], %[c2]\n" \
  "ds_read_b128 v[100:103], %[vw] offset:52224\n" \
  "s_waitcnt lgkmcnt(8)\n" \
  "v_mfma_f32_16x16x32_bf16 %[c3], v[108:111], v[88:91], %[c3]\n" \
  "ds_read_b128 v[104:107], %[vw] offset:53248\n" \
  "s_waitcnt lgkmcnt(8)\n" \
  "v_mfma_f32_16x16x32_bf16 %[c4], v[112:115], v[88:91], %[c4]\n" \
  "s_waitcnt vmcnt(8)\n" \
  "ds_read_b128 v[108:111], %[vw] offset:54272\n" \
  "s_waitcnt lgkmcnt(8)\n" \
  "v_mfma_f32_16x16x32_bf16 %[c0], v[116:119], v[92:95], %[c0]\n" \
  "buffer_load_dwordx4 %[l16], %[rs], %[ds0] offen offset:3072 sc1 lds\n" \
  "buffer_load_dwordx4 v[84:87], %[va0], %[rs], %[sa] offen offset:1024 sc1\n" \
  "ds_read_b128 v[112:115], %[vw] offset:55296\n" \
  "s_waitcnt lgkmcnt(8)\n" \
  "v_mfma_f32_16x16x32_bf16 %[c1], v[120:123], v[92:95], %[c1]\n" \
  "ds_read_b128 v[116:119], %[vw] offset:61440\n" \
  "s_waitcnt lgkmcnt(8)\n" \
  "v_mfma_f32_16x16x32_bf16 %[c2], v[124:127], v[92:95], %[c2]\n" \
  "ds_read_b128 v[120:123], %[vw] offset:62464\n" \
  "s_waitcnt lgkmcnt(8)\n" \
  "v_mfma_f32_16x16x32_bf16 %[c3], v[128:131], v[92:95], %[c3]\n" \
  "ds_read_b128 v[124:127], %[vw] offset:63488\n" \
  "s_waitcnt lgkmcnt(8)\n" \
  "v_mfma_f32_16x16x32_bf16 %[c4], v[132:135], v[92:95], %[c4]\n" \
  "s_waitcnt vmcnt(9)\n" \
  "ds_read_b128 v[128:131], %[vw] offset:64512\n" \
  "s_waitcnt lgkmcnt(8)\n" \
  "v_mfma_f32_16x16x32_bf16 %[c0], v[96:99], v[64:67], %[c0]\n" \
  "s_add_u32 %[dm0], %[dm0], 4096\n" \
  "s_add_u32 %[ds0], %[ds0], 4096\n" \
  "s_mov_b32 m0, %[dm0]\n" \
  "s_nop 0\n" \
  "buffer_load_dwordx4 %[l16], %[rs], %[ds0] offen offset:0 sc1 lds\n" \
  "buffer_load_dwordx4 v[88:91], %[va0], %[rs], %[sa] offen offset:2048 sc1\n" \
  "v_add_u32 %[vw], 65536, %[vw]\n" \
  "ds_read_b128 v[132:135], %[vw] offset:0\n" \
  "s_waitcnt lgkmcnt(8)\n" \
  "v_mfma_f32_16x16x32_bf16 %[c1], v[100:103], v[64:67], %[c1]\n" \
  "ds_read_b128 v[96:99], %[vw] offset:6144\n" \
  "s_waitcnt lgkmcnt(8)\n" \
  "v_mfma_f32_16x16x32_bf16 %[c2], v[104:107], v[64:67], %[c2]\n" \
  "ds_read_b128 v[100:103], %[vw] offset:7168\n" \
  "s_waitcnt lgkmcnt(8)\n" \
  "v_mfma_f32_16x16x32_bf16 %[c3], v[108:111], v[64:67], %[c3]\n" \
  "ds_read_b128 v[104:107], %[vw] offset:8192\n" \
  "s_waitcnt lgkmcnt(8)\n" \
  "v_mfma_f32_16x16x32_bf16 %[c4], v[112:115], v[64:67], %[c4]\n" \
  "s_waitcnt vmcnt(10)\n" \
  "ds_read_b128 v[108:111], %[vw] offset:9216\n" \
  "s_waitcnt lgkmcnt(8)\n" \
  "v_mfma_f32_16x16x32_bf16 %[c0], v[116:119], v[68:71], %[c0]\n" \
  "buffer_load_dwordx4 v[92:95], %[va0], %[rs], %[sa] offen offset:3072 sc1\n" \
  "ds_read_b128 v[112:115], %[vw] offset:10240\n" \
  "s_waitcnt lgkmcnt(8)\n" \
  "v_mfma_f32_16x16x32_bf16 %[c1], v[120:123], v[68:71], %[c1]\n" \
  "ds_read_b128 v[116:119], %[vw] offset:16384\n" \
  "s_waitcnt lgkmcnt(8)\n" \
  "v_mfma_f32_16x16x32_bf16 %[c2], v[124:127], v[68:71], %[c2]\n" \
  "ds_read_b128 v[120:123], %[vw] offset:17408\n" \
  "s_waitcnt lgkmcnt(8)\n" \
  "v_mfma_f32_16x16x32_bf16 %[c3], v[128:131], v[68:71], %[c3]\n" \
  "ds_read_b128 v[124:127], %[vw] offset:18432\n" \
  "s_waitcnt lgkmcnt(8)\n" \
  "v_mfma_f32_16x16x32_bf16 %[c4], v[132:135], v[68:71], %[c4]\n" \
  "s_barrier\n" \
  "s_waitcnt vmcnt(9)\n" \
  "ds_read_b128 v[128:131], %[vw] offset:19456\n" \
  "s_waitcnt lgkmcnt(8)\n" \
  "v_mfma_f32_16x16x32_bf16 %[c0], v[96:99], v[72:75], %[c0]\n" \
  "s_mov_b32 m0, %[dm1]\n" \
  "s_nop 0\n" \
  "buffer_load_dwordx4 %[l16], %[rs], %[ds1] offen offset:0 sc1 lds\n" \
  "s_add_u32 %[sa], %[sa], 4096\n" \
  "buffer_load_dwordx4 v[64:67], %[va0], %[rs], %[sa] offen offset:0 sc1\n" \
  "ds_read_b128 v[132:135], %[vw] offset:20480\n" \
  "s_waitcnt lgkmcnt(8)\n" \
  "v_mfma_f32_16x16x32_bf16 %[c1], v[100:103], v[72:75], %[c1]\n" \
  "ds_read_b128 v[96:99], %[vw] offset:26624\n" \
  "s_waitcnt lgkmcnt(8)\n" \
  "v_mfma_f32_16x16x32_bf16 %[c2], v[104:107], v[72:75], %[c2]\n" \
  "buffer_load_dwordx4 %[l16], %[rs], %[ds1] offen offset:1024 sc1 lds\n" \
  "ds_read_b128 v[100:103], %[vw] offset:27648\n" \
  "s_waitcnt lgkmcnt(8)\n" \
  "v_mfma_f32_16x16x32_bf16 %[c3], v[108:111], v[72:75], %[c3]\n" \
  "ds_read_b128 v[104:107], %[vw] offset:28672\n" \
  "s_waitcnt lgkmcnt(8)\n" \
  "v_mfma_f32_16x16x32_bf16 %[c4], v[112:115], v[72:75], %[c4]\n" \
  "buffer_load_dwordx4 %[l16], %[rs], %[ds1] offen offset:2048 sc1 lds\n" \
  "s_waitcnt vmcnt(11)\n" \
  "ds_read_b128 v[108:111], %[vw] offset:29696\n" \
  "s_waitcnt lgkmcnt(8)\n" \
  "v_mfma_f32_16x16x32_bf16 %[c0], v[116:119], v[76:79], %[c0]\n" \
  "buffer_load_dwordx4 v[68:71], %[va0], %[rs], %[sa] offen offset:1024 sc1\n" \
  "ds_read_b128 v[112:115], %[vw] offset:30720\n" \
  "s_waitcnt lgkmcnt(8)\n" \
  "v_mfma_f32_16x16x32_bf16 %[c1], v[120:123], v[76:79], %[c1]\n" \
  "buffer_load_dwordx4 %[l16], %[rs], %[ds1] offen offset:3072 sc1 lds\n" \
  "ds_read_b128 v[116:119], %[vw] offset:36864\n" \
  "s_waitcnt lgkmcnt(8)\n" \
  "v_mfma_f32_16x16x32_bf16 %[c2], v[124:127], v[76:79], %[c2]\n" \
  "ds_read_b128 v[120:123], %[vw] offset:37888\n" \
  "s_waitcnt lgkmcnt(8)\n" \
  "v_mfma_f32_16x16x32_bf16 %[c3], v[128:131], v[76:79], %[c3]\n" \
  "s_add_u32 %[dm1], %[dm1], 4096\n" \
  "s_add_u32 %[ds1], %[ds1], 4096\n" \
  "s_mov_b32 m0, %[dm1]\n" \
  "s_nop 0\n" \
  "buffer_load_dwordx4 %[l16], %[rs], %[ds1] offen offset:0 sc1 lds\n" \
  "ds_read_b128 v[124:127], %[vw] offset:38912\n" \
  "s_waitcnt lgkmcnt(8)\n" \
  "v_mfma_f32_16x16x32_bf16 %[c4], v[132:135], v[76:79], %[c4]\n" \
  "s_waitcnt vmcnt(12)\n" \
  "ds_read_b128 v[128:131], %[vw] offset:39936\n" \
  "s_waitcnt lgkmcnt(8)\n" \
  "v_mfma_f32_16x16x32_bf16 %[c0], v[96:99], v[80:83], %[c0]\n" \
  "buffer_load_dwordx4 %[l16], %[rs], %[ds1] offen offset:1024 sc1 lds\n" \
  "buffer_load_dwordx4 v[72:75], %[va0], %[rs], %[sa] offen offset:2048 sc1\n" \
  "ds_read_b128 v[132:135], %[vw] offset:40960\n" \
  "s_waitcnt lgkmcnt(8)\n" \
  "v_mfma_f32_16x16x32_bf16 %[c1], v[100:103], v[80:83], %[c1]\n" \
  "ds_read_b128 v[96:99], %[vw] offset:47104\n" \
  "s_waitcnt lgkmcnt(8)\n" \
  "v_mfma_f32_16x16x32_bf16 %[c2], v[104:107], v[80:83], %[c2]\n" \
  "buffer_load_dwordx4 %[l16], %[rs], %[ds1] offen offset:2048 sc1 lds\n" \
  "ds_read_b128 v[100:103], %[vw] offset:48128\n" \
  "s_waitcnt lgkmcnt(8)\n" \
  "v_mfma_f32_16x16x32_bf16 %[c3], v[108:111], v[80:83], %[c3]\n" \
  "ds_read_b128 v[104:107], %[vw] offset:49152\n" \
  "s_waitcnt lgkmcnt(8)\n" \
  "v_mfma_f32_16x16x32_bf16 %[c4], v[112:115], v[80:83], %[c4]\n" \
  "s_waitcnt vmcnt(13)\n" \
  "ds_read_b128 v[108:111], %[vw] offset:50176\n" \
  "s_waitcnt lgkmcnt(8)\n" \
  "v_mfma_f32_16x16x32_bf16 %[c0], v[116:119], v[84:87], %[c0]\n" \
  "buffer_load_dwordx4 v[76:79], %[va0], %[rs], %[sa] offen offset:3072 sc1\n" \
  "ds_read_b128 v[112:115], %[vw] offset:51200\n" \
  "s_waitcnt lgkmcnt(8)\n" \
  "v_mfma_f32_16x16x32_bf16 %[c1], v[120:123], v[84:87], %[c1]\n" \
  "s_waitcnt vmcnt(13)\n" \
  "s_barrier\n" \
  "ds_read_b128 v[116:119], %[vw] offset:57344\n" \
  "s_waitcnt lgkmcnt(8)\n" \
  "v_mfma_f32_16x16x32_bf16 %[c2], v[124:127], v[84:87], %[c2]\n" \
  "ds_read_b128 v[120:123], %[vw] offset:58368\n" \
  "s_waitcnt lgkmcnt(8)\n" \
  "v_mfma_f32_16x16x32_bf16 %[c3], v[128:131], v[84:87], %[c3]\n" \
  "ds_read_b128 v[124:127], %[vw] offset:59392\n" \
  "s_waitcnt lgkmcnt(8)\n" \
  "v_mfma_f32_16x16x32_bf16 %[c4], v[132:135], v[84:87], %[c4]\n" \
  "s_waitcnt vmcnt(12)\n" \
  "ds_read_b128 v[128:131], %[vw] offset:60416\n" \
  "s_waitcnt lgkmcnt(8)\n" \
  "v_mfma_f32_16x16x32_bf16 %[c0], v[96:99], v[88:91], %[c0]\n" \
  "s_add_u32 %[sa], %[sa], 4096\n" \
  "buffer_load_dwordx4 v[80:83], %[va0], %[rs], %[sa] offen offset:0 sc1\n" \
  "ds_read_b128 v[132:135], %[vw] offset:61440\n" \
  "s_waitcnt lgkmcnt(8)\n" \
  "v_mfma_f32_16x16x32_bf16 %[c1], v[100:103], v[88:91], %[c1]\n" \
  "v_add_u32 %[vw], 65536, %[vw]\n" \
  "ds_read_b128 v[96:99], %[vw] offset:2048\n" \
  "s_waitcnt lgkmcnt(8)\n" \
  "v_mfma_f32_16x16x32_bf16 %[c2], v[104:107], v[88:91], %[c2]\n" \
  "ds_read_b128 v[100:103], %[vw] offset:3072\n" \
  "s_waitcnt lgkmcnt(8)\n" \
  "v_mfma_f32_16x16x32_bf16 %[c3], v[108:111], v[88:91], %[c3]\n" \
  "ds_read_b128 v[104:107], %[vw] offset:4096\n" \
  "s_waitcnt lgkmcnt(8)\n" \
  "v_mfma_f32_16x16x32_bf16 %[c4], v[112:115], v[88:91], %[c4]\n" \
  "s_waitcnt vmcnt(12)\n" \
  "ds_read_b128 v[108:111], %[vw] offset:5120\n" \
  "s_waitcnt lgkmcnt(8)\n" \
  "v_mfma_f32_16x16x32_bf16 %[c0], v[116:119], v[92:95], %[c0]\n" \
  "buffer_load_dwordx4 v[84:87], %[va0], %[rs], %[sa] offen offset:1024 sc1\n" \
  "ds_read_b128 v[112:115], %[vw] offset:6144\n" \
  "s_waitcnt lgkmcnt(8)\n" \
  "v_mfma_f32_16x16x32_bf16 %[c1], v[120:123], v[92:95], %[c1]\n" \
  "ds_read_b128 v[116:119], %[vw] offset:12288\n" \
  "s_waitcnt lgkmcnt(8)\n" \
  "v_mfma_f32_16x16x32_bf16 %[c2], v[124:127], v[92:95], %[c2]\n" \
  "ds_read_b128 v[120:123], %[vw] offset:13312\n" \
  "s_waitcnt lgkmcnt(8)\n" \
  "v_mfma_f32_16x16x32_bf16 %[c3], v[128:131], v[92:95], %[c3]\n" \
  "ds_read_b128 v[124:127], %[vw] offset:14336\n" \
  "s_waitcnt lgkmcnt(8)\n" \
  "v_mfma_f32_16x16x32_bf16 %[c4], v[132:135], v[92:95], %[c4]\n" \
  "s_waitcnt vmcnt(11)\n" \
  "ds_read_b128 v[128:131], %[vw] offset:15360\n" \
  "s_waitcnt lgkmcnt(8)\n" \
  "v_mfma_f32_16x16x32_bf16 %[c0], v[96:99], v[64:67], %[c0]\n" \
  "buffer_load_dwordx4 v[88:91], %[va0], %[rs], %[sa] offen offset:2048 sc1\n" \
  "ds_read_b128 v[132:135], %[vw] offset:16384\n" \
  "s_waitcnt lgkmcnt(8)\n" \
  "v_mfma_f32_16x16x32_bf16 %[c1], v[100:103], v[64:67], %[c1]\n" \
  "v_add_u32 %[vw], 4294836224, %[vw]\n" \
  "ds_read_b128 v[96:99], %[vw] offset:0\n" \
  "s_waitcnt lgkmcnt(8)\n" \
  "v_mfma_f32_16x16x32_bf16 %[c2], v[104:107], v[64:67], %[c2]\n" \
  "ds_read_b128 v[100:103], %[vw] offset:1024\n" \
  "s_waitcnt lgkmcnt(8)\n" \
  "v_mfma_f32_16x16x32_bf16 %[c3], v[108:111], v[64:67], %[c3]\n" \
  "ds_read_b128 v[104:107], %[vw] offset:2048\n" \
  "s_waitcnt lgkmcnt(8)\n" \
  "v_mfma_f32_16x16x32_bf16 %[c4], v[112:115], v[64:67], %[c4]\n" \
  "s_waitcnt vmcnt(9)\n" \
  "ds_read_b128 v[108:111], %[vw] offset:3072\n" \
  "s_waitcnt lgkmcnt(8)\n" \
  "v_mfma_f32_16x16x32_bf16 %[c0], v[116:119], v[68:71], %[c0]\n" \
  "buffer_load_dwordx4 v[92:95], %[va0], %[rs], %[sa] offen offset:3072 sc1\n" \
  "ds_read_b128 v[112:115], %[vw] offset:4096\n" \
  "s_waitcnt lgkmcnt(8)\n" \
  "v_mfma_f32_16x16x32_bf16 %[c1], v[120:123], v[68:71], %[c1]\n" \
  "s_waitcnt vmcnt(5)\n" \
  "s_barrier\n" \
  "ds_read_b128 v[116:119], %[vw] offset:10240\n" \
  "s_waitcnt lgkmcnt(8)\n" \
  "v_mfma_f32_16x16x32_bf16 %[c2], v[124:127], v[68:71], %[c2]\n" \
  "ds_read_b128 v[120:123], %[vw] offset:11264\n" \
  "s_waitcnt lgkmcnt(8)\n" \
  "v_mfma_f32_16x16x32_bf16 %[c3], v[128:131], v[68:71], %[c3]\n" \
  "ds_read_b128 v[124:127], %[vw] offset:12288\n" \
  "s_waitcnt lgkmcnt(8)\n" \
  "v_mfma_f32_16x16x32_bf16 %[c4], v[132:135], v[68:71], %[c4]\n" \
  "s_waitcnt vmcnt(6)\n" \
  "ds_read_b128 v[128:131], %[vw] offset:13312\n" \
  "s_waitcnt lgkmcnt(8)\n" \
  "v_mfma_f32_16x16x32_bf16 %[c0], v[96:99], v[72:75], %[c0]\n" \
  "ds_read_b128 v[132:135], %[vw] offset:14336\n" \
  "s_waitcnt lgkmcnt(8)\n" \
  "v_mfma_f32_16x16x32_bf16 %[c1], v[100:103], v[72:75], %[c1]\n" \
  "ds_read_b128 v[96:99], %[vw] offset:20480\n" \
  "s_waitcnt lgkmcnt(8)\n" \
  "v_mfma_f32_16x16x32_bf16 %[c2], v[104:107], v[72:75], %[c2]\n" \
  "ds_read_b128 v[100:103], %[vw] offset:21504\n" \
  "s_waitcnt lgkmcnt(8)\n" \
  "v_mfma_f32_16x16x32_bf16 %[c3], v[108:111], v[72:75], %[c3]\n" \
  "ds_read_b128 v[104:107], %[vw] offset:22528\n" \
  "s_waitcnt lgkmcnt(8)\n" \
  "v_mfma_f32_16x16x32_bf16 %[c4], v[112:115], v[72:75], %[c4]\n" \
  "s_waitcnt vmcnt(4)\n" \
  "ds_read_b128 v[108:111], %[vw] offset:23552\n" \
  "s_waitcnt lgkmcnt(8)\n" \
  "v_mfma_f32_16x16x32_bf16 %[c0], v[116:119], v[76:79], %[c0]\n" \
  "ds_read_b128 v[112:115], %[vw] offset:24576\n" \
  "s_waitcnt lgkmcnt(8)\n" \
  "v_mfma_f32_16x16x32_bf16 %[c1], v[120:123], v[76:79], %[c1]\n" \
  "ds_read_b128 v[116:119], %[vw] offset:30720\n" \
  "s_waitcnt lgkmcnt(8)\n" \
  "v_mfma_f32_16x16x32_bf16 %[c2], v[124:127], v[76:79], %[c2]\n" \
  "ds_read_b128 v[120:123], %[vw] offset:31744\n" \
  "s_waitcnt lgkmcnt(8)\n" \
  "v_mfma_f32_16x16x32_bf16 %[c3], v[128:131], v[76:79], %[c3]\n" \
  "ds_read_b128 v[124:127], %[vw] offset:32768\n" \
  "s_waitcnt lgkmcnt(8)\n" \
  "v_mfma_f32_16x16x32_bf16 %[c4], v[132:135], v[76:79], %[c4]\n" \
  "s_waitcnt vmcnt(3)\n" \
  "ds_read_b128 v[128:131], %[vw] offset:33792\n" \
  "s_waitcnt lgkmcnt(8)\n" \
  "v_mfma_f32_16x16x32_bf16 %[c0], v[96:99], v[80:83], %[c0]\n" \
  "ds_read_b128 v[132:135], %[vw] offset:34816\n" \
  "s_waitcnt lgkmcnt(8)\n" \
  "v_mfma_f32_16x16x32_bf16 %[c1], v[100:103], v[80:83], %[c1]\n" \
  "ds_read_b128 v[96:99], %[vw] offset:40960\n" \
  "s_waitcnt lgkmcnt(8)\n" \
  "v_mfma_f32_16x16x32_bf16 %[c2], v[104:107], v[80:83], %[c2]\n" \
  "ds_read_b128 v[100:103], %[vw] offset:41984\n" \
  "s_waitcnt lgkmcnt(8)\n" \
  "v_mfma_f32_16x16x32_bf16 %[c3], v[108:111], v[80:83], %[c3]\n" \
  "ds_read_b128 v[104:107], %[vw] offset:43008\n" \
  "s_waitcnt lgkmcnt(8)\n" \
  "v_mfma_f32_16x16x32_bf16 %[c4], v[112:115], v[80:83], %[c4]\n" \
  "s_waitcnt vmcnt(2)\n" \
  "ds_read_b128 v[108:111], %[vw] offset:44032\n" \
  "s_waitcnt lgkmcnt(8)\n" \
  "v_mfma_f32_16x16x32_bf16 %[c0], v[116:119], v[84:87], %[c0]\n" \
  "ds_read_b128 v[112:115], %[vw] offset:45056\n" \
  "s_waitcnt lgkmcnt(8)\n" \
  "v_mfma_f32_16x16x32_bf16 %[c1], v[120:123], v[84:87], %[c1]\n" \
  "ds_read_b128 v[116:119], %[vw] offset:51200\n" \
  "s_waitcnt lgkmcnt(8)\n" \
  "v_mfma_f32_16x16x32_bf16 %[c2], v[124:127], v[84:87], %[c2]\n" \
  "ds_read_b128 v[120:123], %[vw] offset:52224\n" \
  "s_waitcnt lgkmcnt(8)\n" \
  "v_mfma_f32_16x16x32_bf16 %[c3], v[128:131], v[84:87], %[c3]\n" \
  "ds_read_b128 v[124:127], %[vw] offset:53248\n" \
  "s_waitcnt lgkmcnt(8)\n" \
  "v_mfma_f32_16x16x32_bf16 %[c4], v[132:135], v[84:87], %[c4]\n" \
  "s_waitcnt vmcnt(1)\n" \
  "ds_read_b128 v[128:131], %[vw] offset:54272\n" \
  "s_waitcnt lgkmcnt(8)\n" \
  "v_mfma_f32_16x16x32_bf16 %[c0], v[96:99], v[88:91], %[c0]\n" \
  "ds_read_b128 v[132:135], %[vw] offset:55296\n" \
  "s_waitcnt lgkmcnt(8)\n" \
  "v_mfma_f32_16x16x32_bf16 %[c1], v[100:103], v[88:91], %[c1]\n" \
  "s_waitcnt lgkmcnt(7)\n" \
  "v_mfma_f32_16x16x32_bf16 %[c2], v[104:107], v[88:91], %[c2]\n" \
  "s_waitcnt lgkmcnt(6)\n" \
  "v_mfma_f32_16x16x32_bf16 %[c3], v[108:111], v[88:91], %[c3]\n" \
  "s_waitcnt lgkmcnt(5)\n" \
  "v_mfma_f32_16x16x32_bf16 %[c4], v[112:115], v[88:91], %[c4]\n" \
  "s_waitcnt vmcnt(0)\n" \
  "s_waitcnt lgkmcnt(4)\n" \
  "v_mfma_f32_16x16x32_bf16 %[c0], v[116:119], v[92:95], %[c0]\n" \
  "s_waitcnt lgkmcnt(3)\n" \
  "v_mfma_f32_16x16x32_bf16 %[c1], v[120:123], v[92:95], %[c1]\n" \
  "s_waitcnt lgkmcnt(2)\n" \
  "v_mfma_f32_16x16x32_bf16 %[c2], v[124:127], v[92:95], %[c2]\n" \
  "s_waitcnt lgkmcnt(1)\n" \
  "v_mfma_f32_16x16x32_bf16 %[c3], v[128:131], v[92:95], %[c3]\n" \
  "s_waitcnt lgkmcnt(0)\n" \
  "v_mfma_f32_16x16x32_bf16 %[c4], v[132:135], v[92:95], %[c4]\n" \
  "s_nop 7\n" \
  ""
#define LN_CLOB "v144", "v145", "v146", "v147", "v148", "v149", "v150", "v151", "v155"

constexpr int S = 2048, D = 640, F = 1536;
constexpr float EPS = 1e-5f;
constexpr unsigned OOB = 0x80000000u;
constexpr int FLG_OFF = 4096;
constexpr int CNT_OFF = FLG_OFF + 2 * 256 * 256;
constexpr int XST_OFF = CNT_OFF + 16 * 8 * 256;
constexpr int HST_OFF = XST_OFF + S * 16;
constexpr int X1ST_OFF = HST_OFF + 128 * 2048;
constexpr int CS1_OFF = X1ST_OFF + 128 * 2048;
constexpr int CS3_OFF = CS1_OFF + 20 * D * 4;
constexpr int CSQ_OFF = CS3_OFF + 20 * F * 4;
constexpr int QREC = 2560, KVREC = 5632;
constexpr int Q_OFF = CSQ_OFF + 3 * D * 8;
constexpr int KV_OFF = Q_OFF + 16 * 64 * QREC;
constexpr int O_OFF = KV_OFF + (16 * 64 + 2) * KVREC;
constexpr int H_OFF = O_OFF + S * D * 2;
constexpr int G_OFF = H_OFF + S * D * 2;
constexpr int X1_OFF = G_OFF + S * D * 2;
constexpr int U_OFF = X1_OFF + S * D * 2;
constexpr int WG_OFF = U_OFF + S * F * 2;
constexpr int WG_STR = 548 * 1024;
constexpr int W3_OFF = WG_OFF + 8 * WG_STR;
constexpr int DBG_OFF = W3_OFF + 16 * 120 * 1024;
constexpr int WS_END = DBG_OFF + 256 * 8 * 128;
enum { FK_P1 = 0, FK_R = 1 };
enum { CK_P2 = 0, CK_P3 = 1, CK_P4 = 3, CK_P5 = 5, CK_P6 = 6 };
constexpr int LDS_BYTES = 163840;
constexpr int RING = 150;
constexpr int MISC = RING * 1024;
constexpr int L_CS1 = MISC, L_CS3 = MISC + 384, L_VOTE = L_CS3 + 384, L_MAX = L_VOTE + 64;
constexpr unsigned SPIN_CAP = 1u << 17;

struct Params {
  const u16 *x, *wqkv, *wo, *w1, *w2, *w3, *w4;
  u16* out;
  unsigned char* ws;
};

DEV rsrc_t mk_rsrc(const void* p, int bytes) { return __builtin_amdgcn_make_buffer_rsrc((void*)p, 0, bytes, 0x00020000); }
DEV float bflo(unsigned v) { return __uint_as_float(v << 16); }
DEV float bfhi(unsigned v) { return __uint_as_float(v & 0xffff0000u); }
DEV unsigned pk2(float a, float b) {
  bf16x2 r = {(__bf16)a, (__bf16)b};
  return __builtin_bit_cast(unsigned, r);
}
DEV float quadsum(float v) {
  auto a = __builtin_amdgcn_permlane16_swap(__float_as_uint(v), __float_as_uint(v), false, false);
  v = __uint_as_float(a[0]) + __uint_as_float(a[1]);
  auto b = __builtin_amdgcn_permlane32_swap(__float_as_uint(v), __float_as_uint(v), false, false);
  return __uint_as_float(b[0]) + __uint_as_float(b[1]);
}
template <int N> DEV float ror_add(float v) { return v + __uint_as_float(__builtin_amdgcn_update_dpp(0u, __float_as_uint(v), 0x120 + N, 0xf, 0xf, true)); }
DEV float wavesum(float v) { return quadsum(ror_add<1>(ror_add<2>(ror_add<4>(ror_add<8>(v))))); }
DEV float dot2(unsigned a, unsigned b, float c) { return __builtin_amdgcn_fdot2_f32_bf16(__builtin_bit_cast(bf16x2, a), __builtin_bit_cast(bf16x2, b), c, false); }
DEV float dot1(unsigned v, float c) { return __builtin_amdgcn_fdot2_f32_bf16(__builtin_bit_cast(bf16x2, v), __builtin_bit_cast(bf16x2, 0x3f803f80u), c, false); }
DEV f32x4 mfma16(v4u a, v4u b, f32x4 c) { return __builtin_amdgcn_mfma_f32_16x16x32_bf16(__builtin_bit_cast(bf16x8, a), __builtin_bit_cast(bf16x8, b), c, 0, 0, 0); }
DEV v2u trrd(LAS char* p) { return __builtin_bit_cast(v2u, __builtin_amdgcn_ds_read_tr16_b64_v4i16((LAS s16x4*)p)); }
template <int AUX> DEV void st16x(rsrc_t rs, unsigned vo, unsigned so, v4u v) {
  __builtin_amdgcn_raw_buffer_store_b128(v, rs, vo, so, AUX);
  asm volatile("s_nop 1" ::"v"(v) : "memory");
}
DEV void st16(rsrc_t rs, unsigned vo, v4u v) { st16x<16>(rs, vo, 0, v); }
DEV void st8(rsrc_t rs, unsigned vo, v2u v) { __builtin_amdgcn_raw_buffer_store_b64(v, rs, vo, 0, 16); }
DEV v4u swap_pair(v2u a, v2u b) {
  auto r0 = __builtin_amdgcn_permlane16_swap(a.x, b.x, false, false);
  auto r1 = __builtin_amdgcn_permlane16_swap(a.y, b.y, false, false);
  return v4u{r0[0], r1[0], r0[1], r1[1]};
}
DEV v2u pk4(f32x4 v) { return v2u{pk2(v.x, v.y), pk2(v.z, v.w)}; }
DEV f32x4 unpk4(v2u v) { return f32x4{bflo(v.x), bfhi(v.x), bflo(v.y), bfhi(v.y)}; }
DEV unsigned gelu2(f32x2 t) {
  const float c0 = -2.f * 0.7978845608028654f * 1.4426950408889634f, c1 = c0 * 0.044715f;
  const f32x2 z = t * __builtin_elementwise_fma(t * t, f32x2{c1, c1}, f32x2{c0, c0});
  const f32x2 d = f32x2{__builtin_amdgcn_exp2f(z.x), __builtin_amdgcn_exp2f(z.y)} + 1.f;
  const f32x2 y = t * f32x2{__builtin_amdgcn_rcpf(d.x), __builtin_amdgcn_rcpf(d.y)};
  return pk2(y.x, y.y);
}
DEV v2u gelu4(f32x4 a, float sc, f32x4 b) {
  const f32x2 s2 = {sc, sc};
  return v2u{gelu2(__builtin_elementwise_fma(f32x2{a.x, a.y}, s2, f32x2{b.x, b.y})), gelu2(__builtin_elementwise_fma(f32x2{a.z, a.w}, s2, f32x2{b.z, b.w}))};
}
template <bool DEVSCOPE> DEV void dma1(rsrc_t rs, unsigned vo, unsigned so, unsigned m0v) {
  if (DEVSCOPE) asm volatile("s_mov_b32 m0, %2\n\ts_nop 3\n\tbuffer_load_dwordx4 %0, %1, %3 offen sc1 lds" ::"v"(vo), "s"(rs), "s"(m0v), "s"(so) : "memory");
  else asm volatile("s_mov_b32 m0, %2\n\ts_nop 3\n\tbuffer_load_dwordx4 %0, %1, %3 offen lds" ::"v"(vo), "s"(rs), "s"(m0v), "s"(so) : "memory");
}
#define WAIT_VM(n) asm volatile("s_waitcnt vmcnt(" #n ")" ::: "memory")
#define BARRIER() asm volatile("s_waitcnt lgkmcnt(0)\n\ts_barrier" ::: "memory")
#define DRAIN_BARRIER() asm volatile("s_waitcnt vmcnt(0) lgkmcnt(0)\n\ts_barrier" ::: "memory")

struct Ctx {
  rsrc_t ws;
  GAS unsigned char* wsp;
  u64 tag;
  int b, w, l, g, G;
};
template <int W0, int NW> DEV void dma_range(const Ctx& c, unsigned lds, unsigned src, int cnt) {
  const int i0 = c.w - W0;
  if (i0 >= 0 && i0 < NW) {
#pragma unroll 1
    for (int i = i0; i < cnt; i += NW) dma1<true>(c.ws, c.l * 16, src + i * 1024, lds + i * 1024);
  }
}
DEV void set_flag(const Ctx& c, int kind) {
  if (threadIdx.x == 0) __hip_atomic_store((GAS u64*)(c.wsp + FLG_OFF + (kind * 256 + c.b) * 256), c.tag, __ATOMIC_RELAXED, __HIP_MEMORY_SCOPE_AGENT);
}
DEV GAS u64* counter(const Ctx& c, int T, int k) { return (GAS u64*)(c.wsp + CNT_OFF + (T * 8 + k) * 256); }
DEV void arrive(GAS u64* cnt) {
  if (threadIdx.x == 0) (void)__hip_atomic_fetch_add(cnt, (u64)1, __ATOMIC_RELAXED, __HIP_MEMORY_SCOPE_AGENT);
}
DEV void poll_counter(GAS u64* cnt, u64 want) {
  for (unsigned spins = 0; spins < SPIN_CAP; ++spins)
    if (__all(__hip_atomic_load(cnt, __ATOMIC_RELAXED, __HIP_MEMORY_SCOPE_AGENT) == want)) break;
  __builtin_amdgcn_fence(__ATOMIC_ACQUIRE, "wavefront");
}
#define STAMP(i) do {} while (0)
#define FINISH() do {} while (0)

#define P1_CALL(NAME)                                                                                                          \
  asm volatile(NAME                                                                                                               \
               : [c00] "=&v"(acc[0][0]), [c01] "=&v"(acc[0][1]), [c02] "=&v"(acc[0][2]), [c03] "=&v"(acc[0][3]),                  \
                 [c10] "=&v"(acc[1][0]), [c11] "=&v"(acc[1][1]), [c12] "=&v"(acc[1][2]), [c13] "=&v"(acc[1][3]),                  \
                 [st] "=&v"(st), [cs] "=&v"(cs)                                                                                   \
               : [xa0] "v"(xa0), [xa1] "v"(xa1), [wa] "v"(wa), [wb] "v"(wb), [xvo] "v"(xvo), [wvo0] "v"(wvo[0]), [wvo1] "v"(wvo[1]), \
                 [stvo] "v"(stvo), [csvo] "v"(csvo), [rx] "s"(rx), [rw] "s"(rw), [rws] "s"(c.ws), [w2k] "s"(w2k), [k2] "s"(k2), \
                 [rvo] "v"(rvo), [rr] "s"(rr), [rst] "s"(rst)                                                                     \
               : P1_CLOB, "scc", "memory")
#define GRAB(ph) do {} while (0)
#define XSTAMP(i) STAMP(i)
#define ASTAMP(k) do {} while (0)
#define PIN(x) asm volatile("" : "+v"(x))

DEV void pair_reduce(LAS char* scr, int w, int l, f32x4 (&c)[6]) {
  const int kh = w >> 2;
  LAS char* mine = scr + l * 16;
  *(LAS f32x4*)(mine + kh * 2048) = c[2];
  *(LAS f32x4*)(mine + kh * 2048 + 1024) = c[3];
  if (kh) *(LAS f32x4*)(mine + 4096) = c[4];
  BARRIER();
  c[0] += *(LAS f32x4*)(mine + (kh ^ 1) * 2048);
  c[1] += *(LAS f32x4*)(mine + (kh ^ 1) * 2048 + 1024);
  if (!kh) c[4] += *(LAS f32x4*)(mine + 4096);
}
template <bool STATS> DEV void store_tile(const Ctx& c, int buf, int stbuf, unsigned oa, unsigned oe, unsigned os, v2u a, v2u b, v2u e) {
  const v4u o = swap_pair(a, b);
  st16x<16>(c.ws, oa, buf, o);
  __builtin_amdgcn_raw_buffer_store_b64(e, c.ws, oe, buf, 16);
  if (STATS) {
    const v4u ones = {0x3f803f80u, 0x3f803f80u, 0x3f803f80u, 0x3f803f80u}, e0 = {e.x, e.y, 0u, 0u};
    const f32x4 z = {0, 0, 0, 0};
    const f32x4 q = mfma16(o, o, mfma16(e0, e0, z)), s = mfma16(ones, o, mfma16(ones, e0, z));
    const bool b0 = c.l & 1, b1 = c.l & 2;
    const float q01 = b0 ? q.y : q.x, q23 = b0 ? q.w : q.z, qd = b1 ? q23 : q01;
    __builtin_amdgcn_raw_buffer_store_b64(v2u{__float_as_uint(s.x), __float_as_uint(qd)}, c.ws, os, stbuf, 16);
  }
}
#define GEMM_RES(NAME, C, VA0, VA1)                                                                                             \
  asm volatile(NAME : [c0] "+v"(C[0]), [c1] "+v"(C[1]), [c2] "=&v"(C[2]), [c3] "=&v"(C[3]), [c4] "+v"(C[4]), [vw] "+v"(vw),       \
               [sa] "+s"(sa), [dm0] "+s"(dm0), [ds0] "+s"(ds0), [dm1] "+s"(dm1), [ds1] "+s"(ds1)                                 \
               : [va0] "v"(VA0), [va1] "v"(VA1), [rs] "s"(c.ws), [l16] "v"(l16), [kh] "s"(kh) : PS_CLOB, "scc", "memory")

DEV void dma2(rsrc_t rs, unsigned vo0, unsigned vo1, unsigned so0, unsigned so1, unsigned m0v) {
  asm volatile("s_mov_b32 m0, %3\n\ts_nop 0\n\tbuffer_load_dwordx4 %0, %2, %4 offen lds\n\tbuffer_load_dwordx4 %1, %2, %5 offen offset:1024 lds"
               ::"v"(vo0), "v"(vo1), "s"(rs), "s"(m0v), "s"(so0), "s"(so1) : "memory");
}
DEV void put_partial(LAS char* a, int l, const f32x16& o) {
  *(LAS v4u*)(a + l * 16) = v4u{pk2(o[0], o[1]), pk2(o[2], o[3]), pk2(o[4], o[5]), pk2(o[6], o[7])};
  const unsigned p0 = pk2(o[8], o[9]), p1 = pk2(o[10], o[11]);
  *(LAS v2u*)(a + 1024 + l * 8) = v2u{l >= 32 ? __float_as_uint(o[8]) : p0, p1};
}
DEV void attn_true_max(rsrc_t rs, LAS char* sm, int w, int l, int hp, int T) {
  const int hh = w & 1, kq = w >> 1, r = l & 31, hi = l >> 5;
  const unsigned kvo = hh * 1024 + l * 16, r8 = hi ? OOB : 2048 + r * 16 + hh * 8, vvo = r <= 20 ? 2560 + hh * 1536 + (hi * 24 + r) * 16 : OOB;
  typedef float f32x8 __attribute__((ext_vector_type(8)));
  typedef __bf16 bf16x16 __attribute__((ext_vector_type(16)));
#pragma unroll 1
  for (int qt = 0; qt < 4; ++qt) {
    const v4u q16 = __builtin_amdgcn_raw_buffer_load_b128(rs, kvo, Q_OFF + (hp * 64 + T * 4 + qt) * QREC, 16);
    const v2u q8 = __builtin_amdgcn_raw_buffer_load_b64(rs, r8, Q_OFF + (hp * 64 + T * 4 + qt) * QREC, 16);
    float m = -3.0e38f;
    f32x16 o = {0, 0, 0, 0, 0, 0, 0, 0, 0, 0, 0, 0, 0, 0, 0, 0};
#pragma unroll 1
    for (int pass = 0; pass < 2; ++pass) {
#pragma unroll 1
      for (int t = 0; t < 16; ++t) {
        const int so = KV_OFF + (hp * 64 + kq * 16 + t) * KVREC;
        const v4u k16 = __builtin_amdgcn_raw_buffer_load_b128(rs, kvo, so, 16);
        const v2u k8 = __builtin_amdgcn_raw_buffer_load_b64(rs, r8, so, 16);
        f32x16 s = {0, 0, 0, 0, 0, 0, 0, 0, 0, 0, 0, 0, 0, 0, 0, 0};
        s = __builtin_amdgcn_mfma_f32_32x32x16_bf16(__builtin_bit_cast(bf16x8, k16), __builtin_bit_cast(bf16x8, q16), s, 0, 0, 0);
        s = __builtin_amdgcn_mfma_f32_32x32x8bf16_1k(__builtin_bit_cast(s16x4, k8), __builtin_bit_cast(s16x4, q8), s, 0, 0, 0);
        if (pass == 0) {
#pragma unroll
          for (int i = 0; i < 16; ++i) m = fmaxf(m, s[i]);
        } else {
          const v4u v0 = __builtin_amdgcn_raw_buffer_load_b128(rs, vvo, so, 16), v1 = __builtin_amdgcn_raw_buffer_load_b128(rs, vvo, so + 768, 16);
          f32x8 p0, p1;
#pragma unroll
          for (int i = 0; i < 8; ++i) { p0[i] = __builtin_amdgcn_exp2f(s[i] - m); p1[i] = __builtin_amdgcn_exp2f(s[8 + i] - m); }
          o = __builtin_amdgcn_smfmac_f32_32x32x32_bf16(__builtin_bit_cast(bf16x8, v0), __builtin_bit_cast(bf16x16, p0), o, (int)0xDDDDDDDD, 0, 0);
          o = __builtin_amdgcn_smfmac_f32_32x32x32_bf16(__builtin_bit_cast(bf16x8, v1), __builtin_bit_cast(bf16x16, p1), o, (int)0xDDDDDDDD, 0, 0);
        }
      }
      if (pass == 0) {
        *(LAS float*)(sm + L_MAX + (w * 64 + l) * 4) = m;
        BARRIER();
#pragma unroll
        for (int k = 0; k < 8; ++k) m = fmaxf(m, *(LAS float*)(sm + L_MAX + ((2 * (k >> 1) + hh) * 64 + (k & 1) * 32 + r) * 4));
        BARRIER();
      }
    }
    put_partial(sm + 102400 + ((hh * 4 + kq) * 4 + qt) * 1536, l, o);
  }
}

__global__ void __launch_bounds__(512, 2) mk_fwd(Params p) {
  extern __shared__ __attribute__((aligned(16))) char smem_[];
  LAS char* sm = (LAS char*)smem_;
  const int b = blockIdx.x, w = __builtin_amdgcn_readfirstlane(threadIdx.x >> 6), l = threadIdx.x & 63;
  const int xc = b & 7, j = b >> 3;
  STAMP(0);
  {
    unsigned len;
    u64 pc;
    asm volatile("s_getpc_b64 %0\nLMK_pc:\n\ts_mov_b32 %1, LMK_end-LMK_pc" : "=s"(pc), "=s"(len));
    const unsigned vo = l < 2 ? ((j * 8 + w) * 2 + l) * 128 : OOB;
    asm volatile("s_mov_b32 m0, %2\n\ts_nop 0\n\tbuffer_load_dword %0, %1, 0 offen lds" ::"v"(vo), "s"(mk_rsrc((const void*)pc, len)), "s"(LDS_BYTES - 256) : "memory");
  }
  Ctx c;
  c.ws = mk_rsrc(p.ws, WS_END);
  c.wsp = (GAS unsigned char*)p.ws;
  c.b = b; c.w = w; c.l = l; c.g = j & 7; c.G = j & 15;
  const unsigned l16 = l * 16;
  const int R = l >> 4, t16 = l & 15;
  const rsrc_t rx = mk_rsrc(p.x, S * D * 2);

  {
    const int T = 8 * (xc & 1) + (j & 7), sg = 4 * (xc >> 1) + (j >> 3);
    const rsrc_t rw = mk_rsrc(p.wqkv, D * 3 * D * 2);
    v4u xr0, xr1;
    {
      const unsigned vo = l < 40 ? (8 * b + w) * 1280 + l * 32 : OOB;
      asm volatile("buffer_load_dwordx4 %0, %2, %3, 0 offen\n\tbuffer_load_dwordx4 %1, %2, %3, 0 offen offset:16" : "=&v"(xr0), "=&v"(xr1) : "v"(vo), "s"(rx) : "memory");
    }
    const unsigned xvo = (128 * T + 16 * w + (l >> 3)) * 1280 + (((l ^ (l >> 3)) & 7) << 4);
    unsigned wvo[2];
#pragma unroll
    for (int i = 0; i < 2; ++i) wvo[i] = (32 * (w >> 2) + 16 * ((w >> 1) & 1) + 8 * i + 4 * (w & 1) + R) * 3840 + sg * 256 + (t16 ^ ((4 * i + R) << 1)) * 16 - 1024 * i;
    const unsigned w2k = w * 2048;
    const int k0 = (j + 3 * xc) % 10, k1 = k0 == 9 ? 0 : k0 + 1, k2 = k1 == 9 ? 0 : k1 + 1;
    if (sg < 15) {
      dma2(rx, xvo, xvo, k0 * 128, k0 * 128 + 9216, w2k);
      dma2(rw, wvo[0], wvo[1], k0 * (64 * 3840), k0 * (64 * 3840), 16384 + w2k);
      dma2(rx, xvo, xvo, k1 * 128, k1 * 128 + 9216, 32768 + w2k);
      dma2(rw, wvo[0], wvo[1], k1 * (64 * 3840), k1 * (64 * 3840), 32768 + 16384 + w2k);
    }
    if (w < 2) {
      const rsrc_t rt = w ? mk_rsrc(p.out, S * D * 2) : c.ws;
      asm volatile("s_mov_b32 m0, %2\n\ts_nop 0\n\tbuffer_load_dword %0, %1, 0 offen lds" ::"v"(l * 524288 + 8192), "s"(rt), "s"(LDS_BYTES - 256) : "memory");
    }
    const int u0 = 8 * b + w;
    const bool is3 = u0 >= 1080;
    const int cu = is3 ? (u0 - 1080) / 20 : u0 / 108, kk = is3 ? u0 - 1080 - 20 * cu : u0 - 108 * cu;
    const int mi = is3 ? 4 : kk < 60 ? kk / 20 : 3, ks = mi < 3 ? kk - 20 * mi : mi == 3 ? kk - 60 : kk;
    const rsrc_t rr = mk_rsrc(mi == 0 ? p.wo : mi == 1 ? p.w1 : mi == 2 ? p.w2 : mi == 3 ? p.w4 : p.w3, mi < 3 ? D * D * 2 : D * F * 2);
    const unsigned rst = (is3 ? 8 * 3072 : 8 * 1280) - 1024;
    unsigned rvo = u0 < 1560 ? (32 * ks + (l >> 3)) * (is3 ? 3072 : 1280) + cu * 128 + (l & 7) * 16 : OOB, dstv, csv;
    {
      const int N = 4 * cu + (l & 3), g5 = (N * 205) >> 10, nt = N - 5 * g5, half = mi < 3 ? 10 : 24, k2h = ks >= half;
      dstv = is3 ? W3_OFF + (((N * 43) >> 8) * 114 + ks * 6 + N) * 1024
                 : WG_OFF + g5 * WG_STR + (mi * 100 + (ks - k2h * half) * 10 + k2h * 5 + ((k2h && nt < 4) ? nt ^ 2 : nt)) * 1024;
      csv = (is3 ? CS3_OFF + ks * F * 4 : CS1_OFF + ks * D * 4) + N * 64;
      PIN(rvo); PIN(dstv); PIN(csv);
    }
    const unsigned E = *(const CAS unsigned*)p.ws;
    const unsigned tlo = E * 0x9E3779B1u + 0x7F4A7C15u, thi = tlo * 0x85EBCA6Bu + 0xC2B2AE35u;
    c.tag = ((u64)thi << 32) | tlo;
    const int wc = w & 3, th = w >> 2;
    const int row0 = 64 * th + t16, r7 = 4 * (R & 1) + (t16 >> 2);
    unsigned xa0 = row0 * 128 + ((R ^ (row0 & 7)) << 4), xa1 = xa0 ^ 64;
    unsigned wa = 16384 + (16 * (R >> 1) + r7) * 256 + (((4 * wc + ((l >> 1) & 1)) ^ (r7 << 1)) << 4) + (l & 1) * 8, wb = wa ^ 32;
    unsigned stvo = XST_OFF + (128 * T + 64 * th + l) * 16, csvo = CSQ_OFF + (128 * sg + 32 * wc + (l & 31)) * 8;
    PIN(xa0); PIN(xa1); PIN(wa); PIN(wb); PIN(stvo); PIN(csvo);
    {
      asm volatile("s_cmp_lt_u32 %2, 15\n\ts_cbranch_scc1 LXW%=\n\ts_waitcnt vmcnt(0)\nLXW%=:\n\ts_waitcnt vmcnt(8)" : "+v"(xr0), "+v"(xr1) : "s"(sg) : "scc", "memory");
      float s = 0.f, q = 0.f;
#pragma unroll
      for (int i = 0; i < 4; ++i) { s = dot2(xr1[i], 0x3f803f80u, dot2(xr0[i], 0x3f803f80u, s)); q = dot2(xr1[i], xr1[i], dot2(xr0[i], xr0[i], q)); }
      const float mean = wavesum(s) * (1.f / D);
      const float rstd = __builtin_amdgcn_rsqf(fmaxf(wavesum(q) * (1.f / D) - mean * mean, 0.f) + EPS);
      if (sg == 0 && w == 0) st8(c.ws, l < 7 ? CNT_OFF + (T * 8 + l) * 256 : OOB, v2u{tlo, thi});
      const v4u gr = {__float_as_uint(mean), tlo, __float_as_uint(rstd), tlo};
      const unsigned vo = l == 0 ? XST_OFF + (8 * b + w) * 16 : OOB;
      asm volatile("buffer_store_dwordx4 %0, %1, %2, 0 offen sc1\n\ts_nop 1" ::"v"(gr), "v"(vo), "s"(c.ws) : "memory");
    }
    STAMP(1);
    if (sg == 15) {
#pragma unroll 1
      for (int i = 0; i < 4; ++i) dma1<false>(rr, rvo, i * (rst + 1024), 131072 + w * 4096 + i * 1024);
      if (T < 15) {
        f32x4 s0 = {0, 0, 0, 0}, s1 = {0, 0, 0, 0};
        const unsigned vo = (4 * w + R) * 3840 + T * 256 + t16 * 16;
#pragma unroll 1
        for (int h2 = 0; h2 < 2; ++h2) {
          v4u v[10];
#pragma unroll
          for (int i = 0; i < 10; ++i) v[i] = __builtin_amdgcn_raw_buffer_load_b128(rw, vo, (h2 * 10 + i) * (32 * 3840), 0);
          __builtin_amdgcn_sched_barrier(0);
#pragma unroll
          for (int i = 0; i < 10; ++i) { s0 += unpk4(v2u{v[i].x, v[i].y}); s1 += unpk4(v2u{v[i].z, v[i].w}); }
        }
        *(LAS f32x4*)(sm + (4 * w + R) * 512 + t16 * 32) = s0;
        *(LAS f32x4*)(sm + (4 * w + R) * 512 + t16 * 32 + 16) = s1;
        BARRIER();
        if (threadIdx.x < 128) {
          float s = 0.f;
#pragma unroll
          for (int i = 0; i < 32; ++i) s += *(LAS float*)(sm + i * 512 + threadIdx.x * 4);
          st8(c.ws, CSQ_OFF + (128 * T + threadIdx.x) * 8, v2u{__float_as_uint(s), tlo});
        }
      }
      DRAIN_BARRIER();
    } else {
      f32x4 acc[2][4];
      v4u st;
      v2u cs;
      if (sg < 10) P1_CALL(P1_ASM_T);
      else P1_CALL(P1_ASM_N);
      STAMP(2);
      for (unsigned spins = 0; spins < SPIN_CAP && !__all(st.y == tlo && st.w == tlo && cs.y == tlo); ++spins) {
        asm volatile("" ::: "memory");
        st = __builtin_amdgcn_raw_buffer_load_b128(c.ws, stvo, 0, 16);
        cs = __builtin_amdgcn_raw_buffer_load_b64(c.ws, csvo, 0, 16);
      }
      LAS char* scr = sm + 98304 + w * 1024;
      const float rs = __uint_as_float(st.z) * (sg < 5 ? 0.22360679774997896f * 1.4426950408889634f : 1.f);
      *(LAS f32x2*)(scr + l * 8) = f32x2{rs, -__uint_as_float(st.x) * rs};
      *(LAS unsigned*)(scr + 512 + l * 4) = cs.x;
      const int t32 = 4 * T + 2 * th;
      if (sg < 10) {
        const int U = 16 * (sg < 5 ? sg : sg - 5) + 4 * wc + 2 * (R & 1) + (R >> 1), hp = (U * 205) >> 10, r5 = U - 5 * hp;
        const unsigned rec = sg < 5 ? QREC : KVREC;
        const unsigned vo = hp * 64 * rec + (r5 == 2 ? 4 : r5 - (r5 > 2)) * 512 + t16 * 16, so = (sg < 5 ? Q_OFF : KV_OFF) + t32 * rec;
        const f32x4 ca = *(LAS f32x4*)(scr + 512 + R * 16), cb = *(LAS f32x4*)(scr + 576 + R * 16);
#pragma unroll
        for (int m = 0; m < 4; ++m) {
          const f32x2 ab = *(LAS f32x2*)(scr + (16 * m + t16) * 8);
          const v4u o = swap_pair(pk4(acc[0][m] * ab.x + ca * ab.y), pk4(acc[1][m] * ab.x + cb * ab.y));
          st16x<16>(c.ws, vo + (m & 1) * 256, so + (m >> 1) * rec, o);
        }
      } else {
        unsigned vo[2];
        float csv[2];
#pragma unroll
        for (int i = 0; i < 2; ++i) {
          const int col = 128 * (sg - 10) + 32 * wc + 16 * i + t16, hd = (col * 3277) >> 16, d = col - 20 * hd;
          vo[i] = (hd >> 1) * (64 * KVREC) + (hd & 1) * 1536 + ((2 * (R & 1) + (R >> 1)) * 24 + d) * 16;
          csv[i] = *(LAS float*)(scr + 512 + (16 * i + t16) * 4);
          const v4u ones = {0x3f803f80u, 0x3f803f80u, 0x3f803f80u, 0x3f803f80u};
          st16x<16>(c.ws, d == 0 ? vo[i] + 320 : OOB, KV_OFF + 2560 + t32 * KVREC, ones);
          st16x<16>(c.ws, d == 0 ? vo[i] + 320 : OOB, KV_OFF + 2560 + (t32 + 1) * KVREC, ones);
        }
#pragma unroll
        for (int u = 0; u < 2; ++u) {
          f32x4 sa[2], sb[2];
#pragma unroll
          for (int mm = 0; mm < 2; ++mm) {
            const f32x4 p01 = *(LAS f32x4*)(scr + (32 * u + 16 * mm + 4 * R) * 8), p23 = *(LAS f32x4*)(scr + (32 * u + 16 * mm + 4 * R) * 8 + 16);
            sa[mm] = f32x4{p01.x, p01.z, p23.x, p23.z}; sb[mm] = f32x4{p01.y, p01.w, p23.y, p23.w};
          }
#pragma unroll
          for (int i = 0; i < 2; ++i)
            st16x<16>(c.ws, vo[i], KV_OFF + 2560 + (t32 + u) * KVREC, swap_pair(pk4(acc[i][2 * u] * sa[0] + sb[0] * csv[i]), pk4(acc[i][2 * u + 1] * sa[1] + sb[1] * csv[i])));
        }
      }
      DRAIN_BARRIER();
      set_flag(c, FK_P1);
    }
    STAMP(3);
    if (u0 < 1560) {
      v4u fr[4];
#pragma unroll
      for (int f = 0; f < 4; ++f) {
        LAS char* a = sm + 131072 + w * 4096 + (8 * R + (t16 >> 2)) * 128 + f * 32 + (l & 3) * 8;
        const v2u f0 = trrd(a), f1 = trrd(a + 512);
        fr[f] = v4u{f0.x, f0.y, f1.x, f1.y};
      }
#pragma unroll
      for (int f = 0; f < 4; ++f) st16x<16>(c.ws, l16, __builtin_amdgcn_readlane(dstv, f), fr[f]);
      if (mi == 1 || is3) {
        v4u ones = {0x3f803f80u, 0x3f803f80u, 0x3f803f80u, 0x3f803f80u};
        PIN(ones);
        const unsigned vo = R == 0 ? t16 * 4 : OOB;
#pragma unroll
        for (int f = 0; f < 4; ++f)
          __builtin_amdgcn_raw_buffer_store_b32(__float_as_uint(mfma16(ones, fr[f], f32x4{0, 0, 0, 0}).x), c.ws, vo, __builtin_amdgcn_readlane(csv, f), 16);
      }
    }
  }
  const unsigned wg_src = WG_OFF + c.g * WG_STR, w3_src = W3_OFF + c.G * (120 * 1024);
  {
    const int hp = 2 * xc + (j & 1), T = j >> 1;
    const int hh = w & 1, kq = w >> 1;
    const int r = l & 31, hi = l >> 5;
    unsigned fvo = FLG_OFF + (FK_R * 256 + (l < 32 ? min((27 * ((5 * c.g) >> 2)) / 2 + l, 134) : min(135 + (5 * ((6 * c.G) >> 2)) / 2 + (l & 7), 194))) * 256;
    v4u ff = {~0u, ~0u, ~0u, ~0u};
    PIN(fvo); PIN(ff);
    unsigned kvo = hh * 1024 + l16, r8 = hi ? OOB : 2048 + r * 16 + hh * 8, vvo = r <= 20 ? 2560 + hh * 1536 + (hi * 24 + r) * 16 : OOB, idx = 0xDDDDDDDD;
    f32x16 o[4];
#pragma unroll
    for (int i = 0; i < 4; ++i) { o[i] = f32x16{0, 0, 0, 0, 0, 0, 0, 0, 0, 0, 0, 0, 0, 0, 0, 0}; PIN(o[i]); }
    PIN(kvo); PIN(r8); PIN(vvo); PIN(idx);
    if (w == 4) {
      const int s2 = ((l >> 4) & 1) ? (5 * hp + 4) >> 4 : (5 * hp) >> 4, sk = s2 + 5 + 5 * hi;
      GAS u64* f0 = (GAS u64*)(c.wsp + FLG_OFF + (FK_P1 * 256 + ((t16 >> 3) | ((sk >> 2) << 1)) + 8 * (((sk & 3) << 3) | (t16 & 7))) * 256);
      const int sq2 = hi ? 0 : s2;
      GAS u64* f1 = (GAS u64*)(c.wsp + FLG_OFF + (FK_P1 * 256 + ((T >> 3) | ((sq2 >> 2) << 1)) + 8 * (((sq2 & 3) << 3) | (T & 7))) * 256);
      for (unsigned spins = 0; spins < SPIN_CAP; ++spins) {
        const u64 a0 = __hip_atomic_load(f0, __ATOMIC_RELAXED, __HIP_MEMORY_SCOPE_AGENT), a1 = __hip_atomic_load(f1, __ATOMIC_RELAXED, __HIP_MEMORY_SCOPE_AGENT);
        if (__all(a0 == c.tag && a1 == c.tag)) break;
      }
      __builtin_amdgcn_fence(__ATOMIC_ACQUIRE, "wavefront");
    }
    DRAIN_BARRIER();
    set_flag(c, FK_R);
    if (threadIdx.x == 0) *(LAS unsigned*)(sm + L_VOTE) = 0u;
    STAMP(4);
    if (MK_STOP == 1) { asm volatile("LMK_end:"); FINISH(); return; }
    __builtin_amdgcn_s_setprio(3);
    {
      int sk = KV_OFF + (hp * 64 + kq * 16) * KVREC, sv = sk, sq = Q_OFF + (hp * 64 + T * 4) * QREC, cnt = 8;
      const int n = hp * 8 + w;
      int sx = n < 120 ? H_OFF + (n / 40) * (S * D * 2 - 163840) + T * 163840 + n * 4096 : HST_OFF + ((n - 120) >> 2) * (128 * 2048) + T * 16384 + (n & 3) * 4096;
      int smm = 12 * w * 1024, sd = wg_src + smm;
      v4u k16a, k16b;
      v2u k8a, k8b;
      u64 fl;
      asm volatile(ATT_ASM
                   : [o0] "+v"(o[0]), [o1] "+v"(o[1]), [o2] "+v"(o[2]), [o3] "+v"(o[3]), [k16a] "=&{v[160:163]}"(k16a), [k16b] "=&{v[164:167]}"(k16b),
                     [k8a] "=&{v[168:169]}"(k8a), [k8b] "=&{v[170:171]}"(k8b), [sk] "+s"(sk), [sv] "+s"(sv), [sq] "+s"(sq), [cnt] "+s"(cnt),
                     [sx] "+s"(sx), [sm] "+s"(smm), [sd] "+s"(sd), [fl] "=&v"(fl)
                   : [idx] "v"(idx), [kvo] "v"(kvo), [r8] "v"(r8), [vvo] "v"(vvo), [rs] "s"(c.ws), [ff] "v"(ff), [l16] "v"(l16), [fvo] "v"(fvo),
                     [tag] "s"(c.tag)
                   : ATT_CLOB, "vcc", "scc", "memory");
    }
    STAMP(5);
    LAS char* cb = sm + 102400;
#pragma unroll
    for (int i = 0; i < 4; ++i) put_partial(cb + ((hh * 4 + kq) * 4 + i) * 1536, l, o[i]);
    __builtin_amdgcn_s_setprio(0);
    XSTAMP(19);
#pragma unroll 1
    for (int pass = 0;; ++pass) {
      BARRIER();
      const int tok = 16 * w + t16, qt = tok >> 5, rr = tok & 31;
      float inv[2];
      bool bad = false;
#pragma unroll
      for (int hd = 0; hd < 2; ++hd) {
        float ls = 0;
#pragma unroll
        for (int k = 0; k < 4; ++k) ls += *(LAS float*)(cb + ((hd * 4 + k) * 4 + qt) * 1536 + 1024 + (32 + rr) * 8);
        inv[hd] = __builtin_amdgcn_rcpf(ls);
        bad |= !(ls >= 0x1p-60f && ls <= 0x1p60f);
      }
      if (bad) *(LAS unsigned*)(sm + L_VOTE) = 1u;
#pragma unroll
      for (int rnd = 0; rnd < 2; ++rnd) {
        const int ck = rnd ? 4 : R;
        v2u pk[2];
#pragma unroll
        for (int e = 0; e < 2; ++e) {
          const int gi = 2 * ck + e, hd = gi >= 5, pc = gi - 5 * hd;
          const int off = (hd * 16 + qt) * 1536 + (pc < 4 ? ((pc & 1) * 32 + rr) * 16 + (pc >> 1) * 8 : 1024 + rr * 8);
          f32x4 a = {0, 0, 0, 0};
#pragma unroll
          for (int k = 0; k < 4; ++k) a += unpk4(*(LAS v2u*)(cb + off + k * 6144));
          pk[e] = pk4(a * (hd ? inv[1] : inv[0]));
        }
        const int gc = hp * 5 + ck;
        st16(c.ws, (rnd == 0 || R == 0) ? O_OFF + ((8 * T + w) * 20 + (gc >> 2)) * 1024 + ((gc & 3) * 16 + t16) * 16 : OOB, v4u{pk[0].x, pk[0].y, pk[1].x, pk[1].y});
      }
      DRAIN_BARRIER();
      if (pass || __builtin_amdgcn_readfirstlane(*(LAS unsigned*)(sm + L_VOTE)) == 0) break;
      attn_true_max(c.ws, sm, w, l, hp, T);
    }
    ASTAMP(2);
    arrive(counter(c, T, CK_P2));
    STAMP(6);
  }
  if (MK_STOP == 2) { asm volatile("LMK_end:"); FINISH(); return; }
  const int rp = 4 * xc + (j >> 3), T3 = rp >> 1, kh = w >> 2, mt = 4 * rp + (w & 3);
  const unsigned w10 = 10 * w * 1024, w5 = 5 * w * 1024;
  dma_range<2, 6>(c, 96 * 1024, wg_src + 96 * 1024, 54);
  unsigned oa, oe, os, va0 = (mt * 20 + kh * 10) * 1024 + l16;
  f32x4 a[6];
  {
    const unsigned xo = (16 * mt + t16) * 1280 + c.g * 160 + R * 8;
    const v2u x0 = __builtin_amdgcn_raw_buffer_load_b64(rx, xo, kh * 64, 0), x1 = __builtin_amdgcn_raw_buffer_load_b64(rx, xo + 32, kh * 64, 0);
    const v2u x4 = __builtin_amdgcn_raw_buffer_load_b64(rx, kh ? OOB : xo + 128, 0, 0);
    const int cc = 10 * c.g + 4 * kh + 2 * (R & 1) + (R >> 1), c4 = 10 * c.g + 8 + (R >> 1);
    oa = (mt * 20 + (cc >> 2)) * 1024 + ((cc & 3) * 16 + t16) * 16;
    oe = kh ? OOB : (mt * 20 + (c4 >> 2)) * 1024 + ((c4 & 3) * 16 + t16) * 16 + (R & 1) * 8;
    os = R == (t16 >> 2) ? mt * 2048 + c.g * 256 + t16 * 16 + kh * 8 : OOB;
    a[0] = unpk4(x0); a[1] = unpk4(x1); a[4] = unpk4(x4);
    PIN(oa); PIN(oe); PIN(os); PIN(va0); PIN(a[0]); PIN(a[1]); PIN(a[4]);
  }
  if (w == 1) {
        const int c4 = l % 24, pt = l / 24;
        const unsigned o1 = (l < 48 && c4 < 20) ? (pt * 10 * D + c.g * 80 + c4 * 4) * 4 : OOB, o3 = l < 48 ? (pt * 10 * F + c.G * 96 + c4 * 4) * 4 : OOB;
        f32x4 s1 = {0, 0, 0, 0}, s3 = {0, 0, 0, 0};
#pragma unroll
        for (int k = 0; k < 10; ++k) {
          s1 += __builtin_bit_cast(f32x4, __builtin_amdgcn_raw_buffer_load_b128(c.ws, o1, CS1_OFF + k * D * 4, 16));
          s3 += __builtin_bit_cast(f32x4, __builtin_amdgcn_raw_buffer_load_b128(c.ws, o3, CS3_OFF + k * F * 4, 16));
        }
#pragma unroll
        for (int i = 0; i < 4; ++i) { s1[i] += __shfl(s1[i], l + 24); s3[i] += __shfl(s3[i], l + 24); }
        if (l < 24) { *(LAS f32x4*)(sm + L_CS1 + l16) = s1; *(LAS f32x4*)(sm + L_CS3 + l16) = s3; }
  }
  if (w == 0) poll_counter(counter(c, T3, CK_P2), c.tag + 16);
  BARRIER();
  STAMP(7);
  const unsigned vw0 = l16 + kh * 5120;
  v2u hq[3];
  unsigned vst, vc, vc4, ga, ge;
  {
    unsigned sa = O_OFF, vw = vw0, dm0 = w10, ds0 = wg_src + 150 * 1024 + w10, dm1 = 0, ds1 = 0;
    GEMM_RES(P3_ASM, a, va0, va0);
    GRAB(3);
    STAMP(8);
    pair_reduce(sm + (40 + 5 * (w & 3)) * 1024, w, l, a);
    hq[0] = pk4(a[0]); hq[1] = pk4(a[1]); hq[2] = kh ? v2u{0, 0} : pk4(a[4]);
    store_tile<true>(c, H_OFF, HST_OFF, oa, oe, os, hq[0], hq[1], hq[2]);
    BARRIER();
    ASTAMP(3);
    arrive(counter(c, T3, CK_P3 + (rp & 1)));
    dma_range<1, 7>(c, 40 * 1024, wg_src + 190 * 1024, 60);
    vst = HST_OFF + mt * 2048 + R * 512 + t16 * 16; vc = L_CS1 + kh * 128 + R * 16; vc4 = L_CS1 + 256 + R * 16;
    {
      const int tk = 16 * mt + t16 + ((c.g & 2) ? -1 : 1) * ((c.g >> 2) + 1), dl = (((tk >> 4) - mt) * 20 * 64 + (tk & 15) - t16) * 16;
      const bool in = tk >= 0 && tk < S;
      ga = in ? oa + dl : OOB; ge = (in && !kh) ? oe + dl : OOB;
    }
    PIN(vst); PIN(vc); PIN(vc4); PIN(ga); PIN(ge);
    if (w == 0) {
      const int Tn = min(max(T3 + (l & 3) - 2, 0), 15);
      poll_counter((l & 3) ? counter(c, Tn, CK_P2) : counter(c, T3, CK_P3 + (rp & 1)), c.tag + ((l & 3) ? 16 : 8));
    }
    WAIT_VM(8);
    BARRIER();
    STAMP(9);
  }
  if (MK_STOP == 3) { asm volatile("LMK_end:"); FINISH(); return; }
  unsigned va5[2];
  {
    unsigned sa = H_OFF, vw = vw0, dm0 = 100 * 1024 + w10, ds0 = wg_src + 250 * 1024 + w10;
    float mean, r1, sc;
    f32x4 b0, b1, b4;
    asm volatile(P4_ASM
                 : [c0] "=&v"(a[0]), [c1] "=&v"(a[1]), [c2] "=&v"(a[2]), [c3] "=&v"(a[3]), [c4] "=&v"(a[4]), [vw] "+v"(vw), [sa] "+s"(sa),
                   [dm0] "+s"(dm0), [ds0] "+s"(ds0), "=&{v152}"(mean), "=&{v153}"(r1), "=&{v154}"(sc),
                   "=&{v[156:159]}"(b0), "=&{v[160:163]}"(b1), "=&{v[164:167]}"(b4)
                 : [va0] "v"(va0), [rs] "s"(c.ws), [l16] "v"(l16), [vst] "v"(vst), [vc] "v"(vc), [vc4] "v"(vc4), [kh] "s"(kh)
                 : PS_CLOB, LN_CLOB, "scc", "memory");
    GRAB(4);
    STAMP(10);
    pair_reduce(sm + ((w & 2) ? 5 * (w & 1) : 140 + 5 * (w & 1)) * 1024, w, l, a);
    XSTAMP(20);
    const v2u g0 = gelu4(a[0], sc, b0), g1 = gelu4(a[1], sc, b1);
    v2u g4 = {0, 0};
    if (!kh) g4 = gelu4(a[4], sc, b4);
    store_tile<false>(c, G_OFF, 0, ga, ge, os, g0, g1, g4);
    XSTAMP(21);
    BARRIER();
    XSTAMP(22);
    ASTAMP(4);
    arrive(counter(c, T3, CK_P4 + (rp & 1)));
    dma_range<1, 7>(c, 140 * 1024, wg_src + 290 * 1024, 10);
    dma_range<1, 7>(c, 0, w3_src, 50);
    a[0] = (unpk4(hq[0]) - mean) * r1; a[1] = (unpk4(hq[1]) - mean) * r1;
    a[4] = kh ? f32x4{0, 0, 0, 0} : (unpk4(hq[2]) - mean) * r1;
#pragma unroll
    for (int i = 0; i < 2; ++i) {
      const int tk = 16 * mt + t16 + (i ? kh + 1 : -(kh + 1));
      va5[i] = (tk >= 0 && tk < S) ? va0 : OOB;
    }
    PIN(a[0]); PIN(a[1]); PIN(a[4]); PIN(va5[0]); PIN(va5[1]);
    XSTAMP(23);
    if (w == 0) {
      const int rq = min(max(rp + min(l, 2) - 1, 0), 31);
      poll_counter(counter(c, rq >> 1, CK_P4 + (rq & 1)), c.tag + 8);
    }
    WAIT_VM(8);
    BARRIER();
    STAMP(11);
  }
  if (MK_STOP == 4) { asm volatile("LMK_end:"); FINISH(); return; }
  const int m6 = 8 * T3 + w;
  unsigned va6, ou6;
  f32x4 keep[3];
  {
    unsigned sa = G_OFF, vw = vw0, dm0 = 50 * 1024 + w10, ds0 = w3_src + 50 * 1024 + w10, dm1 = 0, ds1 = 0;
    GEMM_RES(P5_ASM, a, va5[0], va5[1]);
    GRAB(5);
    STAMP(12);
    pair_reduce(sm + (90 + 5 * (w & 3)) * 1024, w, l, a);
    const v2u q0 = pk4(a[0]), q1 = pk4(a[1]), q4 = kh ? v2u{0, 0} : pk4(a[4]);
    store_tile<true>(c, X1_OFF, X1ST_OFF, oa, oe, os, q0, q1, q4);
    BARRIER();
    ASTAMP(5);
    arrive(counter(c, T3, CK_P5));
    dma_range<1, 7>(c, 90 * 1024, w3_src + 90 * 1024, 30);
    dma_range<1, 7>(c, 120 * 1024, wg_src + 300 * 1024, 30);
    keep[0] = unpk4(q0) + unpk4(hq[0]); keep[1] = unpk4(q1) + unpk4(hq[1]); keep[2] = unpk4(q4) + unpk4(hq[2]);
    va6 = m6 * 20 * 1024 + l16; vst = X1ST_OFF + m6 * 2048 + R * 512 + t16 * 16; vc = L_CS3 + R * 16;
    ou6 = (m6 * 48 + 3 * c.G) * 1024 + ((2 * (R & 1) + (R >> 1)) * 16 + t16) * 16;
    PIN(keep[0]); PIN(keep[1]); PIN(keep[2]); PIN(va6); PIN(vst); PIN(vc); PIN(ou6);
    if (w == 0) poll_counter(counter(c, T3, CK_P5), c.tag + 16);
    WAIT_VM(8);
    BARRIER();
    STAMP(13);
  }
  if (MK_STOP == 5) { asm volatile("LMK_end:"); FINISH(); return; }
  unsigned va7, oo7;
  {
    unsigned sa = X1_OFF, vw = l16, dm0 = w5, ds0 = wg_src + 330 * 1024 + w5;
    float mean, r1, sc;
    f32x4 bb[6];
    if (w < 4) __builtin_amdgcn_s_setprio(1);
    asm volatile(P6_ASM
                 : [c0] "=&v"(a[0]), [c1] "=&v"(a[1]), [c2] "=&v"(a[2]), [c3] "=&v"(a[3]), [c4] "=&v"(a[4]), [c5] "=&v"(a[5]), [vw] "+v"(vw), [sa] "+s"(sa),
                   [dm0] "+s"(dm0), [ds0] "+s"(ds0), "=&{v152}"(mean), "=&{v153}"(r1),
                   "=&{v154}"(sc), "=&{v[156:159]}"(bb[0]), "=&{v[160:163]}"(bb[1]), "=&{v[164:167]}"(bb[2]), "=&{v[168:171]}"(bb[3]),
                   "=&{v[172:175]}"(bb[4]), "=&{v[176:179]}"(bb[5])
                 : [va0] "v"(va6), [rs] "s"(c.ws), [l16] "v"(l16), [vst] "v"(vst), [vc] "v"(vc)
                 : PS_CLOB, LN_CLOB, "scc", "memory");
    __builtin_amdgcn_s_setprio(0);
    GRAB(6);
    STAMP(14);
#pragma unroll
    for (int i = 0; i < 3; ++i)
      st16x<16>(c.ws, ou6 + i * 1024, U_OFF, swap_pair(gelu4(a[2 * i], sc, bb[2 * i]), gelu4(a[2 * i + 1], sc, bb[2 * i + 1])));
    dma_range<0, 4>(c, 40 * 1024, wg_src + 370 * 1024, 40);
    DRAIN_BARRIER();
    ASTAMP(6);
    arrive(counter(c, T3, CK_P6));
    dma_range<1, 7>(c, 80 * 1024, wg_src + 410 * 1024, 40);
    va7 = (mt * 48 + kh * 24) * 1024 + l16; oo7 = (16 * mt + t16) * 1280 + c.g * 160;
    a[0] = keep[0]; a[1] = keep[1]; a[4] = keep[2];
    PIN(va7); PIN(oo7); PIN(a[0]); PIN(a[1]); PIN(a[4]);
    if (w == 0) poll_counter(counter(c, T3, CK_P6), c.tag + 16);
    BARRIER();
    STAMP(15);
  }
  if (MK_STOP == 6) { asm volatile("LMK_end:"); FINISH(); return; }
  {
    unsigned sa = U_OFF, vw = vw0;
    unsigned dm0 = (w < 6 ? 120 * 1024 : -30 * 1024) + w5, ds0 = wg_src + 450 * 1024 + w5, dm1 = (10 + 7 * w) * 1024, ds1 = wg_src + (490 + 7 * w) * 1024;
    GEMM_RES(P7_ASM, a, va7, va7);
    GRAB(7);
    STAMP(16);
    pair_reduce(sm + (70 + 5 * (w & 3)) * 1024, w, l, a);
    const rsrc_t ro = mk_rsrc(p.out, S * D * 2);
    st16x<MK_OUT_AUX>(ro, oo7 + (R & 1) * 32 + (R >> 1) * 16, kh * 64, swap_pair(pk4(a[0]), pk4(a[1])));
    if (!kh) __builtin_amdgcn_raw_buffer_store_b64(pk4(a[4]), ro, oo7 + 128 + R * 8, 0, MK_OUT_AUX);
    if (b == 0 && threadIdx.x == 0) __hip_atomic_store((GAS unsigned*)c.wsp, (unsigned)c.tag, __ATOMIC_RELAXED, __HIP_MEMORY_SCOPE_AGENT);
    STAMP(17);
  }
  asm volatile("LMK_end:");
  FINISH();
}

extern "C" void kernel_launch(void* const* d_in, const int* in_sizes, int n_in, void* d_out, int out_size, void* d_ws, size_t ws_size, hipStream_t stream) {
  Params p;
  p.x = (const u16*)d_in[0];
  p.wqkv = (const u16*)d_in[1];
  p.wo = (const u16*)d_in[2];
  p.w1 = (const u16*)d_in[4];
  p.w2 = (const u16*)d_in[5];
  p.w3 = (const u16*)d_in[6];
  p.w4 = (const u16*)d_in[7];
  p.out = (u16*)d_out;
  p.ws = (unsigned char*)d_ws;
  (void)hipFuncSetAttribute((const void*)mk_fwd, hipFuncAttributeMaxDynamicSharedMemorySize, LDS_BYTES);
  mk_fwd<<<256, 512, LDS_BYTES, stream>>>(p);
}
```
